```python
import torch

def _softmax(x, axis=-1):
    return torch.softmax(x, dim=axis)
def _gelu(x):
    return torch.nn.functional.gelu(x, approximate="tanh")
def _silu(x):
    return torch.nn.functional.silu(x)
def _sigmoid(x):
    return torch.sigmoid(x)
def _layernorm(x, axis=-1, eps=1e-5):
    m = x.mean(dim=axis, keepdim=True)
    v = x.var(dim=axis, keepdim=True, unbiased=False)
    return (x - m) / torch.sqrt(v + eps)
def _scatter_add(tgt, idx, src, axis=0):
    return tgt.clone().index_add_(axis, idx, src)
def _segment_sum(src, seg, n):
    out = torch.zeros((n,) + src.shape[1:], dtype=src.dtype)
    return out.index_add_(0, seg, src)
def _pad(x, pw):
    flat = tuple(v for pair in reversed(pw) for v in pair)
    return torch.nn.functional.pad(x, flat)

kernel_name = 'interleave_layers__ssm_conv_s4d__dense_activation_relu__prompt'

def setup_inputs():

    _w = (lambda a: (lambda t: t.to(torch.bfloat16) if t.is_floating_point() else t)(torch.as_tensor(a)))
    def _A_setup():
        Bsz, L, D, N = 1, 2048, 512, 128
        return {
            "u": _w((lambda *s: torch.randn(*s, dtype=torch.bfloat16) * 0.05)(Bsz, L, D)),
            "log_A_real": _w((lambda *s: torch.randn(*s, dtype=torch.bfloat16) * 3.2)(D, N) * 0.5),
            "A_imag": _w((lambda *s: torch.randn(*s, dtype=torch.bfloat16) * 0.2)(D, N) * 3.14),
            "C_real": _w((lambda *s: torch.randn(*s, dtype=torch.bfloat16) * 0.2)(D, N) / N ** 0.5),
            "C_imag": _w((lambda *s: torch.randn(*s, dtype=torch.bfloat16) * 0.2)(D, N) / N ** 0.5),
            "log_dt": _w((lambda *s: torch.randn(*s, dtype=torch.bfloat16) * 0.2)(D) * 0.5 - 4.0),
            "D_skip": _w((lambda *s: torch.randn(*s, dtype=torch.bfloat16) * 0.2)(D)),
        }
    _A_in = _A_setup()

    def _B_setup():
        B, S, D, Dff = 1, 2048, 512, 1792
        return {
            "x": _w((lambda *s: torch.randn(*s, dtype=torch.bfloat16) * 0.05)(B, S, D)),
            "w1": _w((lambda *s: torch.randn(*s, dtype=torch.bfloat16) * 0.05)(D, Dff)),
            "w2": _w((lambda *s: torch.randn(*s, dtype=torch.bfloat16) * 0.05)(Dff, D)),
        }
    _B_in = _B_setup()

    return {
        "A_u": _A_in["u"],
        "A_log_A_real": _A_in["log_A_real"],
        "A_A_imag": _A_in["A_imag"],
        "A_C_real": _A_in["C_real"],
        "A_C_imag": _A_in["C_imag"],
        "A_log_dt": _A_in["log_dt"],
        "A_D_skip": _A_in["D_skip"],
        "B_x": _B_in["x"],
        "B_w1": _B_in["w1"],
        "B_w2": _B_in["w2"],
    }

def reference(A_u, A_log_A_real, A_A_imag, A_C_real, A_C_imag, A_log_dt, A_D_skip, B_x, B_w1, B_w2):
    def _A_ref(u, log_A_real, A_imag, C_real, C_imag, log_dt, D_skip):
        L = u.shape[1]
        dt = torch.exp(log_dt)
        A = -torch.exp(log_A_real) + 1j * A_imag
        C = (C_real + 1j * C_imag) * dt[:, None]
        pos = (lambda x, dt: x.to(dt))(torch.arange(L), log_dt.dtype)
        P = torch.exp((A * dt[:, None])[:, :, None] * pos[None, None, :])
        K = 2.0 * torch.einsum("dn,dnl->dl", C, P).real
        L2 = 2 * L
        u_t = (lambda x, *d: x.permute(*d))(u, 0, 2, 1)
        Kf = (lambda x, *a, **k: torch.fft.rfft(x.float(), *a, **k))(K, L2)
        Uf = (lambda x, *a, **k: torch.fft.rfft(x.float(), *a, **k))(u_t, L2)
        y = (lambda x, *a, **k: torch.fft.irfft(x, *a, **k).to(torch.bfloat16))(Uf * Kf[None, :, :], L2)[:, :, :L]
        y = (lambda x, *d: x.permute(*d))(y, 0, 2, 1)
        return y + u * D_skip[None, None, :]

    def _B_ref(x, w1, w2):
        h = torch.einsum("bsd,df->bsf", x, w1)
        zero = torch.zeros_like(h)
        h = torch.maximum(h, zero)
        return torch.einsum("bsf,fd->bsd", h, w2)

    x = A_u
    x = x + _A_ref(_layernorm(x, axis=-1), A_log_A_real, A_A_imag, A_C_real, A_C_imag, A_log_dt, A_D_skip)
    x = x + _B_ref(_layernorm(x, axis=-1), B_w1, B_w2)
    x = x + _A_ref(_layernorm(x, axis=-1), A_log_A_real, A_A_imag, A_C_real, A_C_imag, A_log_dt, A_D_skip)
    x = x + _B_ref(_layernorm(x, axis=-1), B_w1, B_w2)
    return x
```

```cpp
#include <hip/hip_runtime.h>
#include <hip/hip_bf16.h>
#include <stdint.h>

typedef __attribute__((ext_vector_type(8))) __bf16 bf16x8;
typedef __attribute__((ext_vector_type(2))) __bf16 bf16x2;
typedef __attribute__((ext_vector_type(4))) short s16x4;
typedef __attribute__((ext_vector_type(8))) short s16x8;
typedef __attribute__((ext_vector_type(8))) _Float16 f16x8;
typedef __attribute__((ext_vector_type(2))) _Float16 f16x2;
typedef __attribute__((ext_vector_type(2))) float f32x2;
typedef __attribute__((ext_vector_type(4))) float f32x4;
typedef __attribute__((ext_vector_type(16))) float f32x16;
typedef __attribute__((ext_vector_type(4))) unsigned u32x4;
typedef __attribute__((ext_vector_type(2))) unsigned u32x2;
#define LAS __attribute__((address_space(3)))
#define DI __device__ __forceinline__

constexpr int L_ = 2048, D_ = 512, N_ = 128, F_ = 1792;

constexpr size_t MiB = 1024 * 1024;
constexpr size_t WS_G    = 0;
constexpr size_t WS_XT0  = 2 * MiB;
constexpr size_t WS_XT1  = 4 * MiB;
constexpr size_t WS_XT2  = 6 * MiB;
constexpr size_t WS_XT3  = 8 * MiB;
constexpr size_t WS_W1T  = 10 * MiB;
constexpr size_t WS_W2T  = 12 * MiB;
constexpr size_t WS_H    = 14 * MiB;
constexpr size_t WS_KINV = 22 * MiB;
constexpr size_t WS_ST0  = WS_KINV + 4096;
constexpr size_t WS_ST1  = WS_ST0 + 16384;
constexpr size_t WS_W1S  = WS_ST1 + 16384;

DI float bf_lo(unsigned u) { return __uint_as_float(u << 16); }
DI float bf_hi(unsigned u) { return __uint_as_float(u & 0xffff0000u); }
DI unsigned pk_bf16(float a, float b) {
  f32x2 v = {a, b};
  return __builtin_bit_cast(unsigned, __builtin_convertvector(v, bf16x2));
}
DI unsigned pk_f16(float a, float b) {
  f32x2 v = {a, b};
  return __builtin_bit_cast(unsigned, __builtin_convertvector(v, f16x2));
}
DI float rbf(float a) { return bf_lo(pk_bf16(a, 0.f)); }
DI float ld_bf(const unsigned short* p) { return __uint_as_float(((unsigned)*p) << 16); }
DI unsigned perm_lo(unsigned hi, unsigned lo) { return __builtin_amdgcn_perm(hi, lo, 0x05040100u); }
DI unsigned perm_hi(unsigned hi, unsigned lo) { return __builtin_amdgcn_perm(hi, lo, 0x07060302u); }
DI float wave_sum(float v) {
#pragma unroll
  for (int o = 32; o >= 1; o >>= 1) v += __shfl_xor(v, o, 64);
  return v;
}
template <int N> DI void wait_vm() { asm volatile("s_waitcnt vmcnt(%0)" ::"n"(N) : "memory"); }
template <int OFF> DI void gload16(u32x4& dst, const char* p) {
  asm volatile("global_load_dwordx4 %0, %1, off offset:%2" : "=v"(dst) : "v"(p), "n"(OFF));
}
template <int N> DI void wait_vm4(u32x4& a, u32x4& b, u32x4& c, u32x4& d) {
  asm volatile("s_waitcnt vmcnt(%4)" : "+v"(a), "+v"(b), "+v"(c), "+v"(d) : "n"(N) : "memory");
}
template <int OFF> DI void tr_read(u32x2& d, unsigned addr) {
  asm volatile("ds_read_b64_tr_b16 %0, %1 offset:%2" : "=v"(d) : "v"(addr), "n"(OFF));
}
template <int N> DI void wait_lgkm8(u32x2* b) {
  asm volatile("s_waitcnt lgkmcnt(%8)"
               : "+v"(b[0]), "+v"(b[1]), "+v"(b[2]), "+v"(b[3]), "+v"(b[4]), "+v"(b[5]), "+v"(b[6]), "+v"(b[7])
               : "n"(N) : "memory");
}
template <int N> DI void wait_vm_lgkm0() { asm volatile("s_waitcnt vmcnt(%0) lgkmcnt(0)" ::"n"(N) : "memory"); }
template <int I, int E, class F> DI void static_for(F&& f) {
  if constexpr (I < E) { f(std::integral_constant<int, I>{}); static_for<I + 1, E>(f); }
}

constexpr int PB_KGEN = 512, PB_XT = 64, PB_W1 = 56, PB_W2 = 56, PB_WS = 28;
constexpr int PREP_BLOCKS = PB_KGEN + PB_XT + PB_W1 + PB_W2 + PB_WS;
constexpr int TROW = 528;

__host__ __device__ constexpr int tab_w(int idx) {
  return idx < 16 ? idx : (idx < 40 ? ((8 + ((idx - 16) & 7)) << (1 + ((idx - 16) >> 3))) : 128);
}
__host__ __device__ constexpr int ilog2c(int w) { return w <= 1 ? 0 : 1 + ilog2c(w >> 1); }
__host__ __device__ constexpr int tab_idx(int w) {
  return w < 16 ? w : (w == 128 ? 40 : 16 + 8 * (ilog2c(w) - 4) + ((w >> (ilog2c(w) - 3)) - 8));
}
DI int tab_idx_rt(int w) {
  if (w < 16) return w;
  int lg = 31 - __clz(w);
  return 16 + 8 * (lg - 4) + ((w >> (lg - 3)) - 8);
}

__global__ __launch_bounds__(256) void prep_kernel(
    const unsigned short* __restrict__ x0, const unsigned short* __restrict__ logA,
    const unsigned short* __restrict__ Aim, const unsigned short* __restrict__ Cre,
    const unsigned short* __restrict__ Cim, const unsigned short* __restrict__ logdt,
    const unsigned short* __restrict__ w1, const unsigned short* __restrict__ w2,
    char* __restrict__ ws) {
  __shared__ __attribute__((aligned(16))) char smem[82 * TROW];
  const int tid = threadIdx.x, wave = tid >> 6, lane = tid & 63;
  int b = blockIdx.x;
  if (b < PB_KGEN) {
    const int d = b;
    const int isH = wave >> 1;
    const int n = (wave & 1) * 64 + lane;
    const float dt = rbf(expf(ld_bf(logdt + d)));
    const float ar = -rbf(expf(ld_bf(logA + d * N_ + n))) * dt;
    const float ai = ld_bf(Aim + d * N_ + n) * dt;
    const float c0r = ld_bf(Cre + d * N_ + lane) * dt, c0i = ld_bf(Cim + d * N_ + lane) * dt;
    const float c1r = ld_bf(Cre + d * N_ + 64 + lane) * dt, c1i = ld_bf(Cim + d * N_ + 64 + lane) * dt;
    float S = wave_sum(2.f * (fabsf(c0r) + fabsf(c0i) + fabsf(c1r) + fabsf(c1i)));
    int ex = 0;
    (void)frexpf(S, &ex);
    if (!(S > 0.f) || !(S < 3.0e38f)) ex = 15;
    ex = max(-100, min(ex, 120));
    const float scale = ldexpf(1.f, 15 - ex);
    const float cr = (wave & 1) ? c1r : c0r, ci = (wave & 1) ? c1i : c0i;
    const float base = isH ? 16.f : 1.f;
    const float er = ar * 1.44269504088896340736f * base;
    const float IH = 0.15915494309189533577f;
    const float IL = (float)(0.15915494309189533577 - (double)0.15915494309189533577f);
    float fh = ai * IH;
    float fl = fmaf(ai, IH, -fh) + ai * IL;
    fh *= base; fl *= base;
    float Er[8], Ei[8];
#pragma unroll
    for (int k = 0; k < 8; ++k) {
      const float p = (float)(1 << k);
      const float mag = __builtin_amdgcn_exp2f(er * p);
      const float ph = __builtin_amdgcn_fractf(fh * p) + fl * p;
      Er[k] = mag * __builtin_amdgcn_cosf(ph);
      Ei[k] = mag * __builtin_amdgcn_sinf(ph);
    }
    float vr[41], vi[41];
    vr[0] = isH ? 2.f * scale * cr : 1.f;
    vi[0] = isH ? 2.f * scale * ci : 0.f;
    const float sgn = isH ? -1.f : 1.f;
    char* trow = smem + isH * (41 * TROW) + n * 4;
    *(unsigned*)trow = pk_f16(vr[0], sgn * vi[0]);
    static_for<1, 41>([&](auto I) {
      constexpr int idx = I;
      constexpr int w = tab_w(idx);
      constexpr int lg = ilog2c(w);
      constexpr int ridx = tab_idx(w - (1 << lg));
      vr[idx] = Er[lg] * vr[ridx] - Ei[lg] * vi[ridx];
      vi[idx] = Er[lg] * vi[ridx] + Ei[lg] * vr[ridx];
      *(unsigned*)(trow + idx * TROW) = pk_f16(vr[idx], sgn * vi[idx]);
    });
    __syncthreads();
    const int s = wave, i16 = lane & 15, g = lane >> 4;
    int hw = (s == 0) ? i16 : (i16 < 8 ? ((8 + i16) << s) : 128);
    const char* arow = smem + 41 * TROW + tab_idx_rt(hw) * TROW + g * 16;
    const char* brow = smem + tab_idx_rt(i16 << s) * TROW + g * 16;
    f32x4 acc = {0.f, 0.f, 0.f, 0.f};
#pragma unroll
    for (int ks = 0; ks < 8; ++ks) {
      f16x8 a = *(const f16x8*)(arow + ks * 64);
      f16x8 bb = *(const f16x8*)(brow + ks * 64);
      acc = __builtin_amdgcn_mfma_f32_16x16x32_f16(a, bb, acc, 0, 0, 0);
    }
    __syncthreads();
    _Float16* kc = (_Float16*)smem;
#pragma unroll
    for (int r = 0; r < 4; ++r) {
      const int i = 4 * g + r;
      const int cidx = (s == 0) ? (16 * i + i16) : (256 + 128 * (s - 1) + 16 * i + i16);
      const bool ok = (s == 0) || (i < 8) || (s == 3 && i == 8 && i16 == 0);
      if (ok) kc[cidx] = (_Float16)acc[r];
    }
    __syncthreads();
    unsigned short o[8];
#pragma unroll
    for (int e = 0; e < 8; ++e) {
      const int l = 2047 - (tid * 8 + e);
      int cidx = l;
      if (l >= 256) {
        const int sh = (31 - __clz(l)) - 7;
        const int q = (l + (1 << (sh - 1)) - 1 + ((l >> sh) & 1)) >> sh;
        cidx = 256 + 128 * (sh - 1) + (q - 128);
      }
      o[e] = ((const unsigned short*)kc)[cidx];
    }
    u32x4 ov = {(unsigned)o[0] | ((unsigned)o[1] << 16), (unsigned)o[2] | ((unsigned)o[3] << 16),
                (unsigned)o[4] | ((unsigned)o[5] << 16), (unsigned)o[6] | ((unsigned)o[7] << 16)};
    *(u32x4*)(ws + WS_G + (size_t)d * 4096 + tid * 16) = ov;
    if (tid == 0) ((float*)(ws + WS_KINV))[d] = ldexpf(1.f, ex - 15);
    return;
  }
  b -= PB_KGEN;
  if (b < PB_XT) {
    const int t0 = b * 32 + wave * 8;
    u32x4 r[8];
#pragma unroll
    for (int j = 0; j < 8; ++j) r[j] = *(const u32x4*)(x0 + (size_t)(t0 + j) * D_ + lane * 8);
    float mean_l = 0.f, var_l = 0.f;
#pragma unroll
    for (int j = 0; j < 8; ++j) {
      float s = 0.f;
#pragma unroll
      for (int c = 0; c < 4; ++c) s += bf_lo(r[j][c]) + bf_hi(r[j][c]);
      const float mean = wave_sum(s) * (1.f / D_);
      float q = 0.f;
#pragma unroll
      for (int c = 0; c < 4; ++c) {
        const float a = bf_lo(r[j][c]) - mean, bb = bf_hi(r[j][c]) - mean;
        q += a * a + bb * bb;
      }
      const float var = wave_sum(q) * (1.f / D_);
      if (lane == j) { mean_l = mean; var_l = var; }
    }
    if (lane < 8) {
      ((f32x2*)(ws + WS_ST0))[t0 + lane] = f32x2{mean_l, var_l};
      ((f32x2*)(ws + WS_ST1))[t0 + lane] = f32x2{0.f, 0.f};
    }
    unsigned short* xt = (unsigned short*)(ws + WS_XT0);
#pragma unroll
    for (int c = 0; c < 4; ++c) {
      u32x4 lo = {perm_lo(r[1][c], r[0][c]), perm_lo(r[3][c], r[2][c]), perm_lo(r[5][c], r[4][c]), perm_lo(r[7][c], r[6][c])};
      u32x4 hi = {perm_hi(r[1][c], r[0][c]), perm_hi(r[3][c], r[2][c]), perm_hi(r[5][c], r[4][c]), perm_hi(r[7][c], r[6][c])};
      *(u32x4*)(xt + (size_t)(lane * 8 + 2 * c) * L_ + t0) = lo;
      *(u32x4*)(xt + (size_t)(lane * 8 + 2 * c + 1) * L_ + t0) = hi;
    }
    return;
  }
  b -= PB_XT;
  if (b < PB_W1 + PB_W2) {
    const bool isW2 = b >= PB_W1;
    if (isW2) b -= PB_W1;
#pragma unroll
    for (int it = 0; it < 2; ++it) {
      const int u = (b * 2 + it) * 256 + tid;
      u32x2 r[8];
      size_t dst;
      if (!isW2) {
        const int fg = u % 448, o = u / 448;
        const int f = 4 * fg;
#pragma unroll
        for (int j = 0; j < 8; ++j) r[j] = *(const u32x2*)(w1 + (size_t)(8 * o + j) * F_ + f);
        dst = WS_W1T + ((size_t)(((f >> 5) * 32 + (o >> 1)) * 64 + (o & 1) * 32 + (f & 31))) * 16;
      } else {
        const int dg = u & 127, o = u >> 7;
        const int dd = 4 * dg, ks = o >> 1, hh = o & 1;
#pragma unroll
        for (int j = 0; j < 8; ++j)
          r[j] = *(const u32x2*)(w2 + (size_t)(16 * ks + 8 * (j >> 2) + 4 * hh + (j & 3)) * D_ + dd);
        dst = WS_W2T + ((size_t)(((dd >> 5) * 112 + ks) * 64 + hh * 32 + (dd & 31))) * 16;
      }
#pragma unroll
      for (int c = 0; c < 2; ++c) {
        u32x4 lo = {perm_lo(r[1][c], r[0][c]), perm_lo(r[3][c], r[2][c]), perm_lo(r[5][c], r[4][c]), perm_lo(r[7][c], r[6][c])};
        u32x4 hi = {perm_hi(r[1][c], r[0][c]), perm_hi(r[3][c], r[2][c]), perm_hi(r[5][c], r[4][c]), perm_hi(r[7][c], r[6][c])};
        *(u32x4*)(ws + dst + (2 * c) * 16) = lo;
        *(u32x4*)(ws + dst + (2 * c + 1) * 16) = hi;
      }
    }
    return;
  }
  b -= PB_W1 + PB_W2;
  {
    const int cg = tid & 7, rg = tid >> 3;
    const int f0 = b * 64 + cg * 8;
    float s[8] = {0.f, 0.f, 0.f, 0.f, 0.f, 0.f, 0.f, 0.f};
#pragma unroll
    for (int j = 0; j < 16; ++j) {
      const u32x4 v = *(const u32x4*)(w1 + (size_t)(rg * 16 + j) * F_ + f0);
#pragma unroll
      for (int c = 0; c < 4; ++c) { s[2 * c] += bf_lo(v[c]); s[2 * c + 1] += bf_hi(v[c]); }
    }
    float* red = (float*)smem;
#pragma unroll
    for (int e = 0; e < 8; ++e) red[rg * 64 + cg * 8 + e] = s[e];
    __syncthreads();
    if (tid < 64) {
      float t = 0.f;
#pragma unroll
      for (int r = 0; r < 32; ++r) t += red[r * 64 + tid];
      ((float*)(ws + WS_W1S))[b * 64 + tid] = t;
    }
  }
}

constexpr int CV_CS = 260 * 16;
constexpr int CV_KCH = 8 * CV_CS;
constexpr int CV_U0 = 2 * CV_KCH;
constexpr int CV_UCH = (256 + 2048) * 2;
constexpr int CV_RED = CV_U0 + 2 * CV_UCH;
constexpr int CV_LDS = CV_RED + 16384;

template <int SUMS>
__global__ __launch_bounds__(256) void conv_kernel(
    const unsigned short* __restrict__ xin, const f32x2* __restrict__ stats,
    const char* __restrict__ G, const float* __restrict__ kinv,
    const unsigned short* __restrict__ dskip, unsigned short* __restrict__ xout) {
  extern __shared__ __attribute__((aligned(16))) char smem[];
  const int tid = threadIdx.x, wave = tid >> 6, lane = tid & 63;
  const int d0 = blockIdx.x * 2;
  const int ch = wave >> 1, half = wave & 1;
  const int j16 = lane & 15, g = lane >> 4;
  u32x4 ga[2], gb[2];
#pragma unroll
  for (int c = 0; c < 2; ++c) {
    const char* gp = G + (size_t)(d0 + c) * 4096 + tid * 16;
    ga[c] = *(const u32x4*)gp;
    gb[c] = (tid < 255) ? *(const u32x4*)(gp + 16) : u32x4{0u, 0u, 0u, 0u};
  }
  const int toff = 16 * j16 + 4 * g;
  u32x2 xv[4];
  f32x4 st[4][2];
#pragma unroll
  for (int e = 0; e < 4; ++e) {
    const int t = 256 * (4 * half + e) + toff;
    xv[e] = *(const u32x2*)(xin + (size_t)(d0 + ch) * L_ + t);
    st[e][0] = *(const f32x4*)(stats + t);
    st[e][1] = *(const f32x4*)(stats + t + 2);
  }
  const float kscale = kinv[d0 + ch];
  const float dsk = ld_bf(dskip + d0 + ch);
  if (tid < 16) *(u32x4*)(smem + (tid >> 3) * CV_KCH + (tid & 7) * CV_CS + 256 * 16) = u32x4{0u, 0u, 0u, 0u};
  if (tid >= 64 && tid < 128) {
    const int q = tid - 64;
    *(u32x4*)(smem + CV_U0 + (q >> 5) * CV_UCH + (q & 31) * 16) = u32x4{0u, 0u, 0u, 0u};
  }
#pragma unroll
  for (int c = 0; c < 2; ++c) {
    unsigned w[8] = {ga[c][0], ga[c][1], ga[c][2], ga[c][3], gb[c][0], gb[c][1], gb[c][2], gb[c][3]};
    unsigned o[7];
#pragma unroll
    for (int k = 0; k < 7; ++k) o[k] = __builtin_amdgcn_alignbit(w[k + 1], w[k], 16);
    char* dst = smem + c * CV_KCH + tid * 16;
#pragma unroll
    for (int m = 0; m < 4; ++m) {
      *(u32x4*)(dst + (2 * m) * CV_CS) = u32x4{w[m], w[m + 1], w[m + 2], w[m + 3]};
      *(u32x4*)(dst + (2 * m + 1) * CV_CS) = u32x4{o[m], o[m + 1], o[m + 2], o[m + 3]};
    }
  }
  u32x2 ub[4];
#pragma unroll
  for (int e = 0; e < 4; ++e) {
    float uu[4];
#pragma unroll
    for (int r = 0; r < 4; ++r) {
      float a = st[e][r >> 1][(r & 1) * 2], bq = st[e][r >> 1][(r & 1) * 2 + 1];
      float mean, var;
      if (SUMS) { mean = a * (1.f / D_); var = fmaxf(bq * (1.f / D_) - mean * mean, 0.f); }
      else { mean = a; var = bq; }
      const float m = rbf(mean);
      const float sd = rbf(sqrtf(rbf(rbf(var) + 1e-5f)));
      const float x = (r & 1) ? bf_hi(xv[e][r >> 1]) : bf_lo(xv[e][r >> 1]);
      uu[r] = rbf(rbf(x - m) / sd);
    }
    ub[e] = u32x2{pk_bf16(uu[0], uu[1]), pk_bf16(uu[2], uu[3])};
    const int t = 256 * (4 * half + e) + toff;
    *(u32x2*)(smem + CV_U0 + ch * CV_UCH + (256 + t) * 2) = u32x2{pk_f16(uu[0], uu[1]), pk_f16(uu[2], uu[3])};
  }
  __syncthreads();
  const int cc = (7 - j16) & 7;
  const int zl = (2031 - j16 - cc) / 8 + g;
  const char* abase = smem + ch * CV_KCH + cc * CV_CS + (zl - 32 * 7 - 4 * (4 * half + 3)) * 16;
  const char* bbase = smem + CV_U0 + ch * CV_UCH + 480 + 32 * j16 + 16 * g - 64 * (4 * half + 3);
  f32x4 acc[8];
#pragma unroll
  for (int q = 0; q < 8; ++q) acc[q] = f32x4{0.f, 0.f, 0.f, 0.f};
#pragma unroll
  for (int it = 0; it < 4; ++it) {
    f16x8 A[8], B[8];
#pragma unroll
    for (int k = 0; k < 8; ++k) {
      A[k] = *(const f16x8*)(abase + (7 - k) * 512 + (3 - it) * 64);
      B[k] = *(const f16x8*)(bbase + k * 512 + (3 - it) * 64);
    }
#pragma unroll
    for (int b1 = 0; b1 < 8; ++b1)
#pragma unroll
      for (int q = b1; q < 8; ++q)
        acc[q] = __builtin_amdgcn_mfma_f32_16x16x32_f16(A[b1], B[q - b1], acc[q], 0, 0, 0);
  }
  f32x4 keep[4];
  {
    char* wr = smem + CV_RED + (ch * 2 + half) * 4096 + lane * 16;
#pragma unroll
    for (int e = 0; e < 4; ++e) {
      f32x4 give;
#pragma unroll
      for (int r = 0; r < 4; ++r) {
        const float lo = acc[e][r], hi = acc[4 + e][r];
        give[r] = half ? lo : hi;
        keep[e][r] = half ? hi : lo;
      }
      *(f32x4*)(wr + e * 1024) = give;
    }
  }
  __syncthreads();
  {
    const char* rd = smem + CV_RED + (ch * 2 + (half ^ 1)) * 4096 + lane * 16;
#pragma unroll
    for (int e = 0; e < 4; ++e) {
      const f32x4 o = *(const f32x4*)(rd + e * 1024);
      const f32x4 y = keep[e] + o;
      float xo[4];
#pragma unroll
      for (int r = 0; r < 4; ++r) {
        const float u = (r & 1) ? bf_hi(ub[e][r >> 1]) : bf_lo(ub[e][r >> 1]);
        const float x = (r & 1) ? bf_hi(xv[e][r >> 1]) : bf_lo(xv[e][r >> 1]);
        const float a = rbf(rbf(y[r] * kscale) + rbf(u * dsk));
        xo[r] = x + a;
      }
      const int t = 256 * (4 * half + e) + toff;
      *(u32x2*)(xout + (size_t)(d0 + ch) * L_ + t) = u32x2{pk_bf16(xo[0], xo[1]), pk_bf16(xo[2], xo[3])};
    }
  }
}

constexpr int G1_LDS = 128 * 1024 + 1024;

__global__ __launch_bounds__(256, 1) void gemm1_kernel(
    const unsigned short* __restrict__ xt, const char* __restrict__ w1t,
    const float* __restrict__ w1s, char* __restrict__ hbuf) {
  extern __shared__ __attribute__((aligned(16))) char smem[];
  const int tid = threadIdx.x, lane = tid & 63;
  const int wave = __builtin_amdgcn_readfirstlane(tid >> 6);
  const int bx = blockIdx.x & 7, bi = blockIdx.x >> 3;
  const int mt = (bx >> 1) * 4 + (bi & 3), nt = (bx & 1) * 7 + (bi >> 2);
  const int ft = nt * 4 + wave;
  const int r32 = lane & 31, hh = lane >> 5;
  f32x4 ws4[4];
#pragma unroll
  for (int q = 0; q < 4; ++q) ws4[q] = *(const f32x4*)(w1s + ft * 32 + 8 * q + 4 * hh);
  u32x4 wf[32];
  const char* wsrc = w1t + (size_t)ft * 32 * 1024 + lane * 16;
  const int grow = lane >> 4, gslot = lane & 15;
  const unsigned short* xsrc = xt + (size_t)(wave * 16 + grow) * L_ + mt * 128 + ((gslot ^ (grow << 2)) * 8);
  static_for<0, 8>([&](auto C) {
    constexpr int c = C;
#pragma unroll
    for (int i = 0; i < 4; ++i)
      __builtin_amdgcn_global_load_lds((const unsigned*)(xsrc + (size_t)(c * 64 + i * 4) * L_),
                                       (LAS unsigned*)(smem + (c * 64 + wave * 16 + i * 4) * 256), 16, 0, 0);
    gload16<0>(wf[4 * c + 0], wsrc + (4 * c) * 1024);
    gload16<1024>(wf[4 * c + 1], wsrc + (4 * c) * 1024);
    gload16<2048>(wf[4 * c + 2], wsrc + (4 * c) * 1024);
    gload16<3072>(wf[4 * c + 3], wsrc + (4 * c) * 1024);
  });
  const int blk = (lane >> 4) & 1, q4 = (lane & 15) >> 2, p4 = lane & 3;
  const unsigned sbase = (unsigned)(uintptr_t)(LAS char*)smem;
  unsigned tb[4], tb2[4];
#pragma unroll
  for (int ct = 0; ct < 4; ++ct) {
    const int cta = (ct + wave) & 3;
    tb[ct] = sbase + (unsigned)((8 * hh + q4) * 256 + (((4 * cta + 2 * blk + (p4 >> 1)) ^ (q4 << 2)) * 16) + 8 * (p4 & 1));
    tb2[ct] = tb[ct] + 65536u;
  }
  f32x16 acc[4];
#pragma unroll
  for (int ct = 0; ct < 4; ++ct)
#pragma unroll
    for (int i = 0; i < 16; ++i) acc[ct][i] = 0.f;
  float ssum = 0.f, ssq = 0.f;
  const bf16x2 ones = {(__bf16)1.0f, (__bf16)1.0f};
  u32x2 bb[2][8];
  auto issue_reads = [&](auto KS) {
    constexpr int ks = KS;
    static_for<0, 4>([&](auto CT) {
      constexpr int ct = CT;
      if constexpr (ks < 16) {
        tr_read<ks * 4096>(bb[ks & 1][2 * ct], tb[ct]);
        tr_read<ks * 4096 + 1024>(bb[ks & 1][2 * ct + 1], tb[ct]);
      } else {
        tr_read<(ks - 16) * 4096>(bb[ks & 1][2 * ct], tb2[ct]);
        tr_read<(ks - 16) * 4096 + 1024>(bb[ks & 1][2 * ct + 1], tb2[ct]);
      }
    });
  };
  wait_vm4<56>(wf[0], wf[1], wf[2], wf[3]);
  __builtin_amdgcn_s_barrier();
  issue_reads(std::integral_constant<int, 0>{});
  static_for<0, 32>([&](auto KS) {
    constexpr int ks = KS;
    if constexpr (ks + 1 < 32) {
      if constexpr ((ks + 1) % 4 == 0) {
        constexpr int c = (ks + 1) / 4;
        wait_vm4<56 - 8 * c>(wf[4 * c], wf[4 * c + 1], wf[4 * c + 2], wf[4 * c + 3]);
        __builtin_amdgcn_s_barrier();
      }
      issue_reads(std::integral_constant<int, ks + 1>{});
      wait_lgkm8<8>(bb[ks & 1]);
    } else {
      wait_lgkm8<0>(bb[ks & 1]);
    }
    const bf16x8 a = __builtin_bit_cast(bf16x8, wf[ks]);
#pragma unroll
    for (int ct = 0; ct < 4; ++ct) {
      const u32x4 bu = {bb[ks & 1][2 * ct][0], bb[ks & 1][2 * ct][1], bb[ks & 1][2 * ct + 1][0], bb[ks & 1][2 * ct + 1][1]};
      acc[ct] = __builtin_amdgcn_mfma_f32_32x32x16_bf16(a, __builtin_bit_cast(bf16x8, bu), acc[ct], 0, 0, 0);
      if (ct == 0) {
#pragma unroll
        for (int e = 0; e < 4; ++e) {
          const unsigned wv = bu[e];
          const bf16x2 v = __builtin_bit_cast(bf16x2, wv);
          ssum = __builtin_amdgcn_fdot2_f32_bf16(v, ones, ssum, false);
          ssq = __builtin_amdgcn_fdot2_f32_bf16(v, v, ssq, false);
        }
      }
    }
  });
  ssum += __shfl_xor(ssum, 32, 64);
  ssq += __shfl_xor(ssq, 32, 64);
  f32x2* sst = (f32x2*)(smem + 128 * 1024);
  if (lane < 32) {
    const float mean = ssum * (1.f / D_);
    const float var = fmaxf(ssq * (1.f / D_) - mean * mean, 0.f);
    const float m = rbf(mean);
    const float sd = rbf(sqrtf(rbf(rbf(var) + 1e-5f)));
    sst[wave * 32 + lane] = f32x2{m, 1.f / sd};
  }
  __syncthreads();
#pragma unroll
  for (int ct = 0; ct < 4; ++ct) {
    const int cta = (ct + wave) & 3;
    const f32x2 ms = sst[cta * 32 + r32];
    char* dst = hbuf + ((size_t)((mt * 4 + cta) * 112 + ft * 2) * 64 + lane) * 16;
#pragma unroll
    for (int s = 0; s < 2; ++s) {
      float v[8];
#pragma unroll
      for (int j = 0; j < 8; ++j) {
        const int reg = 8 * s + j;
        const float wsum = ws4[reg >> 2][reg & 3];
        v[j] = fmaxf((acc[ct][reg] - ms[0] * wsum) * ms[1], 0.f);
      }
      *(u32x4*)(dst + s * 1024) = u32x4{pk_bf16(v[0], v[1]), pk_bf16(v[2], v[3]), pk_bf16(v[4], v[5]), pk_bf16(v[6], v[7])};
    }
  }
}

constexpr int G2_R = 8;
constexpr int G2_LDS = G2_R * 16384;

template <int LAST>
__global__ __launch_bounds__(256, 1) void gemm2_kernel(
    const char* __restrict__ hbuf, const char* __restrict__ w2t,
    const unsigned short* __restrict__ xin, unsigned short* __restrict__ xout,
    float* __restrict__ stats) {
  extern __shared__ __attribute__((aligned(16))) char smem[];
  const int tid = threadIdx.x, lane = tid & 63;
  const int wave = __builtin_amdgcn_readfirstlane(tid >> 6);
  const int bx = blockIdx.x & 7, bi = blockIdx.x >> 3;
  const int mt = (bx >> 1) * 8 + (bi & 7), nt = (bx & 1) * 4 + (bi >> 3);
  const int mi = wave >> 1, ni = wave & 1;
  const int r32 = lane & 31, hh = lane >> 5;
  const int tok = mt * 64 + mi * 32 + r32;
  const int dbase = nt * 64 + ni * 32 + 4 * hh;
  unsigned short xr[16];
#pragma unroll
  for (int i = 0; i < 16; ++i) xr[i] = xin[(size_t)(dbase + (i & 3) + 8 * (i >> 2)) * L_ + tok];
  const char* src = (wave < 2) ? (hbuf + (size_t)(mt * 2 + wave) * 112 * 1024) : (w2t + (size_t)(nt * 2 + wave - 2) * 112 * 1024);
  src += lane * 16;
  auto issue = [&](int c) {
#pragma unroll
    for (int kk = 0; kk < 4; ++kk)
      __builtin_amdgcn_global_load_lds((const unsigned*)(src + (size_t)(c * 4 + kk) * 1024),
                                       (LAS unsigned*)(smem + (c % G2_R) * 16384 + (kk * 4 + wave) * 1024), 16, 0, 0);
  };
#pragma unroll
  for (int c = 0; c < G2_R - 1; ++c) issue(c);
  f32x16 acc;
#pragma unroll
  for (int i = 0; i < 16; ++i) acc[i] = 0.f;
  const char* rdh = smem + mi * 1024 + lane * 16;
  const char* rdw = smem + (2 + ni) * 1024 + lane * 16;
  static_for<0, 28>([&](auto C) {
    constexpr int c = C;
    constexpr int outstanding = (27 - c < G2_R - 2) ? (27 - c) : (G2_R - 2);
    wait_vm_lgkm0<4 * outstanding>();
    __builtin_amdgcn_s_barrier();
    if constexpr (c + G2_R - 1 < 28) issue(c + G2_R - 1);
    bf16x8 hf[4], wfr[4];
#pragma unroll
    for (int kk = 0; kk < 4; ++kk) {
      hf[kk] = *(const bf16x8*)(rdh + (c % G2_R) * 16384 + kk * 4096);
      wfr[kk] = *(const bf16x8*)(rdw + (c % G2_R) * 16384 + kk * 4096);
    }
#pragma unroll
    for (int kk = 0; kk < 4; ++kk)
      acc = __builtin_amdgcn_mfma_f32_32x32x16_bf16(wfr[kk], hf[kk], acc, 0, 0, 0);
  });
  float xo[16];
  float s1 = 0.f, s2 = 0.f;
#pragma unroll
  for (int i = 0; i < 16; ++i) {
    const float v = rbf(__uint_as_float(((unsigned)xr[i]) << 16) + rbf(acc[i]));
    xo[i] = v;
    s1 += v; s2 += v * v;
  }
  if (LAST) {
#pragma unroll
    for (int q = 0; q < 4; ++q)
      *(u32x2*)(xout + (size_t)tok * D_ + dbase + 8 * q) =
          u32x2{pk_bf16(xo[4 * q], xo[4 * q + 1]), pk_bf16(xo[4 * q + 2], xo[4 * q + 3])};
  } else {
#pragma unroll
    for (int i = 0; i < 16; ++i)
      xout[(size_t)(dbase + (i & 3) + 8 * (i >> 2)) * L_ + tok] = (unsigned short)(__float_as_uint(xo[i]) >> 16);
    s1 += __shfl_xor(s1, 32, 64);
    s2 += __shfl_xor(s2, 32, 64);
    atomicAdd(stats + 2 * tok + hh, hh ? s2 : s1);
  }
}

extern "C" void kernel_launch(void* const* d_in, const int* in_sizes, int n_in,
                              void* d_out, int out_size, void* d_ws, size_t ws_size,
                              hipStream_t stream) {
  const unsigned short* x0 = (const unsigned short*)d_in[0];
  const unsigned short* logA = (const unsigned short*)d_in[1];
  const unsigned short* Aim = (const unsigned short*)d_in[2];
  const unsigned short* Cre = (const unsigned short*)d_in[3];
  const unsigned short* Cim = (const unsigned short*)d_in[4];
  const unsigned short* logdt = (const unsigned short*)d_in[5];
  const unsigned short* dskip = (const unsigned short*)d_in[6];
  const unsigned short* w1 = (const unsigned short*)d_in[8];
  const unsigned short* w2 = (const unsigned short*)d_in[9];
  char* ws = (char*)d_ws;
  (void)hipFuncSetAttribute((const void*)conv_kernel<0>, hipFuncAttributeMaxDynamicSharedMemorySize, CV_LDS);
  (void)hipFuncSetAttribute((const void*)conv_kernel<1>, hipFuncAttributeMaxDynamicSharedMemorySize, CV_LDS);
  (void)hipFuncSetAttribute((const void*)gemm1_kernel, hipFuncAttributeMaxDynamicSharedMemorySize, G1_LDS);
  (void)hipFuncSetAttribute((const void*)gemm2_kernel<0>, hipFuncAttributeMaxDynamicSharedMemorySize, G2_LDS);
  (void)hipFuncSetAttribute((const void*)gemm2_kernel<1>, hipFuncAttributeMaxDynamicSharedMemorySize, G2_LDS);

  unsigned short* xt0 = (unsigned short*)(ws + WS_XT0);
  unsigned short* xt1 = (unsigned short*)(ws + WS_XT1);
  unsigned short* xt2 = (unsigned short*)(ws + WS_XT2);
  unsigned short* xt3 = (unsigned short*)(ws + WS_XT3);
  const float* kinv = (const float*)(ws + WS_KINV);
  const float* w1s = (const float*)(ws + WS_W1S);

  prep_kernel<<<PREP_BLOCKS, 256, 0, stream>>>(x0, logA, Aim, Cre, Cim, logdt, w1, w2, ws);
  conv_kernel<0><<<256, 256, CV_LDS, stream>>>(xt0, (const f32x2*)(ws + WS_ST0), ws + WS_G, kinv, dskip, xt1);
  gemm1_kernel<<<224, 256, G1_LDS, stream>>>(xt1, ws + WS_W1T, w1s, ws + WS_H);
  gemm2_kernel<0><<<256, 256, G2_LDS, stream>>>(ws + WS_H, ws + WS_W2T, xt1, xt2, (float*)(ws + WS_ST1));
  conv_kernel<1><<<256, 256, CV_LDS, stream>>>(xt2, (const f32x2*)(ws + WS_ST1), ws + WS_G, kinv, dskip, xt3);
  gemm1_kernel<<<224, 256, G1_LDS, stream>>>(xt3, ws + WS_W1T, w1s, ws + WS_H);
  gemm2_kernel<1><<<256, 256, G2_LDS, stream>>>(ws + WS_H, ws + WS_W2T, xt3, (unsigned short*)d_out, nullptr);
}
```

```cpp
#include <hip/hip_runtime.h>
#include <hip/hip_bf16.h>
#include <stdint.h>

typedef __attribute__((ext_vector_type(8))) __bf16 bf16x8;
typedef __attribute__((ext_vector_type(2))) __bf16 bf16x2;
typedef __attribute__((ext_vector_type(4))) short s16x4;
typedef __attribute__((ext_vector_type(2))) short s16x2;
typedef __attribute__((ext_vector_type(8))) short s16x8;
typedef __attribute__((ext_vector_type(8))) _Float16 f16x8;
typedef __attribute__((ext_vector_type(2))) _Float16 f16x2;
typedef __attribute__((ext_vector_type(2))) float f32x2;
typedef __attribute__((ext_vector_type(4))) float f32x4;
typedef __attribute__((ext_vector_type(16))) float f32x16;
typedef __attribute__((ext_vector_type(4))) unsigned u32x4;
typedef __attribute__((ext_vector_type(2))) unsigned u32x2;
#define LAS __attribute__((address_space(3)))
#define DI __device__ __forceinline__

constexpr int L_ = 2048, D_ = 512, N_ = 128, F_ = 1792;

constexpr size_t MiB = 1024 * 1024;
constexpr size_t WS_G    = 0;
constexpr size_t WS_XT0  = 2 * MiB;
constexpr size_t WS_XT1  = 4 * MiB;
constexpr size_t WS_XT2  = 6 * MiB;
constexpr size_t WS_XT3  = 8 * MiB;
constexpr size_t WS_W1T  = 10 * MiB;
constexpr size_t WS_W2T  = 12 * MiB;
constexpr size_t WS_H    = 14 * MiB;
constexpr size_t WS_KINV = 22 * MiB;
constexpr size_t WS_ST0  = WS_KINV + 4096;
constexpr size_t WS_ST1  = WS_ST0 + 16384;
constexpr size_t WS_W1S  = WS_ST1 + 16384;
constexpr size_t WS_SW   = WS_W1S + 8192;

#define STAMP(kid, i)
DI float bf_lo(unsigned u) { return __uint_as_float(u << 16); }
DI float bf_hi(unsigned u) { return __uint_as_float(u & 0xffff0000u); }
DI unsigned pk_bf16(float a, float b) {
  f32x2 v = {a, b};
  return __builtin_bit_cast(unsigned, __builtin_convertvector(v, bf16x2));
}
DI unsigned pk_f16(float a, float b) {
  f32x2 v = {a, b};
  return __builtin_bit_cast(unsigned, __builtin_convertvector(v, f16x2));
}
DI float rbf(float a) { return bf_lo(pk_bf16(a, 0.f)); }
DI float ld_bf(const unsigned short* p) { return __uint_as_float(((unsigned)*p) << 16); }
DI unsigned perm_lo(unsigned hi, unsigned lo) { return __builtin_amdgcn_perm(hi, lo, 0x05040100u); }
DI unsigned perm_hi(unsigned hi, unsigned lo) { return __builtin_amdgcn_perm(hi, lo, 0x07060302u); }
DI float wave_sum(float v) {
#pragma unroll
  for (int o = 32; o >= 1; o >>= 1) v += __shfl_xor(v, o, 64);
  return v;
}
template <int N> DI void wait_vm() { asm volatile("s_waitcnt vmcnt(%0)" ::"n"(N) : "memory"); }
template <int OFF> DI void gload16(u32x4& dst, const char* p) {
  asm volatile("global_load_dwordx4 %0, %1, off offset:%2" : "=v"(dst) : "v"(p), "n"(OFF));
}
template <int N> DI void wait_vm4(u32x4& a, u32x4& b, u32x4& c, u32x4& d) {
  asm volatile("s_waitcnt vmcnt(%4)" : "+v"(a), "+v"(b), "+v"(c), "+v"(d) : "n"(N) : "memory");
}
template <int OFF> DI void tr_read(u32x2& d, unsigned addr) {
  asm volatile("ds_read_b64_tr_b16 %0, %1 offset:%2" : "=v"(d) : "v"(addr), "n"(OFF));
}
template <int N> DI void wait_lgkm8(u32x2* b) {
  asm volatile("s_waitcnt lgkmcnt(%8)"
               : "+v"(b[0]), "+v"(b[1]), "+v"(b[2]), "+v"(b[3]), "+v"(b[4]), "+v"(b[5]), "+v"(b[6]), "+v"(b[7])
               : "n"(N) : "memory");
}
template <int N> DI void wait_vm_lgkm0() { asm volatile("s_waitcnt vmcnt(%0) lgkmcnt(0)" ::"n"(N) : "memory"); }
template <int I, int E, class F> DI void static_for(F&& f) {
  if constexpr (I < E) { f(std::integral_constant<int, I>{}); static_for<I + 1, E>(f); }
}

constexpr int PB_KGEN = 512, PB_XT = 64, PB_WS = 28;
constexpr int PREP_BLOCKS = PB_KGEN + PB_XT + PB_WS;
constexpr int TROW = 528;

__host__ __device__ constexpr int tab_w(int idx) {
  return idx < 16 ? idx : (idx < 40 ? ((8 + ((idx - 16) & 7)) << (1 + ((idx - 16) >> 3))) : 128);
}
__host__ __device__ constexpr int ilog2c(int w) { return w <= 1 ? 0 : 1 + ilog2c(w >> 1); }
__host__ __device__ constexpr int tab_idx(int w) {
  return w < 16 ? w : (w == 128 ? 40 : 16 + 8 * (ilog2c(w) - 4) + ((w >> (ilog2c(w) - 3)) - 8));
}
DI int tab_idx_rt(int w) {
  if (w < 16) return w;
  int lg = 31 - __clz(w);
  return 16 + 8 * (lg - 4) + ((w >> (lg - 3)) - 8);
}

__global__ __launch_bounds__(256) void prep_kernel(
    const unsigned short* __restrict__ x0, const unsigned short* __restrict__ logA,
    const unsigned short* __restrict__ Aim, const unsigned short* __restrict__ Cre,
    const unsigned short* __restrict__ Cim, const unsigned short* __restrict__ logdt,
    const unsigned short* __restrict__ w1, const unsigned short* __restrict__ w2,
    char* __restrict__ ws) {
  __shared__ __attribute__((aligned(16))) char smem[82 * TROW];
  const int tid = threadIdx.x, wave = tid >> 6, lane = tid & 63;
  int b = blockIdx.x;
  STAMP(0, 0);
  if (b < PB_KGEN) {
    const int d = b;
    const int isH = wave >> 1;
    const int n = (wave & 1) * 64 + lane;
    const float dt = rbf(expf(ld_bf(logdt + d)));
    const float ar = -rbf(expf(ld_bf(logA + d * N_ + n))) * dt;
    const float ai = ld_bf(Aim + d * N_ + n) * dt;
    const float c0r = ld_bf(Cre + d * N_ + lane) * dt, c0i = ld_bf(Cim + d * N_ + lane) * dt;
    const float c1r = ld_bf(Cre + d * N_ + 64 + lane) * dt, c1i = ld_bf(Cim + d * N_ + 64 + lane) * dt;
    float S = wave_sum(2.f * (fabsf(c0r) + fabsf(c0i) + fabsf(c1r) + fabsf(c1i)));
    int ex = 0;
    (void)frexpf(S, &ex);
    if (!(S > 0.f) || !(S < 3.0e38f)) ex = 15;
    ex = max(-100, min(ex, 120));
    const float scale = ldexpf(1.f, 15 - ex);
    const float cr = (wave & 1) ? c1r : c0r, ci = (wave & 1) ? c1i : c0i;
    const float base = isH ? 16.f : 1.f;
    const float er = ar * 1.44269504088896340736f * base;
    const float IH = 0.15915494309189533577f;
    const float IL = (float)(0.15915494309189533577 - (double)0.15915494309189533577f);
    float fh = ai * IH;
    float fl = fmaf(ai, IH, -fh) + ai * IL;
    fh *= base; fl *= base;
    STAMP(0, 1);
    float Er[8], Ei[8];
#pragma unroll
    for (int k = 0; k < 8; ++k) {
      const float p = (float)(1 << k);
      const float mag = __builtin_amdgcn_exp2f(er * p);
      const float ph = __builtin_amdgcn_fractf(fh * p) + fl * p;
      Er[k] = mag * __builtin_amdgcn_cosf(ph);
      Ei[k] = mag * __builtin_amdgcn_sinf(ph);
    }
    float vr[41], vi[41];
    vr[0] = isH ? 2.f * scale * cr : 1.f;
    vi[0] = isH ? 2.f * scale * ci : 0.f;
    const float sgn = isH ? -1.f : 1.f;
    char* trow = smem + isH * (41 * TROW) + n * 4;
    *(unsigned*)trow = pk_f16(vr[0], sgn * vi[0]);
    static_for<1, 41>([&](auto I) {
      constexpr int idx = I;
      constexpr int w = tab_w(idx);
      constexpr int lg = ilog2c(w);
      constexpr int ridx = tab_idx(w - (1 << lg));
      vr[idx] = Er[lg] * vr[ridx] - Ei[lg] * vi[ridx];
      vi[idx] = Er[lg] * vi[ridx] + Ei[lg] * vr[ridx];
      *(unsigned*)(trow + idx * TROW) = pk_f16(vr[idx], sgn * vi[idx]);
    });
    STAMP(0, 2);
    __syncthreads();
    const int s = wave, i16 = lane & 15, g = lane >> 4;
    int hw = (s == 0) ? i16 : (i16 < 8 ? ((8 + i16) << s) : 128);
    const char* arow = smem + 41 * TROW + tab_idx_rt(hw) * TROW + g * 16;
    const char* brow = smem + tab_idx_rt(i16 << s) * TROW + g * 16;
    f32x4 acc = {0.f, 0.f, 0.f, 0.f};
#pragma unroll
    for (int ks = 0; ks < 8; ++ks) {
      f16x8 a = *(const f16x8*)(arow + ks * 64);
      f16x8 bb = *(const f16x8*)(brow + ks * 64);
      acc = __builtin_amdgcn_mfma_f32_16x16x32_f16(a, bb, acc, 0, 0, 0);
    }
    __syncthreads();
    STAMP(0, 3);
    _Float16* kc = (_Float16*)smem;
#pragma unroll
    for (int r = 0; r < 4; ++r) {
      const int i = 4 * g + r;
      const int cidx = (s == 0) ? (16 * i + i16) : (256 + 128 * (s - 1) + 16 * i + i16);
      const bool ok = (s == 0) || (i < 8) || (s == 3 && i == 8 && i16 == 0);
      if (ok) kc[cidx] = (_Float16)acc[r];
    }
    __syncthreads();
    unsigned short o[8];
#pragma unroll
    for (int e = 0; e < 8; ++e) {
      const int l = 2047 - (tid * 8 + e);
      int cidx = l;
      if (l >= 256) {
        const int sh = (31 - __clz(l)) - 7;
        const int q = (l + (1 << (sh - 1)) - 1 + ((l >> sh) & 1)) >> sh;
        cidx = 256 + 128 * (sh - 1) + (q - 128);
      }
      o[e] = ((const unsigned short*)kc)[cidx];
    }
    u32x4 ov = {(unsigned)o[0] | ((unsigned)o[1] << 16), (unsigned)o[2] | ((unsigned)o[3] << 16),
                (unsigned)o[4] | ((unsigned)o[5] << 16), (unsigned)o[6] | ((unsigned)o[7] << 16)};
    *(u32x4*)(ws + WS_G + (size_t)d * 4096 + tid * 16) = ov;
    if (tid == 0) ((float*)(ws + WS_KINV))[d] = ldexpf(1.f, ex - 15);
    STAMP(0, 15);
    return;
  }
  b -= PB_KGEN;
  if (b < PB_XT) {
    const int t0 = b * 32 + wave * 8;
    u32x4 r[8];
#pragma unroll
    for (int j = 0; j < 8; ++j) r[j] = *(const u32x4*)(x0 + (size_t)(t0 + j) * D_ + lane * 8);
    float mean_l = 0.f, var_l = 0.f;
#pragma unroll
    for (int j = 0; j < 8; ++j) {
      float s = 0.f;
#pragma unroll
      for (int c = 0; c < 4; ++c) s += bf_lo(r[j][c]) + bf_hi(r[j][c]);
      const float mean = wave_sum(s) * (1.f / D_);
      float q = 0.f;
#pragma unroll
      for (int c = 0; c < 4; ++c) {
        const float a = bf_lo(r[j][c]) - mean, bb = bf_hi(r[j][c]) - mean;
        q += a * a + bb * bb;
      }
      const float var = wave_sum(q) * (1.f / D_);
      if (lane == j) { mean_l = mean; var_l = var; }
    }
    if (lane < 8) {
      ((f32x2*)(ws + WS_ST0))[t0 + lane] = f32x2{mean_l, var_l};
      ((f32x2*)(ws + WS_ST1))[t0 + lane] = f32x2{0.f, 0.f};
      ((unsigned*)(ws + WS_SW))[t0 + lane] = 0u;
      ((unsigned*)(ws + WS_SW))[2048 + t0 + lane] = 0u;
    }
    unsigned short* xt = (unsigned short*)(ws + WS_XT0);
#pragma unroll
    for (int c = 0; c < 4; ++c) {
      u32x4 lo = {perm_lo(r[1][c], r[0][c]), perm_lo(r[3][c], r[2][c]), perm_lo(r[5][c], r[4][c]), perm_lo(r[7][c], r[6][c])};
      u32x4 hi = {perm_hi(r[1][c], r[0][c]), perm_hi(r[3][c], r[2][c]), perm_hi(r[5][c], r[4][c]), perm_hi(r[7][c], r[6][c])};
      *(u32x4*)(xt + (size_t)(lane * 8 + 2 * c) * L_ + t0) = lo;
      *(u32x4*)(xt + (size_t)(lane * 8 + 2 * c + 1) * L_ + t0) = hi;
    }
    STAMP(0, 15);
    return;
  }
  b -= PB_XT;
  {
    const int cg = tid & 7, rg = tid >> 3;
    const int f0 = b * 64 + cg * 8;
    float s[8] = {0.f, 0.f, 0.f, 0.f, 0.f, 0.f, 0.f, 0.f};
#pragma unroll
    for (int j = 0; j < 16; ++j) {
      const u32x4 v = *(const u32x4*)(w1 + (size_t)(rg * 16 + j) * F_ + f0);
#pragma unroll
      for (int c = 0; c < 4; ++c) { s[2 * c] += bf_lo(v[c]); s[2 * c + 1] += bf_hi(v[c]); }
    }
    float* red = (float*)smem;
#pragma unroll
    for (int e = 0; e < 8; ++e) red[rg * 64 + cg * 8 + e] = s[e];
    __syncthreads();
    if (tid < 64) {
      float t = 0.f;
#pragma unroll
      for (int r = 0; r < 32; ++r) t += red[r * 64 + tid];
      ((float*)(ws + WS_W1S))[b * 64 + tid] = t;
    }
    STAMP(0, 15);
  }
}

constexpr int CV_CS = 260 * 16;
constexpr int CV_KCH = 8 * CV_CS;
constexpr int CV_U0 = 2 * CV_KCH;
constexpr int CV_UCH = (256 + 2048) * 2;
constexpr int CV_XS = CV_U0 + 2 * CV_UCH;
constexpr int CV_RED = CV_XS + 2 * 8192;
constexpr int CV_LDS = CV_RED + 16384;

DI void retile_load(int u, const unsigned short* __restrict__ w1, const unsigned short* __restrict__ w2, u32x2* r, size_t& dst) {
  if (u < 28672) {
    const int fg = u % 448, o = u / 448;
    const int f = 4 * fg;
#pragma unroll
    for (int j = 0; j < 8; ++j) r[j] = *(const u32x2*)(w1 + (size_t)(8 * o + j) * F_ + f);
    dst = WS_W1T + ((size_t)(((f >> 5) * 32 + (o >> 1)) * 64 + (o & 1) * 32 + (f & 31))) * 16;
  } else {
    const int v = u - 28672;
    const int dg = v & 127, o = v >> 7;
    const int dd = 4 * dg, ks = o >> 1, hh = o & 1;
#pragma unroll
    for (int j = 0; j < 8; ++j)
      r[j] = *(const u32x2*)(w2 + (size_t)(16 * ks + 8 * (j >> 2) + 4 * hh + (j & 3)) * D_ + dd);
    dst = WS_W2T + ((size_t)(((dd >> 5) * 112 + ks) * 64 + hh * 32 + (dd & 31))) * 16;
  }
}
DI void retile_store(const u32x2* r, char* ws, size_t dst) {
#pragma unroll
  for (int c = 0; c < 2; ++c) {
    u32x4 lo = {perm_lo(r[1][c], r[0][c]), perm_lo(r[3][c], r[2][c]), perm_lo(r[5][c], r[4][c]), perm_lo(r[7][c], r[6][c])};
    u32x4 hi = {perm_hi(r[1][c], r[0][c]), perm_hi(r[3][c], r[2][c]), perm_hi(r[5][c], r[4][c]), perm_hi(r[7][c], r[6][c])};
    *(u32x4*)(ws + dst + (2 * c) * 16) = lo;
    *(u32x4*)(ws + dst + (2 * c + 1) * 16) = hi;
  }
}

template <int SUMS>
__global__ __launch_bounds__(512, 2) void conv_kernel(
    const unsigned short* __restrict__ xin, const f32x2* __restrict__ stats,
    const char* __restrict__ G, const float* __restrict__ kinv,
    const unsigned short* __restrict__ dskip, unsigned short* __restrict__ xout,
    const unsigned short* __restrict__ w1, const unsigned short* __restrict__ w2, char* __restrict__ ws) {
  extern __shared__ __attribute__((aligned(16))) char smem[];
  const int tid = threadIdx.x, lane = tid & 63;
  const int wave = __builtin_amdgcn_readfirstlane(tid >> 6);
  const int d0 = blockIdx.x * 2;
  STAMP(1 + 3 * SUMS, 0);
  const int gz = tid & 255, gc = tid >> 8;
  const char* gp = G + (size_t)(d0 + gc) * 4096 + gz * 16;
  const u32x4 ga = *(const u32x4*)gp;
  const u32x4 gb = (gz < 255) ? *(const u32x4*)(gp + 16) : u32x4{0u, 0u, 0u, 0u};
  const int t4 = tid * 4;
  const f32x4 st0 = *(const f32x4*)(stats + t4), st1 = *(const f32x4*)(stats + t4 + 2);
  u32x2 xv[2];
  xv[0] = *(const u32x2*)(xin + (size_t)d0 * L_ + t4);
  xv[1] = *(const u32x2*)(xin + (size_t)(d0 + 1) * L_ + t4);
  const unsigned dsk2 = *(const unsigned*)(dskip + d0);
  const int ch = wave >> 2, half = wave & 1, qs = (wave >> 1) & 1;
  const float kscale = kinv[d0 + ch];
  u32x2 rt[8];
  size_t rdst = 0;
  const bool do_rt = (!SUMS) && tid < 224;
  if (tid < 16) *(u32x4*)(smem + (tid >> 3) * CV_KCH + (tid & 7) * CV_CS + 256 * 16) = u32x4{0u, 0u, 0u, 0u};
  if (tid >= 64 && tid < 128) {
    const int q = tid - 64;
    *(u32x4*)(smem + CV_U0 + (q >> 5) * CV_UCH + (q & 31) * 16) = u32x4{0u, 0u, 0u, 0u};
  }
  {
    unsigned w[8] = {ga[0], ga[1], ga[2], ga[3], gb[0], gb[1], gb[2], gb[3]};
    unsigned o[7];
#pragma unroll
    for (int k = 0; k < 7; ++k) o[k] = __builtin_amdgcn_alignbit(w[k + 1], w[k], 16);
    char* dst = smem + gc * CV_KCH + gz * 16;
#pragma unroll
    for (int m = 0; m < 4; ++m) {
      *(u32x4*)(dst + (2 * m) * CV_CS) = u32x4{w[m], w[m + 1], w[m + 2], w[m + 3]};
      *(u32x4*)(dst + (2 * m + 1) * CV_CS) = u32x4{o[m], o[m + 1], o[m + 2], o[m + 3]};
    }
  }
  STAMP(1 + 3 * SUMS, 1);
  {
    float m[4], sd[4], rs[4];
#pragma unroll
    for (int r = 0; r < 4; ++r) {
      const float a = (r < 2) ? st0[(r & 1) * 2] : st1[(r & 1) * 2];
      const float bq = (r < 2) ? st0[(r & 1) * 2 + 1] : st1[(r & 1) * 2 + 1];
      float mean, var;
      if (SUMS) { mean = a * (1.f / D_); var = fmaxf(bq * (1.f / D_) - mean * mean, 0.f); }
      else { mean = a; var = bq; }
      m[r] = rbf(mean);
      sd[r] = rbf(__builtin_amdgcn_sqrtf(rbf(rbf(var) + 1e-5f)));
      rs[r] = __builtin_amdgcn_rcpf(sd[r]);
    }
#pragma unroll
    for (int c = 0; c < 2; ++c) {
      const float dsk = c ? bf_hi(dsk2) : bf_lo(dsk2);
      float uu[4];
      unsigned xs[4];
#pragma unroll
      for (int r = 0; r < 4; ++r) {
        const unsigned xw = xv[c][r >> 1];
        const float x = (r & 1) ? bf_hi(xw) : bf_lo(xw);
        const float dd = rbf(x - m[r]);
        float q = dd * rs[r];
        q = fmaf(fmaf(-q, sd[r], dd), rs[r], q);
        uu[r] = rbf(q);
        xs[r] = pk_bf16(x, uu[r] * dsk);
      }
      *(u32x2*)(smem + CV_U0 + c * CV_UCH + (256 + t4) * 2) = u32x2{pk_f16(uu[0], uu[1]), pk_f16(uu[2], uu[3])};
      *(u32x4*)(smem + CV_XS + c * 8192 + t4 * 4) = u32x4{xs[0], xs[1], xs[2], xs[3]};
    }
  }
  STAMP(1 + 3 * SUMS, 2);
  if (do_rt) retile_load(blockIdx.x * 224 + tid, w1, w2, rt, rdst);
  __syncthreads();
  STAMP(1 + 3 * SUMS, 3);
  const int j16 = lane & 15, g = lane >> 4;
  const int cc = (7 - j16) & 7;
  const int zl = (2031 - j16 - cc) / 8 + g;
  const char* abase = smem + ch * CV_KCH + cc * CV_CS + (zl - 32 * 7 - 4 * (4 * half + 3)) * 16;
  const char* bbase = smem + CV_U0 + ch * CV_UCH + 480 + 32 * j16 + 16 * g - 64 * (4 * half + 3);
  f32x4 acc[4];
#pragma unroll
  for (int q = 0; q < 4; ++q) acc[q] = f32x4{0.f, 0.f, 0.f, 0.f};
  auto body = [&](auto QS) {
    constexpr int S = QS;
    constexpr int Q[4] = {S ? 6 : 7, S ? 5 : 4, S ? 2 : 3, S ? 1 : 0};
    constexpr int NF = S ? 7 : 8;
#pragma unroll
    for (int it = 0; it < 4; ++it) {
      f16x8 A[NF], B[NF];
#pragma unroll
      for (int k = 0; k < NF; ++k) {
        A[k] = *(const f16x8*)(abase + (7 - k) * 512 + (3 - it) * 64);
        B[k] = *(const f16x8*)(bbase + k * 512 + (3 - it) * 64);
      }
#pragma unroll
      for (int b1 = 0; b1 < NF; ++b1)
#pragma unroll
        for (int e = 0; e < 4; ++e)
          if (b1 <= Q[e]) acc[e] = __builtin_amdgcn_mfma_f32_16x16x32_f16(A[b1], B[Q[e] - b1], acc[e], 0, 0, 0);
    }
  };
  if (qs) body(std::integral_constant<int, 1>{}); else body(std::integral_constant<int, 0>{});
  STAMP(1 + 3 * SUMS, 4);
  if (do_rt) retile_store(rt, ws, rdst);
  f32x4 keep[2];
  {
    char* wr = smem + CV_RED + wave * 2048 + lane * 16;
#pragma unroll
    for (int e = 0; e < 2; ++e) {
      f32x4 give;
#pragma unroll
      for (int r = 0; r < 4; ++r) {
        const float lo = acc[e][r], hi = acc[2 + e][r];
        give[r] = half ? lo : hi;
        keep[e][r] = half ? hi : lo;
      }
      *(f32x4*)(wr + e * 1024) = give;
    }
  }
  __syncthreads();
  {
    const char* rd = smem + CV_RED + (wave ^ 1) * 2048 + lane * 16;
    const int toff = 16 * j16 + 4 * g;
#pragma unroll
    for (int e = 0; e < 2; ++e) {
      const int q = half ? (e ? (qs ? 1 : 0) : (qs ? 2 : 3)) : (e ? (qs ? 5 : 4) : (qs ? 6 : 7));
      const int t = 256 * q + toff;
      const f32x4 o = *(const f32x4*)(rd + e * 1024);
      const u32x4 xs = *(const u32x4*)(smem + CV_XS + ch * 8192 + t * 4);
      const f32x4 y = keep[e] + o;
      float xo[4];
#pragma unroll
      for (int r = 0; r < 4; ++r) {
        const unsigned w = xs[r];
        xo[r] = bf_lo(w) + rbf(rbf(y[r] * kscale) + bf_hi(w));
      }
      *(u32x2*)(xout + ((size_t)(t >> 7) * D_ + d0 + ch) * 128 + (t & 127)) = u32x2{pk_bf16(xo[0], xo[1]), pk_bf16(xo[2], xo[3])};
    }
  }
  STAMP(1 + 3 * SUMS, 15);
}

constexpr int G1_R = 9;
constexpr int G1_LDS = G1_R * 16384 + 1024;
#define G1_W0 5
#define G1_RAMP 1
#define G1_WMAX 6
__host__ __device__ constexpr int g1_iss(int i) {
  int v = G1_W0 + G1_RAMP * i;
  if (v > i + G1_WMAX) v = i + G1_WMAX;
  return v < 16 ? v : 16;
}

template <int N> DI void wait_vm1(u32x4& a) { asm volatile("s_waitcnt vmcnt(%1)" : "+v"(a) : "n"(N) : "memory"); }
template <int N> DI void wait_lgkm2(u32x2& a, u32x2& b) {
  asm volatile("s_waitcnt lgkmcnt(%2)" : "+v"(a), "+v"(b) : "n"(N) : "memory");
}
template <int N> DI void wait_lgkm3(u32x2& a, u32x2& b, u32x4& c) {
  asm volatile("s_waitcnt lgkmcnt(%3)" : "+v"(a), "+v"(b), "+v"(c) : "n"(N) : "memory");
}
template <int N> DI void wait_lgkm4(u32x2* b) {
  asm volatile("s_waitcnt lgkmcnt(%4)" : "+v"(b[0]), "+v"(b[1]), "+v"(b[2]), "+v"(b[3]) : "n"(N) : "memory");
}
template <int OFF> DI void lds_read16(u32x4& d, unsigned addr) {
  asm volatile("ds_read_b128 %0, %1 offset:%2" : "=v"(d) : "v"(addr), "n"(OFF));
}

__global__ __launch_bounds__(512, 2) void gemm1_kernel(
    const unsigned short* __restrict__ xt, const char* __restrict__ w1t,
    const float* __restrict__ w1s, char* __restrict__ hbuf, unsigned* __restrict__ sw) {
  extern __shared__ __attribute__((aligned(16))) char smem[];
  const int tid = threadIdx.x, lane = tid & 63;
  const int wave8 = __builtin_amdgcn_readfirstlane(tid >> 6);
  const int wave = wave8 & 3;
  const int bx = blockIdx.x & 7, bi = blockIdx.x >> 3;
  if (bi >= 28) {
    const int ht = bx * 4 + (bi - 28);
    const int t8 = lane & 7, dg = wave8 * 8 + (lane >> 3);
    const unsigned short* src = xt + ((size_t)(ht >> 1) * D_ + dg * 8) * 128 + (ht & 1) * 64 + t8 * 8;
    u32x4 v[8];
#pragma unroll
    for (int j = 0; j < 8; ++j) v[j] = *(const u32x4*)(src + j * 128);
    float* red = (float*)smem;
    float sm[8];
#pragma unroll
    for (int e = 0; e < 8; ++e) {
      float a = 0.f;
#pragma unroll
      for (int j = 0; j < 8; ++j) a += (e & 1) ? bf_hi(v[j][e >> 1]) : bf_lo(v[j][e >> 1]);
      a += __shfl_xor(a, 8, 64); a += __shfl_xor(a, 16, 64); a += __shfl_xor(a, 32, 64);
      sm[e] = a;
    }
    if (lane < 8) {
#pragma unroll
      for (int e = 0; e < 8; ++e) red[wave8 * 64 + t8 * 8 + e] = sm[e];
    }
    __syncthreads();
    float mean[8];
#pragma unroll
    for (int e = 0; e < 8; ++e) {
      float a = 0.f;
#pragma unroll
      for (int w = 0; w < 8; ++w) a += red[w * 64 + t8 * 8 + e];
      mean[e] = a * (1.f / D_);
    }
#pragma unroll
    for (int e = 0; e < 8; ++e) {
      float a = 0.f;
#pragma unroll
      for (int j = 0; j < 8; ++j) {
        const float dd = ((e & 1) ? bf_hi(v[j][e >> 1]) : bf_lo(v[j][e >> 1])) - mean[e];
        a = fmaf(dd, dd, a);
      }
      a += __shfl_xor(a, 8, 64); a += __shfl_xor(a, 16, 64); a += __shfl_xor(a, 32, 64);
      sm[e] = a;
    }
    if (lane < 8) {
#pragma unroll
      for (int e = 0; e < 8; ++e) red[512 + wave8 * 64 + t8 * 8 + e] = sm[e];
    }
    __syncthreads();
    if (tid < 64) {
      float a = 0.f, q = 0.f;
#pragma unroll
      for (int w = 0; w < 8; ++w) { a += red[w * 64 + tid]; q += red[512 + w * 64 + tid]; }
      const float m = a * (1.f / D_), var = q * (1.f / D_);
      const float sd = sqrtf(rbf(rbf(var) + 1e-5f));
      __hip_atomic_store(sw + ht * 64 + tid, pk_bf16(m, sd), __ATOMIC_RELAXED, __HIP_MEMORY_SCOPE_AGENT);
    }
    return;
  }
  const int mt = (bx >> 1) * 4 + (bi & 3), nt = (bx & 1) * 7 + (bi >> 2);
  const int r32 = lane & 31, hh = lane >> 5;
  const int blk = (lane >> 4) & 1, q4 = (lane & 15) >> 2, p4 = lane & 3;
  const unsigned sbase = (unsigned)(uintptr_t)(LAS char*)smem;
  f32x2* sst = (f32x2*)(smem + G1_R * 16384);
  const int ft = nt * 4 + wave;
  STAMP(2, 0);
  if (wave8 >= 4) {
    const int grow = lane >> 4, gslot = lane & 15;
    const unsigned short* xsrc = xt + ((size_t)mt * D_ + wave * 8 + grow) * 128 + ((gslot ^ (grow << 2)) * 8);
    const char* wsrc = w1t + (size_t)ft * 32 * 1024 + lane * 16;
    auto issue = [&](int c) {
      char* slot = smem + (c % G1_R) * 16384;
#pragma unroll
      for (int i = 0; i < 2; ++i)
        __builtin_amdgcn_global_load_lds((const unsigned*)(xsrc + (size_t)(c * 32 + i * 4) * 128),
                                         (LAS unsigned*)(slot + (wave * 8 + i * 4) * 256), 16, 0, 0);
#pragma unroll
      for (int i = 0; i < 2; ++i)
        __builtin_amdgcn_global_load_lds((const unsigned*)(wsrc + (size_t)(c * 2 + i) * 1024),
                                         (LAS unsigned*)(slot + 8192 + (wave * 2 + i) * 1024), 16, 0, 0);
    };
#pragma unroll
    for (int c = 0; c < g1_iss(0); ++c) issue(c);
    static_for<0, 16>([&](auto C) {
      constexpr int c = C;
      wait_vm<4 * (g1_iss(c) - (c + 1))>();
      __builtin_amdgcn_s_barrier();
#pragma unroll
      for (int cn = g1_iss(c); cn < g1_iss(c + 1); ++cn) issue(cn);
    });
    if (lane < 32) {
      unsigned wv = 0;
      for (int it = 0; it < (1 << 22); ++it) {
        wv = __hip_atomic_load(sw + mt * 128 + wave * 32 + lane, __ATOMIC_RELAXED, __HIP_MEMORY_SCOPE_AGENT);
        if (wv != 0u) break;
        __builtin_amdgcn_s_sleep(1);
      }
      const float rs = 1.f / bf_hi(wv);
      sst[wave * 32 + lane] = f32x2{-bf_lo(wv) * rs, rs};
    }
    __syncthreads();
    return;
  }
  f32x4 ws4[4];
#pragma unroll
  for (int q = 0; q < 4; ++q) ws4[q] = *(const f32x4*)(w1s + ft * 32 + 8 * q + 4 * hh);
  unsigned tb[3][4], ta[3];
#pragma unroll
  for (int ct = 0; ct < 4; ++ct) {
    tb[0][ct] = sbase + (unsigned)((8 * hh + q4) * 256 + (((4 * ct + 2 * blk + (p4 >> 1)) ^ (q4 << 2)) * 16) + 8 * (p4 & 1));
    tb[1][ct] = tb[0][ct] + 65536u; tb[2][ct] = tb[0][ct] + 131072u;
  }
  ta[0] = sbase + 8192 + wave * 2048 + lane * 16; ta[1] = ta[0] + 65536u; ta[2] = ta[0] + 131072u;
  f32x16 acc[4];
#pragma unroll
  for (int ct = 0; ct < 4; ++ct)
#pragma unroll
    for (int i = 0; i < 16; ++i) acc[ct][i] = 0.f;
  constexpr int LA = 6;
  u32x2 bb[8][2];
  u32x4 af[3];
  auto issue_pair = [&](auto NN) {
    constexpr int n = NN, ks = n >> 2, ct = n & 3;
    constexpr int so = ((ks >> 1) % G1_R) * 16384, seg = so >> 16, o = (so & 65535) + (ks & 1) * 4096;
    if constexpr (ct == 0) lds_read16<(so & 65535) + (ks & 1) * 1024>(af[ks % 3], ta[seg]);
    tr_read<o>(bb[n & 7][0], tb[seg][ct]);
    tr_read<o + 1024>(bb[n & 7][1], tb[seg][ct]);
  };
  STAMP(2, 1);
  __builtin_amdgcn_s_barrier();
  STAMP(2, 2);
  static_for<0, LA>([&](auto NN) { issue_pair(NN); });
  static_for<0, 128>([&](auto NN) {
    constexpr int n = NN, ks = n >> 2, ct = n & 3;
    if constexpr (n == 32) STAMP(2, 4);
    if constexpr (n == 64) STAMP(2, 5);
    if constexpr (n == 96) STAMP(2, 6);
    constexpr int last = (n + LA - 1 < 127) ? (n + LA - 1) : 127;
    constexpr int younger = 2 * (last - n) + ((last >> 2) - (n >> 2));
    if constexpr (ct == 0) wait_lgkm3<younger>(bb[n & 7][0], bb[n & 7][1], af[ks % 3]);
    else wait_lgkm2<younger>(bb[n & 7][0], bb[n & 7][1]);
    const u32x4 bu = {bb[n & 7][0][0], bb[n & 7][0][1], bb[n & 7][1][0], bb[n & 7][1][1]};
    acc[ct] = __builtin_amdgcn_mfma_f32_32x32x16_bf16(__builtin_bit_cast(bf16x8, af[ks % 3]), __builtin_bit_cast(bf16x8, bu), acc[ct], 0, 0, 0);
    __builtin_amdgcn_sched_barrier(0);
    if constexpr (n + LA < 128) {
      if constexpr ((n + LA) % 8 == 0) __builtin_amdgcn_s_barrier();
      issue_pair(std::integral_constant<int, n + LA>{});
    }
    __builtin_amdgcn_sched_barrier(0);
  });
  STAMP(2, 3);
  __syncthreads();
#pragma unroll
  for (int ct = 0; ct < 4; ++ct) {
    const f32x2 ms = sst[ct * 32 + r32];
    char* dst = hbuf + ((size_t)((mt * 4 + ct) * 112 + ft * 2) * 64 + lane) * 16;
    const f32x2 c2 = {ms[0], ms[0]}, r2 = {ms[1], ms[1]};
#pragma unroll
    for (int s = 0; s < 2; ++s) {
      u32x4 o;
#pragma unroll
      for (int j = 0; j < 4; ++j) {
        const int reg = 8 * s + 2 * j;
        const f32x2 wsum = {ws4[reg >> 2][reg & 3], ws4[reg >> 2][(reg & 3) + 1]};
        const f32x2 a2 = {acc[ct][reg], acc[ct][reg + 1]};
        const f32x2 v2 = __builtin_elementwise_fma(a2, r2, c2 * wsum);
        const s16x2 pv = __builtin_bit_cast(s16x2, __builtin_convertvector(v2, bf16x2));
        const s16x2 z = {0, 0};
        o[j] = __builtin_bit_cast(unsigned, __builtin_elementwise_max(pv, z));
      }
      *(u32x4*)(dst + s * 1024) = o;
    }
  }
  STAMP(2, 15);
}

constexpr int G2_R = 8;
constexpr int G2_LDS = G2_R * 16384;

template <int N> DI void wait_vm2_lgkm0(u32x4& a, u32x4& b) {
  asm volatile("s_waitcnt vmcnt(%2) lgkmcnt(0)" : "+v"(a), "+v"(b) : "n"(N) : "memory");
}
DI void pin4(bf16x8* f) { asm volatile("" : "+v"(f[0]), "+v"(f[1]), "+v"(f[2]), "+v"(f[3])); }

template <int LAST>
__global__ __launch_bounds__(512, 2) void gemm2_kernel(
    const char* __restrict__ hbuf, const char* __restrict__ w2t,
    const unsigned short* __restrict__ xin, unsigned short* __restrict__ xout,
    float* __restrict__ stats) {
  extern __shared__ __attribute__((aligned(16))) char smem[];
  const int tid = threadIdx.x, lane = tid & 63;
  const int wave = __builtin_amdgcn_readfirstlane(tid >> 6);
  const int bx = blockIdx.x & 7, bi = blockIdx.x >> 3;
  const int mt = (bx >> 1) * 8 + (bi & 7), nt = (bx & 1) * 4 + (bi >> 3);
  const int ni = wave & 1, kq = wave >> 1;
  const int r32 = lane & 31, hh = lane >> 5;
  const int mi_f = kq >> 1, kh_f = kq & 1;
  const int tok = mt * 64 + mi_f * 32 + r32;
  const int dbase = nt * 64 + ni * 32 + 4 * hh + 16 * kh_f;
  STAMP(3 + 3 * LAST, 0);
  const char* hsrc = hbuf + (size_t)(mt * 2) * 112 * 1024 + wave * 1024 + lane * 16;
  const char* wsrc = w2t + (size_t)(nt * 2 + ni) * 112 * 1024 + kq * 2048 + lane * 16;
  char* dstb = smem + wave * 2048;
  u32x4 wr[28];
  const int rot = 0;
  auto issue = [&](auto C) {
    constexpr int c = C;
    int ca = c + rot; if (ca >= 14) ca -= 14;
#pragma unroll
    for (int mi = 0; mi < 2; ++mi)
      __builtin_amdgcn_global_load_lds((const unsigned*)(hsrc + (size_t)mi * 112 * 1024 + ca * 8192),
                                       (LAS unsigned*)(dstb + (c % G2_R) * 16384 + mi * 1024), 16, 0, 0);
    gload16<0>(wr[2 * c], wsrc + ca * 8192);
    gload16<1024>(wr[2 * c + 1], wsrc + ca * 8192);
  };
  unsigned short xr[8];
  f32x16 acc[2];
#pragma unroll
  for (int i = 0; i < 16; ++i) { acc[0][i] = 0.f; acc[1][i] = 0.f; }
  const char* rdh = smem + kq * 4096 + lane * 16;
  bf16x8 hf[2][4];
  auto mainloop = [&](auto GRP) {
    constexpr int grp = GRP;
    static_for<0, G2_R - 1>([&](auto C) { issue(C); });
    STAMP(3 + 3 * LAST, 1);
    static_for<0, 15>([&](auto C) {
      constexpr int c = C;
      if constexpr (c < 14) {
        constexpr int outstanding = (13 - c < G2_R - 2) ? (13 - c) : (G2_R - 2);
        constexpr int extra = (c > 8) ? 8 : 0;
        wait_vm2_lgkm0<4 * outstanding + extra>(wr[2 * c], wr[2 * c + 1]);
        __builtin_amdgcn_s_barrier();
        if constexpr (c >= 1) pin4(hf[(c - 1) & 1]);
        if constexpr (c == 0) STAMP(3 + 3 * LAST, 2);
        if constexpr (c == 4) STAMP(3 + 3 * LAST, 3);
        if constexpr (c == 8) STAMP(3 + 3 * LAST, 4);
        if constexpr (c == 12) STAMP(3 + 3 * LAST, 5);
      }
      if constexpr (c < 14) {
#pragma unroll
        for (int e = 0; e < 4; ++e) hf[c & 1][e] = *(const bf16x8*)(rdh + (c % G2_R) * 16384 + e * 1024);
      }
      auto mem = [&]() {
        if constexpr (c + G2_R - 1 < 14) issue(std::integral_constant<int, c + G2_R - 1>{});
        if constexpr (c == 8) {
#pragma unroll
          for (int i = 0; i < 8; ++i) xr[i] = xin[((size_t)(tok >> 7) * D_ + dbase + (i & 3) + 8 * (i >> 2)) * 128 + (tok & 127)];
          asm volatile("" ::: "memory");
        }
      };
      auto mma = [&]() {
        if constexpr (c >= 1) {
          constexpr int p = c - 1;
#pragma unroll
          for (int j = 0; j < 2; ++j)
#pragma unroll
            for (int mi = 0; mi < 2; ++mi)
              acc[mi] = __builtin_amdgcn_mfma_f32_32x32x16_bf16(__builtin_bit_cast(bf16x8, wr[2 * p + j]), hf[p & 1][j * 2 + mi], acc[mi], 0, 0, 0);
        }
      };
      __builtin_amdgcn_sched_barrier(0);
      if constexpr (grp == 0) { mem(); __builtin_amdgcn_sched_barrier(0); mma(); }
      else { mma(); __builtin_amdgcn_sched_barrier(0); mem(); }
      __builtin_amdgcn_sched_barrier(0);
    });
  };
  if (wave < 4) mainloop(std::integral_constant<int, 0>{});
  else mainloop(std::integral_constant<int, 1>{});
  STAMP(3 + 3 * LAST, 6);
  {
#pragma unroll
    for (int pc = 0; pc < 4; ++pc) {
      if (pc != kq) {
        const int slot = (kq > pc) ? kq - 1 : kq;
        char* dst = smem + ((pc * 2 + ni) * 3 + slot) * 2048 + lane * 16;
        f32x4 g0, g1;
#pragma unroll
        for (int i = 0; i < 4; ++i) { g0[i] = acc[pc >> 1][8 * (pc & 1) + i]; g1[i] = acc[pc >> 1][8 * (pc & 1) + 4 + i]; }
        *(f32x4*)dst = g0;
        *(f32x4*)(dst + 1024) = g1;
      }
    }
  }
  float mine[8];
#pragma unroll
  for (int i = 0; i < 8; ++i) {
    const float a0 = kh_f ? acc[0][8 + i] : acc[0][i];
    const float a1 = kh_f ? acc[1][8 + i] : acc[1][i];
    mine[i] = mi_f ? a1 : a0;
  }
  __syncthreads();
  {
    const char* src = smem + (wave * 3) * 2048 + lane * 16;
#pragma unroll
    for (int sl = 0; sl < 3; ++sl) {
      const f32x4 o0 = *(const f32x4*)(src + sl * 2048);
      const f32x4 o1 = *(const f32x4*)(src + sl * 2048 + 1024);
#pragma unroll
      for (int i = 0; i < 4; ++i) { mine[i] += o0[i]; mine[4 + i] += o1[i]; }
    }
  }
  float xo[8];
  float s1 = 0.f, s2 = 0.f;
#pragma unroll
  for (int i = 0; i < 8; ++i) {
    const float v = rbf(__uint_as_float(((unsigned)xr[i]) << 16) + rbf(mine[i]));
    xo[i] = v;
    s1 += v; s2 += v * v;
  }
  if (LAST) {
#pragma unroll
    for (int q = 0; q < 2; ++q)
      *(u32x2*)(xout + (size_t)tok * D_ + dbase + 8 * q) =
          u32x2{pk_bf16(xo[4 * q], xo[4 * q + 1]), pk_bf16(xo[4 * q + 2], xo[4 * q + 3])};
  } else {
#pragma unroll
    for (int i = 0; i < 8; ++i)
      xout[(size_t)(dbase + (i & 3) + 8 * (i >> 2)) * L_ + tok] = (unsigned short)(__float_as_uint(xo[i]) >> 16);
    s1 += __shfl_xor(s1, 32, 64);
    s2 += __shfl_xor(s2, 32, 64);
    float* sred = (float*)(smem + 3 * 16384);
    sred[wave * 64 + lane] = hh ? s2 : s1;
    __syncthreads();
    if (wave < 2) {
      const int w0 = wave * 4;
      const float t = sred[w0 * 64 + lane] + sred[(w0 + 1) * 64 + lane] + sred[(w0 + 2) * 64 + lane] + sred[(w0 + 3) * 64 + lane];
      atomicAdd(stats + 2 * (mt * 64 + wave * 32 + r32) + hh, t);
    }
  }
  STAMP(3 + 3 * LAST, 15);
}

extern "C" void kernel_launch(void* const* d_in, const int* in_sizes, int n_in,
                              void* d_out, int out_size, void* d_ws, size_t ws_size,
                              hipStream_t stream) {
  const unsigned short* x0 = (const unsigned short*)d_in[0];
  const unsigned short* logA = (const unsigned short*)d_in[1];
  const unsigned short* Aim = (const unsigned short*)d_in[2];
  const unsigned short* Cre = (const unsigned short*)d_in[3];
  const unsigned short* Cim = (const unsigned short*)d_in[4];
  const unsigned short* logdt = (const unsigned short*)d_in[5];
  const unsigned short* dskip = (const unsigned short*)d_in[6];
  const unsigned short* w1 = (const unsigned short*)d_in[8];
  const unsigned short* w2 = (const unsigned short*)d_in[9];
  char* ws = (char*)d_ws;
  (void)hipFuncSetAttribute((const void*)conv_kernel<0>, hipFuncAttributeMaxDynamicSharedMemorySize, CV_LDS);
  (void)hipFuncSetAttribute((const void*)conv_kernel<1>, hipFuncAttributeMaxDynamicSharedMemorySize, CV_LDS);
  (void)hipFuncSetAttribute((const void*)gemm1_kernel, hipFuncAttributeMaxDynamicSharedMemorySize, G1_LDS);
  (void)hipFuncSetAttribute((const void*)gemm2_kernel<0>, hipFuncAttributeMaxDynamicSharedMemorySize, G2_LDS);
  (void)hipFuncSetAttribute((const void*)gemm2_kernel<1>, hipFuncAttributeMaxDynamicSharedMemorySize, G2_LDS);

  unsigned short* xt0 = (unsigned short*)(ws + WS_XT0);
  unsigned short* xt1 = (unsigned short*)(ws + WS_XT1);
  unsigned short* xt2 = (unsigned short*)(ws + WS_XT2);
  unsigned short* xt3 = (unsigned short*)(ws + WS_XT3);
  const float* kinv = (const float*)(ws + WS_KINV);
  const float* w1s = (const float*)(ws + WS_W1S);

  prep_kernel<<<PREP_BLOCKS, 256, 0, stream>>>(x0, logA, Aim, Cre, Cim, logdt, w1, w2, ws);
  conv_kernel<0><<<256, 512, CV_LDS, stream>>>(xt0, (const f32x2*)(ws + WS_ST0), ws + WS_G, kinv, dskip, xt1, w1, w2, ws);
  gemm1_kernel<<<256, 512, G1_LDS, stream>>>(xt1, ws + WS_W1T, w1s, ws + WS_H, (unsigned*)(ws + WS_SW));
  gemm2_kernel<0><<<256, 512, G2_LDS, stream>>>(ws + WS_H, ws + WS_W2T, xt1, xt2, (float*)(ws + WS_ST1));
  conv_kernel<1><<<256, 512, CV_LDS, stream>>>(xt2, (const f32x2*)(ws + WS_ST1), ws + WS_G, kinv, dskip, xt3, w1, w2, ws);
  gemm1_kernel<<<256, 512, G1_LDS, stream>>>(xt3, ws + WS_W1T, w1s, ws + WS_H, (unsigned*)(ws + WS_SW) + 2048);
  gemm2_kernel<1><<<256, 512, G2_LDS, stream>>>(ws + WS_H, ws + WS_W2T, xt3, (unsigned short*)d_out, nullptr);
}
```

```cpp
#include <hip/hip_runtime.h>
#include <hip/hip_bf16.h>
#include <stdint.h>

typedef __attribute__((ext_vector_type(8))) __bf16 bf16x8;
typedef __attribute__((ext_vector_type(2))) __bf16 bf16x2;
typedef __attribute__((ext_vector_type(4))) short s16x4;
typedef __attribute__((ext_vector_type(2))) short s16x2;
typedef __attribute__((ext_vector_type(8))) short s16x8;
typedef __attribute__((ext_vector_type(8))) _Float16 f16x8;
typedef __attribute__((ext_vector_type(2))) _Float16 f16x2;
typedef __attribute__((ext_vector_type(2))) float f32x2;
typedef __attribute__((ext_vector_type(4))) float f32x4;
typedef __attribute__((ext_vector_type(16))) float f32x16;
typedef __attribute__((ext_vector_type(4))) unsigned u32x4;
typedef __attribute__((ext_vector_type(2))) unsigned u32x2;
#define LAS __attribute__((address_space(3)))
#define DI __device__ __forceinline__

constexpr int L_ = 2048, D_ = 512, N_ = 128, F_ = 1792;

constexpr size_t MiB = 1024 * 1024;
constexpr size_t WS_G    = 0;
constexpr size_t WS_XT0  = 2 * MiB;
constexpr size_t WS_XT1  = 4 * MiB;
constexpr size_t WS_XT2  = 6 * MiB;
constexpr size_t WS_XT3  = 8 * MiB;
constexpr size_t WS_W1T  = 10 * MiB;
constexpr size_t WS_W2T  = 12 * MiB;
constexpr size_t WS_H    = 14 * MiB;
constexpr size_t WS_H2   = 24 * MiB;
constexpr size_t WS_KINV = 22 * MiB;
constexpr size_t WS_W1S  = WS_KINV + 4096;
constexpr size_t WS_Z0   = 23 * MiB;
constexpr size_t WS_ST1  = WS_Z0;
constexpr size_t WS_SW   = WS_ST1 + 16384;
#define FLS 32
#define CREP 8
#define CFS (2 * CREP)
constexpr size_t WS_FL   = WS_SW + 16384;
constexpr size_t WS_CF   = WS_FL + 2048 * FLS;
constexpr size_t WS_ZBYTES = 16384 + 16384 + 2048 * FLS + 2048 * CFS;
constexpr size_t WS_SW0  = WS_Z0 + 512 * 1024;

#define STAMP(kid, i)
#define STAMPT(kid, i, T)
DI float bf_lo(unsigned u) { return __uint_as_float(u << 16); }
DI float bf_hi(unsigned u) { return __uint_as_float(u & 0xffff0000u); }
DI unsigned pk_bf16(float a, float b) {
  f32x2 v = {a, b};
  return __builtin_bit_cast(unsigned, __builtin_convertvector(v, bf16x2));
}
DI unsigned pk_f16(float a, float b) {
  f32x2 v = {a, b};
  return __builtin_bit_cast(unsigned, __builtin_convertvector(v, f16x2));
}
DI float rbf(float a) { return bf_lo(pk_bf16(a, 0.f)); }
DI float ld_bf(const unsigned short* p) { return __uint_as_float(((unsigned)*p) << 16); }
DI unsigned perm_lo(unsigned hi, unsigned lo) { return __builtin_amdgcn_perm(hi, lo, 0x05040100u); }
DI unsigned perm_hi(unsigned hi, unsigned lo) { return __builtin_amdgcn_perm(hi, lo, 0x07060302u); }
template <int CTRL> DI float dpp_add(float v) {
  return v + __uint_as_float(__builtin_amdgcn_update_dpp(0u, __float_as_uint(v), CTRL, 0xf, 0xf, true));
}
DI float wave_sum(float v) {
  v = dpp_add<0xB1>(v);
  v = dpp_add<0x4E>(v);
  v = dpp_add<0x141>(v);
  v = dpp_add<0x140>(v);
  const unsigned u = __float_as_uint(v);
  return (__uint_as_float(__builtin_amdgcn_readlane(u, 0)) + __uint_as_float(__builtin_amdgcn_readlane(u, 16))) +
         (__uint_as_float(__builtin_amdgcn_readlane(u, 32)) + __uint_as_float(__builtin_amdgcn_readlane(u, 48)));
}
#define ST_FLAGS ""
DI void st16(void* p, u32x4 v) { asm volatile("global_store_dwordx4 %0, %1, off " ST_FLAGS "\n\ts_nop 1" ::"v"(p), "v"(v) : "memory"); }
DI void st8(void* p, u32x2 v) { asm volatile("global_store_dwordx2 %0, %1, off " ST_FLAGS "\n\ts_nop 1" ::"v"(p), "v"(v) : "memory"); }
DI void st2(void* p, unsigned v) { asm volatile("global_store_short %0, %1, off " ST_FLAGS "\n\ts_nop 1" ::"v"(p), "v"(v) : "memory"); }
template <int N> DI void wait_vm() { asm volatile("s_waitcnt vmcnt(%0)" ::"n"(N) : "memory"); }
template <int OFF> DI void gload16(u32x4& dst, const char* p) {
  asm volatile("global_load_dwordx4 %0, %1, off offset:%2" : "=v"(dst) : "v"(p), "n"(OFF));
}
template <int N> DI void wait_vm4(u32x4& a, u32x4& b, u32x4& c, u32x4& d) {
  asm volatile("s_waitcnt vmcnt(%4)" : "+v"(a), "+v"(b), "+v"(c), "+v"(d) : "n"(N) : "memory");
}
template <int OFF> DI void tr_read(u32x2& d, unsigned addr) {
  asm volatile("ds_read_b64_tr_b16 %0, %1 offset:%2" : "=v"(d) : "v"(addr), "n"(OFF));
}
template <int N> DI void wait_lgkm8(u32x2* b) {
  asm volatile("s_waitcnt lgkmcnt(%8)"
               : "+v"(b[0]), "+v"(b[1]), "+v"(b[2]), "+v"(b[3]), "+v"(b[4]), "+v"(b[5]), "+v"(b[6]), "+v"(b[7])
               : "n"(N) : "memory");
}
template <int N> DI void wait_vm_lgkm0() { asm volatile("s_waitcnt vmcnt(%0) lgkmcnt(0)" ::"n"(N) : "memory"); }
template <int I, int E, class F> DI void static_for(F&& f) {
  if constexpr (I < E) { f(std::integral_constant<int, I>{}); static_for<I + 1, E>(f); }
}

constexpr int TROW = 528;

__host__ __device__ constexpr int tab_w(int idx) {
  return idx < 16 ? idx : (idx < 40 ? ((8 + ((idx - 16) & 7)) << (1 + ((idx - 16) >> 3))) : 128);
}
__host__ __device__ constexpr int ilog2c(int w) { return w <= 1 ? 0 : 1 + ilog2c(w >> 1); }
__host__ __device__ constexpr int tab_idx(int w) {
  return w < 16 ? w : (w == 128 ? 40 : 16 + 8 * (ilog2c(w) - 4) + ((w >> (ilog2c(w) - 3)) - 8));
}
DI int tab_idx_rt(int w) {
  if (w < 16) return w;
  int lg = 31 - __clz(w);
  return 16 + 8 * (lg - 4) + ((w >> (lg - 3)) - 8);
}

DI void retile_load(int u, const unsigned short* __restrict__ w1, const unsigned short* __restrict__ w2, u32x2* r, size_t& dst) {
  if (u < 28672) {
    const int fg = u % 448, o = u / 448;
    const int f = 4 * fg;
#pragma unroll
    for (int j = 0; j < 8; ++j) r[j] = *(const u32x2*)(w1 + (size_t)(8 * o + j) * F_ + f);
    dst = WS_W1T + ((size_t)(((f >> 5) * 32 + (o >> 1)) * 64 + (o & 1) * 32 + (f & 31))) * 16;
  } else {
    const int v = u - 28672;
    const int dg = v & 127, o = v >> 7;
    const int dd = 4 * dg, ks = o >> 1, hh = o & 1;
#pragma unroll
    for (int j = 0; j < 8; ++j)
      r[j] = *(const u32x2*)(w2 + (size_t)(16 * ks + 8 * (j >> 2) + 4 * hh + (j & 3)) * D_ + dd);
    dst = WS_W2T + ((size_t)(((dd >> 5) * 112 + ks) * 64 + hh * 32 + (dd & 31))) * 16;
  }
}
DI void retile_store(const u32x2* r, char* ws, size_t dst) {
#pragma unroll
  for (int c = 0; c < 2; ++c) {
    u32x4 lo = {perm_lo(r[1][c], r[0][c]), perm_lo(r[3][c], r[2][c]), perm_lo(r[5][c], r[4][c]), perm_lo(r[7][c], r[6][c])};
    u32x4 hi = {perm_hi(r[1][c], r[0][c]), perm_hi(r[3][c], r[2][c]), perm_hi(r[5][c], r[4][c]), perm_hi(r[7][c], r[6][c])};
    st16(ws + dst + (2 * c) * 16, lo);
    st16(ws + dst + (2 * c + 1) * 16, hi);
  }
}

constexpr int PB_KGEN = 512, PB_XT = 64, PB_ST = 64, PB_RT = 112, PB_WS = 16;
constexpr int PREP_BLOCKS = PB_KGEN + PB_XT + PB_ST + PB_RT + PB_WS;

__global__ __launch_bounds__(256) void prep_kernel(
    const unsigned short* __restrict__ x0, const unsigned short* __restrict__ logA,
    const unsigned short* __restrict__ Aim, const unsigned short* __restrict__ Cre,
    const unsigned short* __restrict__ Cim, const unsigned short* __restrict__ logdt,
    const unsigned short* __restrict__ w1, const unsigned short* __restrict__ w2, char* __restrict__ ws) {
  __shared__ __attribute__((aligned(16))) char smem[82 * TROW + 1536];
  float* red = (float*)smem;
  const int tid = threadIdx.x, lane = tid & 63;
  const int wave = __builtin_amdgcn_readfirstlane(tid >> 6);
  int b = blockIdx.x;
  STAMP(0, 0);
  if (b < PB_KGEN) {
    const int d = ((b & 7) * 32 + ((b >> 3) & 31)) * 2 + (b >> 8);
    const int isH = wave >> 1;
    const int n = (wave & 1) * 64 + lane;
    const float pdt = ld_bf(logdt + d), pla = ld_bf(logA + d * N_ + n), pai = ld_bf(Aim + d * N_ + n);
    const float p0r = ld_bf(Cre + d * N_ + lane), p0i = ld_bf(Cim + d * N_ + lane);
    const float p1r = ld_bf(Cre + d * N_ + 64 + lane), p1i = ld_bf(Cim + d * N_ + 64 + lane);
    const float dt = rbf(expf(pdt));
    const float ar = -rbf(expf(pla)) * dt;
    const float ai = pai * (isH ? -dt : dt);
    const float c0r = p0r * dt, c0i = p0i * dt, c1r = p1r * dt, c1i = p1i * dt;
    const float S = wave_sum(2.f * (fabsf(c0r) + fabsf(c0i) + fabsf(c1r) + fabsf(c1i)));
    int ex = 0;
    (void)frexpf(S, &ex);
    if (!(S > 0.f) || !(S < 3.0e38f)) ex = 15;
    ex = max(-100, min(ex, 120));
    const float scale = ldexpf(1.f, 15 - ex);
    const float cr = (wave & 1) ? c1r : c0r, ci = (wave & 1) ? c1i : c0i;
    const float base = isH ? 16.f : 1.f;
    const float er = ar * 1.44269504088896340736f * base;
    const float IH = 0.15915494309189533577f;
    const float IL = (float)(0.15915494309189533577 - (double)0.15915494309189533577f);
    float fh = ai * IH;
    float fl = fmaf(ai, IH, -fh) + ai * IL;
    fh *= base; fl *= base;
    STAMP(0, 1);
    f32x2 Er2[8], Ei2[8];
#pragma unroll
    for (int k = 0; k < 8; ++k) {
      const float pw = (float)(1 << k);
      const float mag = __builtin_amdgcn_exp2f(er * pw);
      const float ph = __builtin_amdgcn_fractf(fh * pw) + fl * pw;
      const float re = mag * __builtin_amdgcn_cosf(ph), im = mag * __builtin_amdgcn_sinf(ph);
      Er2[k] = f32x2{re, re};
      Ei2[k] = f32x2{-im, im};
    }
    f32x2 tv[41];
    tv[0] = isH ? f32x2{2.f * scale * cr, -2.f * scale * ci} : f32x2{1.f, 0.f};
    char* trow = smem + isH * (41 * TROW) + n * 4;
    *(unsigned*)trow = __builtin_bit_cast(unsigned, __builtin_convertvector(tv[0], f16x2));
    static_for<1, 41>([&](auto I) {
      constexpr int idx = I;
      constexpr int w = tab_w(idx);
      constexpr int lg = ilog2c(w);
      constexpr int ridx = tab_idx(w - (1 << lg));
      const f32x2 sw = __builtin_shufflevector(tv[ridx], tv[ridx], 1, 0);
      tv[idx] = __builtin_elementwise_fma(Ei2[lg], sw, Er2[lg] * tv[ridx]);
      *(unsigned*)(trow + idx * TROW) = __builtin_bit_cast(unsigned, __builtin_convertvector(tv[idx], f16x2));
    });
    int cix[8];
#pragma unroll
    for (int e = 0; e < 8; ++e) {
      const int l = 2047 - (tid * 8 + e);
      const int sh = max((31 - __clz(l | 1)) - 7, 0);
      const int q = (l + ((1 << sh) >> 1) - (sh > 0 ? 1 : 0) + ((l >> sh) & (sh > 0 ? 1 : 0))) >> sh;
      cix[e] = (sh > 0) ? (128 * sh + q) : l;
    }
    STAMP(0, 2);
    __syncthreads();
    _Float16* kc = (_Float16*)(smem + 82 * TROW);
    {
      const int s = wave, i16 = lane & 15, g = lane >> 4;
      const int hw = (s == 0) ? i16 : (i16 < 8 ? ((8 + i16) << s) : 128);
      const char* arow = smem + 41 * TROW + tab_idx_rt(hw) * TROW + g * 16;
      const char* brow = smem + tab_idx_rt(i16 << s) * TROW + g * 16;
      f16x8 af[8], bf[8];
#pragma unroll
      for (int ks = 0; ks < 8; ++ks) { af[ks] = *(const f16x8*)(arow + ks * 64); bf[ks] = *(const f16x8*)(brow + ks * 64); }
      f32x4 k0 = {0.f, 0.f, 0.f, 0.f}, k1 = {0.f, 0.f, 0.f, 0.f};
#pragma unroll
      for (int ks = 0; ks < 8; ks += 2) {
        k0 = __builtin_amdgcn_mfma_f32_16x16x32_f16(af[ks], bf[ks], k0, 0, 0, 0);
        k1 = __builtin_amdgcn_mfma_f32_16x16x32_f16(af[ks + 1], bf[ks + 1], k1, 0, 0, 0);
      }
#pragma unroll
      for (int r = 0; r < 4; ++r) {
        const int i = 4 * g + r;
        const int cidx = (s == 0) ? (16 * i + i16) : (256 + 128 * (s - 1) + 16 * i + i16);
        const bool ok = (s == 0) || (i < 8) || (s == 3 && i == 8 && i16 == 0);
        if (ok) kc[cidx] = (_Float16)(k0[r] + k1[r]);
      }
    }
    __syncthreads();
    STAMP(0, 3);
    unsigned short o[8];
#pragma unroll
    for (int e = 0; e < 8; ++e) o[e] = ((const unsigned short*)kc)[cix[e]];
    *(u32x4*)(ws + WS_G + (size_t)d * 4096 + tid * 16) =
        u32x4{(unsigned)o[0] | ((unsigned)o[1] << 16), (unsigned)o[2] | ((unsigned)o[3] << 16),
              (unsigned)o[4] | ((unsigned)o[5] << 16), (unsigned)o[6] | ((unsigned)o[7] << 16)};
    if (tid == 0) ((float*)(ws + WS_KINV))[d] = ldexpf(1.f, ex - 15);
    STAMP(0, 15);
    return;
  }
  b -= PB_KGEN;
  if (b < PB_XT) {
    const int tile = b * 4 + wave, tt = tile >> 3, ct = tile & 7;
    const int tg = lane & 7, cg = lane >> 3;
    const unsigned short* src = x0 + (size_t)(tt * 64 + tg * 8) * D_ + ct * 64 + cg * 8;
    u32x4 r[8];
#pragma unroll
    for (int j = 0; j < 8; ++j) r[j] = *(const u32x4*)(src + (size_t)j * D_);
    unsigned short* dst = (unsigned short*)(ws + WS_XT0) + (size_t)(ct * 64 + cg * 8) * L_ + tt * 64 + tg * 8;
#pragma unroll
    for (int c = 0; c < 4; ++c) {
      u32x4 lo = {perm_lo(r[1][c], r[0][c]), perm_lo(r[3][c], r[2][c]), perm_lo(r[5][c], r[4][c]), perm_lo(r[7][c], r[6][c])};
      u32x4 hi = {perm_hi(r[1][c], r[0][c]), perm_hi(r[3][c], r[2][c]), perm_hi(r[5][c], r[4][c]), perm_hi(r[7][c], r[6][c])};
      *(u32x4*)(dst + (size_t)(2 * c) * L_) = lo;
      *(u32x4*)(dst + (size_t)(2 * c + 1) * L_) = hi;
    }
    STAMP(0, 15);
    return;
  }
  b -= PB_XT;
  if (b < PB_ST) {
    const int t0 = b * 32 + wave * 8;
    u32x4 r[8];
#pragma unroll
    for (int j = 0; j < 8; ++j) r[j] = *(const u32x4*)(x0 + (size_t)(t0 + j) * D_ + lane * 8);
    for (int i = b * 256 + tid; i < (int)(WS_ZBYTES / 16); i += PB_ST * 256) ((u32x4*)(ws + WS_Z0))[i] = u32x4{0u, 0u, 0u, 0u};
    asm volatile("" : "+v"(r[0]), "+v"(r[1]), "+v"(r[2]), "+v"(r[3]), "+v"(r[4]), "+v"(r[5]), "+v"(r[6]), "+v"(r[7]));
    unsigned word = 0u;
#pragma unroll
    for (int j = 0; j < 8; ++j) {
      float s = 0.f;
#pragma unroll
      for (int c = 0; c < 4; ++c) s += bf_lo(r[j][c]) + bf_hi(r[j][c]);
      const float mean = wave_sum(s) * (1.f / D_);
      float q = 0.f;
#pragma unroll
      for (int c = 0; c < 4; ++c) {
        const float a = bf_lo(r[j][c]) - mean, bb = bf_hi(r[j][c]) - mean;
        q += a * a + bb * bb;
      }
      const float var = wave_sum(q) * (1.f / D_);
      const unsigned wv = pk_bf16(mean, sqrtf(rbf(rbf(var) + 1e-5f)));
      if (lane == j) word = wv;
    }
    if (lane < 8) ((unsigned*)(ws + WS_SW0))[t0 + lane] = word;
    STAMP(0, 15);
    return;
  }
  b -= PB_ST;
  if (b < PB_RT) {
    u32x2 r0[8], r1[8];
    size_t d0_, d1_;
    retile_load(b * 512 + tid, w1, w2, r0, d0_);
    retile_load(b * 512 + 256 + tid, w1, w2, r1, d1_);
    retile_store(r0, ws, d0_);
    retile_store(r1, ws, d1_);
    STAMP(0, 15);
    return;
  }
  b -= PB_RT;
  {
    const int cg = tid % 14, rg = tid / 14;
    const int f0 = b * 112 + cg * 8;
    float s[8] = {0.f, 0.f, 0.f, 0.f, 0.f, 0.f, 0.f, 0.f};
    if (tid < 224) {
#pragma unroll
      for (int h = 0; h < 2; ++h) {
        u32x4 v[16];
#pragma unroll
        for (int j = 0; j < 16; ++j) v[j] = *(const u32x4*)(w1 + (size_t)(rg * 32 + h * 16 + j) * F_ + f0);
#pragma unroll
        for (int j = 0; j < 16; ++j)
#pragma unroll
          for (int c = 0; c < 4; ++c) { s[2 * c] += bf_lo(v[j][c]); s[2 * c + 1] += bf_hi(v[j][c]); }
      }
#pragma unroll
      for (int e = 0; e < 8; ++e) red[rg * 112 + cg * 8 + e] = s[e];
    }
    __syncthreads();
    if (tid < 112) {
      float t = 0.f;
#pragma unroll
      for (int r = 0; r < 16; ++r) t += red[r * 112 + tid];
      ((float*)(ws + WS_W1S))[b * 112 + tid] = t;
    }
    STAMP(0, 15);
  }
}

constexpr int CV_CS = 260 * 16;
constexpr int CV_KCH = 8 * CV_CS;
constexpr int CV_U0 = 2 * CV_KCH;
constexpr int CV_UCH = (256 + 2048) * 2;
constexpr int CV_XS = CV_U0 + 2 * CV_UCH;
constexpr int CV_RED = CV_XS + 2 * 8192;
constexpr int G1_R = 9;
constexpr int G2_R = 9;
constexpr int LAYER_LDS = 9 * 16384 + 1024;
static_assert(CV_RED + 16384 <= 9 * 16384, "conv LDS");
#define G1_W0 5
#define G2_W0 6
#define CV_PF1 16
#define CV_PF2 22
#define W1PRE 5
#define POLL_SLEEP 2
static_assert(G1_W0 <= G1_R - 2 && W1PRE <= G1_R, "ring safety");
__host__ __device__ constexpr int g2_iss(int i) {
  int v = G2_W0 + i;
  return v < 14 ? v : 14;
}
__host__ __device__ constexpr int g1_young(int c) {
  int n = 0;
  for (int j = c + 1; j < c + G1_W0 && j < 16; ++j) n += (j >= W1PRE) ? 4 : 2;
  return n;
}
template <int N> DI void wait_lgkm2(u32x2& a, u32x2& b) {
  asm volatile("s_waitcnt lgkmcnt(%2)" : "+v"(a), "+v"(b) : "n"(N) : "memory");
}
template <int N> DI void wait_lgkm3(u32x2& a, u32x2& b, u32x4& c) {
  asm volatile("s_waitcnt lgkmcnt(%3)" : "+v"(a), "+v"(b), "+v"(c) : "n"(N) : "memory");
}
template <int OFF> DI void lds_read16(u32x4& d, unsigned addr) {
  asm volatile("ds_read_b128 %0, %1 offset:%2" : "=v"(d) : "v"(addr), "n"(OFF));
}
DI void pin4(bf16x8* f) { asm volatile("" : "+v"(f[0]), "+v"(f[1]), "+v"(f[2]), "+v"(f[3])); }
DI void pin7(u32x4* f) { asm volatile("" : "+v"(f[0]), "+v"(f[1]), "+v"(f[2]), "+v"(f[3]), "+v"(f[4]), "+v"(f[5]), "+v"(f[6])); }
DI void st16_sc1(void* p, u32x4 v) { asm volatile("global_store_dwordx4 %0, %1, off sc1\n\ts_nop 1" ::"v"(p), "v"(v) : "memory"); }
DI void st8_sc1(void* p, u32x2 v) { asm volatile("global_store_dwordx2 %0, %1, off sc1\n\ts_nop 1" ::"v"(p), "v"(v) : "memory"); }
DI u32x4 ld16_sc1_wait(const void* p) {
  u32x4 v;
  asm volatile("global_load_dwordx4 %0, %1, off sc1\n\ts_waitcnt vmcnt(0)" : "=&v"(v) : "v"(p) : "memory");
  return v;
}

struct LayerArgs {
  const unsigned short* xin;
  const unsigned short* dskip;
  unsigned short* xmid;
  char* hbuf;
  unsigned short* xout;
  char* ws;
};

template <int LAYER>
__global__ __launch_bounds__(512, 2) void layer_kernel(LayerArgs p) {
  extern __shared__ __attribute__((aligned(16))) char smem[];
  const int tid = threadIdx.x, lane = tid & 63;
  const int wave8 = __builtin_amdgcn_readfirstlane(tid >> 6);
  const int wave = wave8 & 3;
  const int bx = blockIdx.x & 7, bi = blockIdx.x >> 3;
  const int r32 = lane & 31, hh = lane >> 5;
  char* ws = p.ws;
  unsigned short* xt = p.xmid;
  char* hbuf = p.hbuf;
  unsigned* sw = (unsigned*)(ws + WS_SW) + LAYER * 2048;
  unsigned* flags = (unsigned*)(ws + WS_FL) + LAYER * 256 * FLS;
  unsigned* cflags = (unsigned*)(ws + WS_CF) + LAYER * (CREP * 16 * 32);
  const char* w1t = ws + WS_W1T;
  const char* w2t = ws + WS_W2T;
  constexpr int KID = 1 + 3 * LAYER;
  const int mt2 = bx * 4 + (bi & 3), nt2 = bi >> 2;
  const int ni = wave8 & 1, kq = wave8 >> 1;
  const int mi_f = kq >> 1, kh_f = kq & 1;
  const int tok = mt2 * 64 + mi_f * 32 + r32;
  const int dbase = nt2 * 64 + ni * 32 + 4 * hh + 16 * kh_f;
  u32x4 wr[28];
  unsigned short xr[8];
  const char* w2src = w2t + (size_t)(nt2 * 2 + ni) * 112 * 1024 + kq * 2048 + lane * 16;
  const unsigned short* xrsrc = xt + ((size_t)(tok >> 7) * D_ + dbase) * 128 + (tok & 127);
  auto pf_w2 = [&](auto J) {
    constexpr int j = J;
    wr[j] = *(const u32x4*)(w2src + (j >> 1) * 8192 + (j & 1) * 1024);
  };
  auto pf_xr = [&](auto J) {
    constexpr int j = J;
    xr[j] = __hip_atomic_load(xrsrc + ((j & 3) + 8 * (j >> 2)) * 128, __ATOMIC_RELAXED, __HIP_MEMORY_SCOPE_AGENT);
  };
  const int mt = (bx >> 1) * 4 + (bi & 3), nt = (bx & 1) * 7 + (bi >> 2);
  const int ft = nt * 4 + wave;
  STAMP(KID, 0);
  {
    const int d0 = 2 * (bx * 32 + bi);
    const int gz = tid & 255, gc = tid >> 8;
    const int t4 = tid * 4;
    const int ch = wave8 >> 2, half = wave8 & 1, qs = (wave8 >> 1) & 1;
    float xf[2][4], m[4], sd[4];
    const unsigned dsk2 = *(const unsigned*)(p.dskip + d0);
    const char* gp = ws + WS_G + (size_t)(d0 + gc) * 4096 + gz * 16;
    const u32x4 ga = *(const u32x4*)gp;
    const u32x4 gb = *(const u32x4*)(gp + 16);
    f32x4 st0, st1;
    u32x4 swv0;
    if constexpr (LAYER == 0) swv0 = *(const u32x4*)(ws + WS_SW0 + t4 * 4);
    else { st0 = *(const f32x4*)(ws + WS_ST1 + t4 * 8); st1 = *(const f32x4*)(ws + WS_ST1 + t4 * 8 + 16); }
    const u32x2 xv0 = *(const u32x2*)(p.xin + (size_t)d0 * L_ + t4);
    const u32x2 xv1 = *(const u32x2*)(p.xin + (size_t)(d0 + 1) * L_ + t4);
    const float kscale = ((const float*)(ws + WS_KINV))[d0 + ch];
    if (tid < 16) *(u32x4*)(smem + (tid >> 3) * CV_KCH + (tid & 7) * CV_CS + 256 * 16) = u32x4{0u, 0u, 0u, 0u};
    if (tid >= 64 && tid < 128) {
      const int q = tid - 64;
      *(u32x4*)(smem + CV_U0 + (q >> 5) * CV_UCH + (q & 31) * 16) = u32x4{0u, 0u, 0u, 0u};
    }
    if (tid == 128) *(unsigned*)(smem + 9 * 16384) = 0u;
    {
      const unsigned msk = (gz < 255) ? 0xffffffffu : 0u;
      unsigned w[8] = {ga[0], ga[1], ga[2], ga[3], gb[0] & msk, gb[1] & msk, gb[2] & msk, gb[3] & msk};
      unsigned o[7];
#pragma unroll
      for (int k = 0; k < 7; ++k) o[k] = __builtin_amdgcn_alignbit(w[k + 1], w[k], 16);
      char* dst = smem + gc * CV_KCH + gz * 16;
#pragma unroll
      for (int mm = 0; mm < 4; ++mm) {
        *(u32x4*)(dst + (2 * mm) * CV_CS) = u32x4{w[mm], w[mm + 1], w[mm + 2], w[mm + 3]};
        *(u32x4*)(dst + (2 * mm + 1) * CV_CS) = u32x4{o[mm], o[mm + 1], o[mm + 2], o[mm + 3]};
      }
    }
#pragma unroll
    for (int r = 0; r < 4; ++r) {
      if constexpr (LAYER == 0) {
        const unsigned wv = swv0[r];
        m[r] = bf_lo(wv); sd[r] = bf_hi(wv);
      } else {
        const float a = (r < 2) ? st0[(r & 1) * 2] : st1[(r & 1) * 2];
        const float bq = (r < 2) ? st0[(r & 1) * 2 + 1] : st1[(r & 1) * 2 + 1];
        const float mean = a * (1.f / D_), var = fmaxf(bq * (1.f / D_) - mean * mean, 0.f);
        m[r] = rbf(mean);
        sd[r] = rbf(__builtin_amdgcn_sqrtf(rbf(rbf(var) + 1e-5f)));
      }
      const unsigned w0 = xv0[r >> 1], w1_ = xv1[r >> 1];
      xf[0][r] = (r & 1) ? bf_hi(w0) : bf_lo(w0);
      xf[1][r] = (r & 1) ? bf_hi(w1_) : bf_lo(w1_);
    }
    {
      float rs[4];
#pragma unroll
      for (int r = 0; r < 4; ++r) rs[r] = __builtin_amdgcn_rcpf(sd[r]);
#pragma unroll
      for (int c = 0; c < 2; ++c) {
        const float dsk = c ? bf_hi(dsk2) : bf_lo(dsk2);
        float uu[4];
        unsigned xs[4];
#pragma unroll
        for (int r = 0; r < 4; ++r) {
          const float x = xf[c][r];
          const float dd = rbf(x - m[r]);
          float q = dd * rs[r];
          q = fmaf(fmaf(-q, sd[r], dd), rs[r], q);
          uu[r] = rbf(q);
          xs[r] = pk_bf16(x, uu[r] * dsk);
        }
        *(u32x2*)(smem + CV_U0 + c * CV_UCH + (256 + t4) * 2) = u32x2{pk_f16(uu[0], uu[1]), pk_f16(uu[2], uu[3])};
        *(u32x4*)(smem + CV_XS + c * 8192 + t4 * 4) = u32x4{xs[0], xs[1], xs[2], xs[3]};
      }
    }
    __syncthreads();
    STAMP(KID, 1);
    const int j16 = lane & 15, g = lane >> 4;
    const int cc = (7 - j16) & 7;
    const int zl = (2031 - j16 - cc) / 8 + g;
    const char* abase = smem + ch * CV_KCH + cc * CV_CS + (zl - 32 * 7 - 4 * (4 * half + 3)) * 16;
    const char* bbase = smem + CV_U0 + ch * CV_UCH + 480 + 32 * j16 + 16 * g - 64 * (4 * half + 3);
    f32x4 acc[4];
#pragma unroll
    for (int q = 0; q < 4; ++q) acc[q] = f32x4{0.f, 0.f, 0.f, 0.f};
    auto body = [&](auto QS) {
      constexpr int S = QS;
      constexpr int Q[4] = {S ? 6 : 7, S ? 5 : 4, S ? 2 : 3, S ? 1 : 0};
      constexpr int NF = S ? 7 : 8;
      f16x8 A[2][NF], B[2][NF];
      auto ld = [&](auto IT) {
        constexpr int it = IT;
#pragma unroll
        for (int k = 0; k < NF; ++k) {
          A[it & 1][k] = *(const f16x8*)(abase + (7 - k) * 512 + (3 - it) * 64);
          B[it & 1][NF - 1 - k] = *(const f16x8*)(bbase + (NF - 1 - k) * 512 + (3 - it) * 64);
        }
      };
      ld(std::integral_constant<int, 0>{});
      __builtin_amdgcn_sched_barrier(0);
      static_for<0, 4>([&](auto IT) {
        constexpr int it = IT;
        if constexpr (it + 1 < 4) ld(std::integral_constant<int, it + 1>{});
        static_for<it * (CV_PF1 / 4), (it + 1) * (CV_PF1 / 4)>([&](auto J) { pf_w2(J); });
#pragma unroll
        for (int b1 = 0; b1 < NF; ++b1)
#pragma unroll
          for (int e = 0; e < 4; ++e)
            if (b1 <= Q[e]) acc[e] = __builtin_amdgcn_mfma_f32_16x16x32_f16(A[it & 1][b1], B[it & 1][Q[e] - b1], acc[e], 0, 0, 0);
#pragma unroll
        for (int k = 0; k < 18; ++k) {
          __builtin_amdgcn_sched_group_barrier(0x8, 1, 0);
          if (it + 1 < 4 && k < 2 * NF) __builtin_amdgcn_sched_group_barrier(0x100, 1, 0);
          if (CV_PF1 > 0 && (k % (16 / (CV_PF1 / 4))) == 1 && k / (16 / (CV_PF1 / 4)) < CV_PF1 / 4)
            __builtin_amdgcn_sched_group_barrier(0x20, 1, 0);
        }
        __builtin_amdgcn_sched_barrier(0);
      });
    };
    if (qs) body(std::integral_constant<int, 1>{}); else body(std::integral_constant<int, 0>{});
    f32x4 keep[2];
    {
      char* wrp = smem + CV_RED + wave8 * 2048 + lane * 16;
#pragma unroll
      for (int e = 0; e < 2; ++e) {
        f32x4 give;
#pragma unroll
        for (int r = 0; r < 4; ++r) {
          const float lo = acc[e][r], hi = acc[2 + e][r];
          give[r] = half ? lo : hi;
          keep[e][r] = half ? hi : lo;
        }
        *(f32x4*)(wrp + e * 1024) = give;
      }
    }
    asm volatile("s_waitcnt lgkmcnt(0)" ::: "memory");
    __builtin_amdgcn_s_barrier();
    STAMP(KID, 2);
    {
      const char* rd = smem + CV_RED + (wave8 ^ 1) * 2048 + lane * 16;
      const int toff = 16 * j16 + 4 * g;
#pragma unroll
      for (int e = 0; e < 2; ++e) {
        const int q = half ? (e ? (qs ? 1 : 0) : (qs ? 2 : 3)) : (e ? (qs ? 5 : 4) : (qs ? 6 : 7));
        const int t = 256 * q + toff;
        const f32x4 o = *(const f32x4*)(rd + e * 1024);
        const u32x4 xs = *(const u32x4*)(smem + CV_XS + ch * 8192 + t * 4);
        const f32x4 y = keep[e] + o;
        float xo[4];
#pragma unroll
        for (int r = 0; r < 4; ++r) {
          const unsigned w = xs[r];
          xo[r] = bf_lo(w) + rbf(rbf(y[r] * kscale) + bf_hi(w));
        }
        st8_sc1(xt + ((size_t)(t >> 7) * D_ + d0 + ch) * 128 + (t & 127), u32x2{pk_bf16(xo[0], xo[1]), pk_bf16(xo[2], xo[3])});
      }
    }
    wait_vm_lgkm0<0>();
    __builtin_amdgcn_s_barrier();
    if (tid < CREP) (void)__hip_atomic_fetch_add(cflags + (tid * 16 + (d0 >> 5)) * 32, 1u, __ATOMIC_RELAXED, __HIP_MEMORY_SCOPE_AGENT);
    STAMP(KID, 3);
  }
  const int grow = lane >> 4, gslot = lane & 15;
  const unsigned short* xsrc = xt + ((size_t)mt * D_ + wave * 8 + grow) * 128 + ((gslot ^ (grow << 2)) * 8);
  const char* wsrc = w1t + (size_t)ft * 32 * 1024 + lane * 16;
  auto issue_w = [&](int c) {
    char* slot = smem + (c % G1_R) * 16384;
#pragma unroll
    for (int i = 0; i < 2; ++i)
      __builtin_amdgcn_global_load_lds((const unsigned*)(wsrc + (size_t)(c * 2 + i) * 1024),
                                       (LAS unsigned*)(slot + 8192 + (wave * 2 + i) * 1024), 16, 0, 0);
  };
  auto issue_x = [&](int c) {
    char* slot = smem + (c % G1_R) * 16384;
#pragma unroll
    for (int i = 0; i < 2; ++i)
      __builtin_amdgcn_global_load_lds((const unsigned*)(xsrc + (size_t)(c * 32 + i * 4) * 128),
                                       (LAS unsigned*)(slot + (wave * 8 + i * 4) * 256), 16, 0, 16  );
  };
  if (bi < 28 && wave8 >= 4) {
    if (wave8 < 6)
      __builtin_amdgcn_global_load_lds((const unsigned*)(ws + WS_W1S) + nt * 128 + wave * 64 + lane,
                                       (LAS unsigned*)(smem + 9 * 16384 + 256 + wave * 256), 4, 0, 0);
#pragma unroll
    for (int c = 0; c < W1PRE; ++c) issue_w(c);
  }
  __builtin_amdgcn_sched_barrier(0);
  static_for<CV_PF1, CV_PF2>([&](auto J) { pf_w2(J); });
  __builtin_amdgcn_sched_barrier(0);
  if (wave8 == 7) {
    const unsigned* cp = cflags + ((blockIdx.x % CREP) * 16 + (lane & 15)) * 32;
    for (int it = 0; it < (1 << 20); ++it) {
      const unsigned f = __hip_atomic_load(cp, __ATOMIC_RELAXED, __HIP_MEMORY_SCOPE_AGENT);
      if (__all(f >= 16u)) break;
      __builtin_amdgcn_s_sleep(POLL_SLEEP);
    }
  }
  __builtin_amdgcn_s_barrier();
  STAMP(KID, 4);
  if (bi >= 28) {
    const int ht = bx * 4 + (bi - 28);
    const int t8 = lane & 7, dg = wave8 * 8 + (lane >> 3);
    const auto rsrc = __builtin_amdgcn_make_buffer_rsrc((void*)xt, 0, L_ * D_ * 2, 0x00020000);
    const int off = (((ht >> 1) * D_ + dg * 8) * 128 + (ht & 1) * 64 + t8 * 8) * 2;
    u32x4 v[8];
#pragma unroll
    for (int j = 0; j < 8; ++j) v[j] = __builtin_amdgcn_raw_buffer_load_b128(rsrc, off + j * 256, 0, 16  );
    static_for<0, 8>([&](auto J) { pf_xr(J); });
    static_for<CV_PF2, 28>([&](auto J) { pf_w2(J); });
    float* red = (float*)smem;
    float sm[8];
#pragma unroll
    for (int e = 0; e < 8; ++e) {
      float a = 0.f;
#pragma unroll
      for (int j = 0; j < 8; ++j) a += (e & 1) ? bf_hi(v[j][e >> 1]) : bf_lo(v[j][e >> 1]);
      sm[e] = a;
    }
#pragma unroll
    for (int o = 8; o <= 32; o <<= 1) {
#pragma unroll
      for (int e = 0; e < 8; ++e) sm[e] += __shfl_xor(sm[e], o, 64);
    }
    if (lane < 8) {
#pragma unroll
      for (int e = 0; e < 8; ++e) red[wave8 * 64 + t8 * 8 + e] = sm[e];
    }
    __syncthreads();
    float mean[8];
#pragma unroll
    for (int e = 0; e < 8; ++e) {
      float a = 0.f;
#pragma unroll
      for (int w = 0; w < 8; ++w) a += red[w * 64 + t8 * 8 + e];
      mean[e] = a * (1.f / D_);
    }
#pragma unroll
    for (int e = 0; e < 8; ++e) {
      float a = 0.f;
#pragma unroll
      for (int j = 0; j < 8; ++j) {
        const float dd = ((e & 1) ? bf_hi(v[j][e >> 1]) : bf_lo(v[j][e >> 1])) - mean[e];
        a = fmaf(dd, dd, a);
      }
      sm[e] = a;
    }
#pragma unroll
    for (int o = 8; o <= 32; o <<= 1) {
#pragma unroll
      for (int e = 0; e < 8; ++e) sm[e] += __shfl_xor(sm[e], o, 64);
    }
    if (lane < 8) {
#pragma unroll
      for (int e = 0; e < 8; ++e) red[512 + wave8 * 64 + t8 * 8 + e] = sm[e];
    }
    __syncthreads();
    if (tid < 64) {
      float a = 0.f, q = 0.f;
#pragma unroll
      for (int w = 0; w < 8; ++w) { a += red[w * 64 + tid]; q += red[512 + w * 64 + tid]; }
      const float mm = a * (1.f / D_), var = q * (1.f / D_);
      const float sdv = sqrtf(rbf(rbf(var) + 1e-5f));
      __hip_atomic_store(sw + ht * 64 + tid, pk_bf16(mm, sdv), __ATOMIC_RELAXED, __HIP_MEMORY_SCOPE_AGENT);
    }
  } else if (wave8 >= 4) {
#pragma unroll
    for (int c = 0; c < G1_W0; ++c) { issue_x(c); if (c >= W1PRE) issue_w(c); }
    static_for<0, 16>([&](auto C) {
      constexpr int c = C;
      constexpr int extra = (c + G1_W0 > 16) ? 8 + (28 - CV_PF2) : 0;
      wait_vm<g1_young(c) + extra>();
      __builtin_amdgcn_s_barrier();
      if constexpr (c + G1_W0 < 16) { issue_x(c + G1_W0); if (c + G1_W0 >= W1PRE) issue_w(c + G1_W0); }
      if constexpr (c + G1_W0 == 16) {
        __builtin_amdgcn_sched_barrier(0);
        static_for<0, 8>([&](auto J) { pf_xr(J); });
        static_for<CV_PF2, 28>([&](auto J) { pf_w2(J); });
        __builtin_amdgcn_sched_barrier(0);
      }
    });
  } else {
    const int blk = (lane >> 4) & 1, q4 = (lane & 15) >> 2, p4 = lane & 3;
    const unsigned sbase = (unsigned)(uintptr_t)(LAS char*)smem;
    unsigned* lcnt = (unsigned*)(smem + 9 * 16384);
    unsigned tb[3][4], ta[3];
#pragma unroll
    for (int ct = 0; ct < 4; ++ct) {
      tb[0][ct] = sbase + (unsigned)((8 * hh + q4) * 256 + (((4 * ct + 2 * blk + (p4 >> 1)) ^ (q4 << 2)) * 16) + 8 * (p4 & 1));
      tb[1][ct] = tb[0][ct] + 65536u; tb[2][ct] = tb[0][ct] + 131072u;
    }
    ta[0] = sbase + 8192 + wave * 2048 + lane * 16; ta[1] = ta[0] + 65536u; ta[2] = ta[0] + 131072u;
    f32x16 acc[4];
#pragma unroll
    for (int ct = 0; ct < 4; ++ct)
#pragma unroll
      for (int i = 0; i < 16; ++i) acc[ct][i] = 0.f;
    constexpr int LA = 6;
    u32x2 bb[8][2];
    u32x4 af[3];
    unsigned swv[4];
    f32x4 ws4[4];
    auto issue_pair = [&](auto NN) {
      constexpr int n = NN, ks = n >> 2, ct = n & 3;
      constexpr int so = ((ks >> 1) % G1_R) * 16384, seg = so >> 16, o = (so & 65535) + (ks & 1) * 4096;
      if constexpr (ct == 0) lds_read16<(so & 65535) + (ks & 1) * 1024>(af[ks % 3], ta[seg]);
      tr_read<o>(bb[n & 7][0], tb[seg][ct]);
      tr_read<o + 1024>(bb[n & 7][1], tb[seg][ct]);
    };
    __builtin_amdgcn_s_barrier();
    STAMP(KID, 5);
    static_for<0, LA>([&](auto NN) { issue_pair(NN); });
    static_for<0, 128>([&](auto NN) {
      constexpr int n = NN, ks = n >> 2, ct = n & 3;
      constexpr int last = (n + LA - 1 < 127) ? (n + LA - 1) : 127;
      constexpr int younger = 2 * (last - n) + ((last >> 2) - (n >> 2));
      if constexpr (ct == 0) wait_lgkm3<younger>(bb[n & 7][0], bb[n & 7][1], af[ks % 3]);
      else wait_lgkm2<younger>(bb[n & 7][0], bb[n & 7][1]);
      const u32x4 bu = {bb[n & 7][0][0], bb[n & 7][0][1], bb[n & 7][1][0], bb[n & 7][1][1]};
      acc[ct] = __builtin_amdgcn_mfma_f32_32x32x16_bf16(__builtin_bit_cast(bf16x8, af[ks % 3]), __builtin_bit_cast(bf16x8, bu), acc[ct], 0, 0, 0);
      __builtin_amdgcn_sched_barrier(0);
      if constexpr (n + LA < 128) {
        if constexpr ((n + LA) % 8 == 0) __builtin_amdgcn_s_barrier();
        issue_pair(std::integral_constant<int, n + LA>{});
      }
      if constexpr (n >= 72 && (n & 3) == 0 && CV_PF2 + (n - 72) / 4 < 28) pf_w2(std::integral_constant<int, CV_PF2 + (n - 72) / 4>{});
      if constexpr (n >= 120 && n < 124)
        swv[n - 120] = __hip_atomic_load(sw + mt * 128 + (n - 120) * 32 + r32, __ATOMIC_RELAXED, __HIP_MEMORY_SCOPE_AGENT);
      __builtin_amdgcn_sched_barrier(0);
    });
    STAMP(KID, 6);
    for (int it = 0; it < (1 << 20); ++it) {
      if (__all(swv[0] != 0u && swv[1] != 0u && swv[2] != 0u && swv[3] != 0u)) break;
      __builtin_amdgcn_s_sleep(1);
#pragma unroll
      for (int ct = 0; ct < 4; ++ct)
        swv[ct] = __hip_atomic_load(sw + mt * 128 + ct * 32 + r32, __ATOMIC_RELAXED, __HIP_MEMORY_SCOPE_AGENT);
    }
    asm volatile("" : "+v"(swv[0]), "+v"(swv[1]), "+v"(swv[2]), "+v"(swv[3]));
#pragma unroll
    for (int q = 0; q < 4; ++q) ws4[q] = *(const f32x4*)(smem + 9 * 16384 + 256 + (wave * 32 + 8 * q + 4 * hh) * 4);
#pragma unroll
    for (int ct = 0; ct < 4; ++ct) {
      const float rs = __builtin_amdgcn_rcpf(bf_hi(swv[ct]));
      const float cm = -bf_lo(swv[ct]) * rs;
      char* dst = hbuf + ((size_t)((mt * 4 + ct) * 112 + ft * 2) * 64 + lane) * 16;
      const f32x2 c2 = {cm, cm}, r2 = {rs, rs};
#pragma unroll
      for (int s = 0; s < 2; ++s) {
        u32x4 o;
#pragma unroll
        for (int j = 0; j < 4; ++j) {
          const int reg = 8 * s + 2 * j;
          const f32x2 wsum = {ws4[reg >> 2][reg & 3], ws4[reg >> 2][(reg & 3) + 1]};
          const f32x2 a2 = {acc[ct][reg], acc[ct][reg + 1]};
          const f32x2 v2 = __builtin_elementwise_fma(a2, r2, c2 * wsum);
          const s16x2 pv = __builtin_bit_cast(s16x2, __builtin_convertvector(v2, bf16x2));
          const s16x2 z = {0, 0};
          o[j] = __builtin_bit_cast(unsigned, __builtin_elementwise_max(pv, z));
        }
        st16_sc1(dst + s * 1024, o);
      }
    }
    STAMP(KID, 7);
    wait_vm<0>();
    if (lane == 0) {
      const unsigned old = __hip_atomic_fetch_add(lcnt, 1u, __ATOMIC_RELAXED, __HIP_MEMORY_SCOPE_WORKGROUP);
      if (old == 3u) __hip_atomic_store(flags + (mt * 16 + nt) * FLS, 1u, __ATOMIC_RELAXED, __HIP_MEMORY_SCOPE_AGENT);
    }
    STAMP(KID, 8);
    static_for<0, 8>([&](auto J) { pf_xr(J); });
  }
  pin7(wr); pin7(wr + 7); pin7(wr + 14); pin7(wr + 21);
  {
    unsigned x0_ = xr[0], x1_ = xr[1], x2_ = xr[2], x3_ = xr[3], x4_ = xr[4], x5_ = xr[5], x6_ = xr[6], x7_ = xr[7];
    asm volatile("" : "+v"(x0_), "+v"(x1_), "+v"(x2_), "+v"(x3_), "+v"(x4_), "+v"(x5_), "+v"(x6_), "+v"(x7_));
    xr[0] = x0_; xr[1] = x1_; xr[2] = x2_; xr[3] = x3_; xr[4] = x4_; xr[5] = x5_; xr[6] = x6_; xr[7] = x7_;
  }
  __builtin_amdgcn_s_barrier();
  if (wave8 == 7) {
    const unsigned* fp = flags + ((mt2 >> 1) * 16 + (lane < 14 ? lane : 0)) * FLS;
    for (int it = 0; it < (1 << 20); ++it) {
      const unsigned f = __hip_atomic_load(fp, __ATOMIC_RELAXED, __HIP_MEMORY_SCOPE_AGENT);
      if (__all(f != 0u)) break;
      __builtin_amdgcn_s_sleep(POLL_SLEEP);
    }
  }
  asm volatile("s_waitcnt lgkmcnt(0)" ::: "memory");
  __builtin_amdgcn_s_barrier();
  STAMP(KID, 9);
  const char* hsrc = hbuf + (size_t)(mt2 * 2) * 112 * 1024 + wave8 * 1024 + lane * 16;
  char* dstb = smem + wave8 * 2048;
  auto issue2 = [&](int c) {
#pragma unroll
    for (int mi = 0; mi < 2; ++mi)
      __builtin_amdgcn_global_load_lds((const unsigned*)(hsrc + (size_t)mi * 112 * 1024 + c * 8192),
                                       (LAS unsigned*)(dstb + (c % G2_R) * 16384 + mi * 1024), 16, 0, 16  );
  };
  f32x16 acc[2];
#pragma unroll
  for (int i = 0; i < 16; ++i) { acc[0][i] = 0.f; acc[1][i] = 0.f; }
  const char* rdh = smem + kq * 4096 + lane * 16;
  bf16x8 hf[2][4];
#pragma unroll
  for (int c = 0; c < g2_iss(0); ++c) issue2(c);
  auto mainloop = [&](auto GRP) {
    constexpr int grp = GRP;
    static_for<0, 15>([&](auto C) {
      constexpr int c = C;
      if constexpr (c < 14) {
        wait_vm_lgkm0<2 * (g2_iss(c) - (c + 1))>();
        __builtin_amdgcn_s_barrier();
        if constexpr (c >= 1) pin4(hf[(c - 1) & 1]);
        if constexpr (c == 0) STAMP(KID, 10);
#pragma unroll
        for (int e = 0; e < 4; ++e) hf[c & 1][e] = *(const bf16x8*)(rdh + (c % G2_R) * 16384 + e * 1024);
      }
      auto mem = [&]() {
        if constexpr (c < 14) {
#pragma unroll
          for (int cn = g2_iss(c); cn < g2_iss(c + 1); ++cn) issue2(cn);
        }
      };
      auto mma = [&]() {
        if constexpr (c >= 1) {
          constexpr int pc = c - 1;
#pragma unroll
          for (int j = 0; j < 2; ++j)
#pragma unroll
            for (int mi = 0; mi < 2; ++mi)
              acc[mi] = __builtin_amdgcn_mfma_f32_32x32x16_bf16(__builtin_bit_cast(bf16x8, wr[2 * pc + j]), hf[pc & 1][j * 2 + mi], acc[mi], 0, 0, 0);
        }
      };
      __builtin_amdgcn_sched_barrier(0);
      if constexpr (grp == 0) { mem(); __builtin_amdgcn_sched_barrier(0); mma(); }
      else { mma(); __builtin_amdgcn_sched_barrier(0); mem(); }
      __builtin_amdgcn_sched_barrier(0);
    });
  };
  if (wave8 < 4) mainloop(std::integral_constant<int, 0>{});
  else mainloop(std::integral_constant<int, 1>{});
  STAMP(KID, 11);
  char* red = smem + 5 * 16384;
  {
#pragma unroll
    for (int pc = 0; pc < 4; ++pc) {
      if (pc != kq) {
        const int slot = (kq > pc) ? kq - 1 : kq;
        char* dst = red + ((pc * 2 + ni) * 3 + slot) * 2048 + lane * 16;
        f32x4 g0, g1;
#pragma unroll
        for (int i = 0; i < 4; ++i) { g0[i] = acc[pc >> 1][8 * (pc & 1) + i]; g1[i] = acc[pc >> 1][8 * (pc & 1) + 4 + i]; }
        *(f32x4*)dst = g0;
        *(f32x4*)(dst + 1024) = g1;
      }
    }
  }
  float mine[8];
#pragma unroll
  for (int i = 0; i < 8; ++i) {
    const float a0 = kh_f ? acc[0][8 + i] : acc[0][i];
    const float a1 = kh_f ? acc[1][8 + i] : acc[1][i];
    mine[i] = mi_f ? a1 : a0;
  }
  __syncthreads();
  {
    const char* src = red + (wave8 * 3) * 2048 + lane * 16;
#pragma unroll
    for (int sl = 0; sl < 3; ++sl) {
      const f32x4 o0 = *(const f32x4*)(src + sl * 2048);
      const f32x4 o1 = *(const f32x4*)(src + sl * 2048 + 1024);
#pragma unroll
      for (int i = 0; i < 4; ++i) { mine[i] += o0[i]; mine[4 + i] += o1[i]; }
    }
  }
  float xo[8];
  float s1 = 0.f, s2 = 0.f;
#pragma unroll
  for (int i = 0; i < 8; ++i) {
    const float v = rbf(__uint_as_float(((unsigned)xr[i]) << 16) + rbf(mine[i]));
    xo[i] = v;
    s1 += v; s2 += v * v;
  }
  unsigned short* xout = p.xout;
  if (LAYER == 1) {
#pragma unroll
    for (int q = 0; q < 2; ++q)
      *(u32x2*)(xout + (size_t)tok * D_ + dbase + 8 * q) =
          u32x2{pk_bf16(xo[4 * q], xo[4 * q + 1]), pk_bf16(xo[4 * q + 2], xo[4 * q + 3])};
  } else {
#pragma unroll
    for (int i = 0; i < 8; ++i)
      xout[(size_t)(dbase + (i & 3) + 8 * (i >> 2)) * L_ + tok] = (unsigned short)(__float_as_uint(xo[i]) >> 16);
    s1 += __shfl_xor(s1, 32, 64);
    s2 += __shfl_xor(s2, 32, 64);
    float* sred = (float*)(smem + 8 * 16384);
    sred[wave8 * 64 + lane] = hh ? s2 : s1;
    __syncthreads();
    if (wave8 < 2) {
      const int w0 = wave8 * 4;
      const float t = sred[w0 * 64 + lane] + sred[(w0 + 1) * 64 + lane] + sred[(w0 + 2) * 64 + lane] + sred[(w0 + 3) * 64 + lane];
      atomicAdd((float*)(ws + WS_ST1) + 2 * (mt2 * 64 + wave8 * 32 + r32) + hh, t);
    }
  }
  STAMP(KID, 15);
}

extern "C" void kernel_launch(void* const* d_in, const int* in_sizes, int n_in,
                              void* d_out, int out_size, void* d_ws, size_t ws_size,
                              hipStream_t stream) {
  const unsigned short* x0 = (const unsigned short*)d_in[0];
  const unsigned short* logA = (const unsigned short*)d_in[1];
  const unsigned short* Aim = (const unsigned short*)d_in[2];
  const unsigned short* Cre = (const unsigned short*)d_in[3];
  const unsigned short* Cim = (const unsigned short*)d_in[4];
  const unsigned short* logdt = (const unsigned short*)d_in[5];
  const unsigned short* dskip = (const unsigned short*)d_in[6];
  const unsigned short* w1 = (const unsigned short*)d_in[8];
  const unsigned short* w2 = (const unsigned short*)d_in[9];
  char* ws = (char*)d_ws;
  (void)hipFuncSetAttribute((const void*)layer_kernel<0>, hipFuncAttributeMaxDynamicSharedMemorySize, LAYER_LDS);
  (void)hipFuncSetAttribute((const void*)layer_kernel<1>, hipFuncAttributeMaxDynamicSharedMemorySize, LAYER_LDS);
  prep_kernel<<<PREP_BLOCKS, 256, 0, stream>>>(x0, logA, Aim, Cre, Cim, logdt, w1, w2, ws);
  LayerArgs a0 = {(const unsigned short*)(ws + WS_XT0), dskip, (unsigned short*)(ws + WS_XT1), ws + WS_H,
                  (unsigned short*)(ws + WS_XT2), ws};
  LayerArgs a1 = {(const unsigned short*)(ws + WS_XT2), dskip, (unsigned short*)(ws + WS_XT3), ws + WS_H2,
                  (unsigned short*)d_out, ws};
  layer_kernel<0><<<256, 512, LAYER_LDS, stream>>>(a0);
  layer_kernel<1><<<256, 512, LAYER_LDS, stream>>>(a1);
}
```

```cpp
#include <hip/hip_runtime.h>
#include <hip/hip_bf16.h>
#include <stdint.h>

typedef __attribute__((ext_vector_type(8))) __bf16 bf16x8;
typedef __attribute__((ext_vector_type(2))) __bf16 bf16x2;
typedef __attribute__((ext_vector_type(4))) short s16x4;
typedef __attribute__((ext_vector_type(2))) short s16x2;
typedef __attribute__((ext_vector_type(8))) short s16x8;
typedef __attribute__((ext_vector_type(8))) _Float16 f16x8;
typedef __attribute__((ext_vector_type(2))) _Float16 f16x2;
typedef __attribute__((ext_vector_type(2))) float f32x2;
typedef __attribute__((ext_vector_type(4))) float f32x4;
typedef __attribute__((ext_vector_type(16))) float f32x16;
typedef __attribute__((ext_vector_type(4))) unsigned u32x4;
typedef __attribute__((ext_vector_type(2))) unsigned u32x2;
#define LAS __attribute__((address_space(3)))
#define DI __device__ __forceinline__

constexpr int L_ = 2048, D_ = 512, N_ = 128, F_ = 1792;

constexpr size_t MiB = 1024 * 1024;
constexpr size_t WS_G    = 0;
constexpr size_t WS_XT0  = 2 * MiB;
constexpr size_t WS_XT1  = 4 * MiB;
constexpr size_t WS_XT2  = 6 * MiB;
constexpr size_t WS_XT3  = 8 * MiB;
constexpr size_t WS_W1T  = 10 * MiB;
constexpr size_t WS_W2T  = 12 * MiB;
constexpr size_t WS_H    = 14 * MiB;
constexpr size_t WS_H2   = 24 * MiB;
constexpr size_t WS_KINV = 22 * MiB;
constexpr size_t WS_W1S  = WS_KINV + 4096;
constexpr size_t WS_Z0   = 23 * MiB;
constexpr size_t WS_ST1  = WS_Z0;
constexpr size_t WS_SW   = WS_ST1 + 16384;
#define FLS 32
#define CREP 8
#define CFS (2 * CREP)
constexpr size_t WS_FL   = WS_SW + 16384;
constexpr size_t WS_CF   = WS_FL + 2048 * FLS;
constexpr size_t WS_ZBYTES = 16384 + 16384 + 2048 * FLS + 2048 * CFS;
constexpr size_t WS_SW0  = WS_Z0 + 512 * 1024;

#define STAMP(kid, i)
#define STAMPT(kid, i, T)
DI float bf_lo(unsigned u) { return __uint_as_float(u << 16); }
DI float bf_hi(unsigned u) { return __uint_as_float(u & 0xffff0000u); }
DI unsigned pk_bf16(float a, float b) {
  f32x2 v = {a, b};
  return __builtin_bit_cast(unsigned, __builtin_convertvector(v, bf16x2));
}
DI unsigned pk_f16(float a, float b) {
  f32x2 v = {a, b};
  return __builtin_bit_cast(unsigned, __builtin_convertvector(v, f16x2));
}
DI float rbf(float a) { return bf_lo(pk_bf16(a, 0.f)); }
DI float ld_bf(const unsigned short* p) { return __uint_as_float(((unsigned)*p) << 16); }
DI unsigned perm_lo(unsigned hi, unsigned lo) { return __builtin_amdgcn_perm(hi, lo, 0x05040100u); }
DI unsigned perm_hi(unsigned hi, unsigned lo) { return __builtin_amdgcn_perm(hi, lo, 0x07060302u); }
template <int CTRL> DI float dpp_add(float v) {
  return v + __uint_as_float(__builtin_amdgcn_update_dpp(0u, __float_as_uint(v), CTRL, 0xf, 0xf, true));
}
DI float wave_sum(float v) {
  v = dpp_add<0xB1>(v);
  v = dpp_add<0x4E>(v);
  v = dpp_add<0x141>(v);
  v = dpp_add<0x140>(v);
  const unsigned u = __float_as_uint(v);
  return (__uint_as_float(__builtin_amdgcn_readlane(u, 0)) + __uint_as_float(__builtin_amdgcn_readlane(u, 16))) +
         (__uint_as_float(__builtin_amdgcn_readlane(u, 32)) + __uint_as_float(__builtin_amdgcn_readlane(u, 48)));
}
#define ST_FLAGS ""
DI void st16(void* p, u32x4 v) { asm volatile("global_store_dwordx4 %0, %1, off " ST_FLAGS "\n\ts_nop 1" ::"v"(p), "v"(v) : "memory"); }
DI void st8(void* p, u32x2 v) { asm volatile("global_store_dwordx2 %0, %1, off " ST_FLAGS "\n\ts_nop 1" ::"v"(p), "v"(v) : "memory"); }
DI void st2(void* p, unsigned v) { asm volatile("global_store_short %0, %1, off " ST_FLAGS "\n\ts_nop 1" ::"v"(p), "v"(v) : "memory"); }
template <int N> DI void wait_vm() { asm volatile("s_waitcnt vmcnt(%0)" ::"n"(N) : "memory"); }
template <int OFF> DI void gload16(u32x4& dst, const char* p) {
  asm volatile("global_load_dwordx4 %0, %1, off offset:%2" : "=v"(dst) : "v"(p), "n"(OFF));
}
template <int N> DI void wait_vm4(u32x4& a, u32x4& b, u32x4& c, u32x4& d) {
  asm volatile("s_waitcnt vmcnt(%4)" : "+v"(a), "+v"(b), "+v"(c), "+v"(d) : "n"(N) : "memory");
}
template <int OFF> DI void tr_read(u32x2& d, unsigned addr) {
  asm volatile("ds_read_b64_tr_b16 %0, %1 offset:%2" : "=v"(d) : "v"(addr), "n"(OFF));
}
template <int N> DI void wait_lgkm8(u32x2* b) {
  asm volatile("s_waitcnt lgkmcnt(%8)"
               : "+v"(b[0]), "+v"(b[1]), "+v"(b[2]), "+v"(b[3]), "+v"(b[4]), "+v"(b[5]), "+v"(b[6]), "+v"(b[7])
               : "n"(N) : "memory");
}
template <int N> DI void wait_vm_lgkm0() { asm volatile("s_waitcnt vmcnt(%0) lgkmcnt(0)" ::"n"(N) : "memory"); }
template <int I, int E, class F> DI void static_for(F&& f) {
  if constexpr (I < E) { f(std::integral_constant<int, I>{}); static_for<I + 1, E>(f); }
}

constexpr int TROW = 528;

__host__ __device__ constexpr int tab_w(int idx) {
  return idx < 16 ? idx : (idx < 40 ? ((8 + ((idx - 16) & 7)) << (1 + ((idx - 16) >> 3))) : 128);
}
__host__ __device__ constexpr int ilog2c(int w) { return w <= 1 ? 0 : 1 + ilog2c(w >> 1); }
__host__ __device__ constexpr int tab_idx(int w) {
  return w < 16 ? w : (w == 128 ? 40 : 16 + 8 * (ilog2c(w) - 4) + ((w >> (ilog2c(w) - 3)) - 8));
}
DI int tab_idx_rt(int w) {
  if (w < 16) return w;
  int lg = 31 - __clz(w);
  return 16 + 8 * (lg - 4) + ((w >> (lg - 3)) - 8);
}

DI void retile_load(int u, const unsigned short* __restrict__ w1, const unsigned short* __restrict__ w2, u32x2* r, size_t& dst) {
  if (u < 28672) {
    const int fg = u % 448, o = u / 448;
    const int f = 4 * fg;
#pragma unroll
    for (int j = 0; j < 8; ++j) r[j] = *(const u32x2*)(w1 + (size_t)(8 * o + j) * F_ + f);
    dst = WS_W1T + ((size_t)(((f >> 5) * 32 + (o >> 1)) * 64 + (o & 1) * 32 + (f & 31))) * 16;
  } else {
    const int v = u - 28672;
    const int dg = v & 127, o = v >> 7;
    const int dd = 4 * dg, ks = o >> 1, hh = o & 1;
#pragma unroll
    for (int j = 0; j < 8; ++j)
      r[j] = *(const u32x2*)(w2 + (size_t)(16 * ks + 8 * (j >> 2) + 4 * hh + (j & 3)) * D_ + dd);
    dst = WS_W2T + ((size_t)(((dd >> 5) * 112 + ks) * 64 + hh * 32 + (dd & 31))) * 16;
  }
}
DI void retile_store(const u32x2* r, char* ws, size_t dst) {
#pragma unroll
  for (int c = 0; c < 2; ++c) {
    u32x4 lo = {perm_lo(r[1][c], r[0][c]), perm_lo(r[3][c], r[2][c]), perm_lo(r[5][c], r[4][c]), perm_lo(r[7][c], r[6][c])};
    u32x4 hi = {perm_hi(r[1][c], r[0][c]), perm_hi(r[3][c], r[2][c]), perm_hi(r[5][c], r[4][c]), perm_hi(r[7][c], r[6][c])};
    st16(ws + dst + (2 * c) * 16, lo);
    st16(ws + dst + (2 * c + 1) * 16, hi);
  }
}

#define LAYER_CODE 22528
#define PREP_CODE 14336
template <int BYTES> DI unsigned warm_code(const char* pc, int t) {
  const int o = t * 128;
  return *(const unsigned*)(pc + (o < BYTES ? o : 0));
}
constexpr int PB_KGEN = 512, PB_XT = 64, PB_ST = 64, PB_RT = 112, PB_WS = 16;
constexpr int PREP_BLOCKS = PB_KGEN + PB_XT + PB_ST + PB_RT + PB_WS;

__global__ __launch_bounds__(256) void prep_kernel(
    const unsigned short* __restrict__ x0, const unsigned short* __restrict__ logA,
    const unsigned short* __restrict__ Aim, const unsigned short* __restrict__ Cre,
    const unsigned short* __restrict__ Cim, const unsigned short* __restrict__ logdt,
    const unsigned short* __restrict__ w1, const unsigned short* __restrict__ w2, char* __restrict__ ws) {
  __shared__ __attribute__((aligned(16))) char smem[82 * TROW + 1536];
  float* red = (float*)smem;
  const int tid = threadIdx.x, lane = tid & 63;
  const int wave = __builtin_amdgcn_readfirstlane(tid >> 6);
  int b = blockIdx.x;
  STAMP(0, 0);
  unsigned cw1 = 0u;
  if (b >= PB_KGEN && b < PB_KGEN + PB_XT) cw1 = warm_code<PREP_CODE>((const char*)__builtin_amdgcn_s_getpc(), tid);
  if (b < PB_KGEN) {
    const int d = ((b & 7) * 32 + ((b >> 3) & 31)) * 2 + (b >> 8);
    const int isH = wave >> 1;
    const int n = (wave & 1) * 64 + lane;
    const float pdt = ld_bf(logdt + d), pla = ld_bf(logA + d * N_ + n), pai = ld_bf(Aim + d * N_ + n);
    const float p0r = ld_bf(Cre + d * N_ + lane), p0i = ld_bf(Cim + d * N_ + lane);
    const float p1r = ld_bf(Cre + d * N_ + 64 + lane), p1i = ld_bf(Cim + d * N_ + 64 + lane);
    const float dt = rbf(expf(pdt));
    const float ar = -rbf(expf(pla)) * dt;
    const float ai = pai * (isH ? -dt : dt);
    const float c0r = p0r * dt, c0i = p0i * dt, c1r = p1r * dt, c1i = p1i * dt;
    const float S = wave_sum(2.f * (fabsf(c0r) + fabsf(c0i) + fabsf(c1r) + fabsf(c1i)));
    int ex = 0;
    (void)frexpf(S, &ex);
    if (!(S > 0.f) || !(S < 3.0e38f)) ex = 15;
    ex = max(-100, min(ex, 120));
    const float scale = ldexpf(1.f, 15 - ex);
    const float cr = (wave & 1) ? c1r : c0r, ci = (wave & 1) ? c1i : c0i;
    const float base = isH ? 16.f : 1.f;
    const float er = ar * 1.44269504088896340736f * base;
    const float IH = 0.15915494309189533577f;
    const float IL = (float)(0.15915494309189533577 - (double)0.15915494309189533577f);
    float fh = ai * IH;
    float fl = fmaf(ai, IH, -fh) + ai * IL;
    fh *= base; fl *= base;
    STAMP(0, 1);
    f32x2 Er2[8], Ei2[8];
#pragma unroll
    for (int k = 0; k < 8; ++k) {
      const float pw = (float)(1 << k);
      const float mag = __builtin_amdgcn_exp2f(er * pw);
      const float ph = __builtin_amdgcn_fractf(fh * pw) + fl * pw;
      const float re = mag * __builtin_amdgcn_cosf(ph), im = mag * __builtin_amdgcn_sinf(ph);
      Er2[k] = f32x2{re, re};
      Ei2[k] = f32x2{-im, im};
    }
    f32x2 tv[41];
    tv[0] = isH ? f32x2{2.f * scale * cr, -2.f * scale * ci} : f32x2{1.f, 0.f};
    char* trow = smem + isH * (41 * TROW) + n * 4;
    *(unsigned*)trow = __builtin_bit_cast(unsigned, __builtin_convertvector(tv[0], f16x2));
    static_for<1, 41>([&](auto I) {
      constexpr int idx = I;
      constexpr int w = tab_w(idx);
      constexpr int lg = ilog2c(w);
      constexpr int ridx = tab_idx(w - (1 << lg));
      const f32x2 sw = __builtin_shufflevector(tv[ridx], tv[ridx], 1, 0);
      tv[idx] = __builtin_elementwise_fma(Ei2[lg], sw, Er2[lg] * tv[ridx]);
      *(unsigned*)(trow + idx * TROW) = __builtin_bit_cast(unsigned, __builtin_convertvector(tv[idx], f16x2));
    });
    int cix[8];
#pragma unroll
    for (int e = 0; e < 8; ++e) {
      const int l = 2047 - (tid * 8 + e);
      const int sh = max((31 - __clz(l | 1)) - 7, 0);
      const int q = (l + ((1 << sh) >> 1) - (sh > 0 ? 1 : 0) + ((l >> sh) & (sh > 0 ? 1 : 0))) >> sh;
      cix[e] = (sh > 0) ? (128 * sh + q) : l;
    }
    STAMP(0, 2);
    __syncthreads();
    _Float16* kc = (_Float16*)(smem + 82 * TROW);
    {
      const int s = wave, i16 = lane & 15, g = lane >> 4;
      const int hw = (s == 0) ? i16 : (i16 < 8 ? ((8 + i16) << s) : 128);
      const char* arow = smem + 41 * TROW + tab_idx_rt(hw) * TROW + g * 16;
      const char* brow = smem + tab_idx_rt(i16 << s) * TROW + g * 16;
      f16x8 af[8], bf[8];
#pragma unroll
      for (int ks = 0; ks < 8; ++ks) { af[ks] = *(const f16x8*)(arow + ks * 64); bf[ks] = *(const f16x8*)(brow + ks * 64); }
      f32x4 k0 = {0.f, 0.f, 0.f, 0.f}, k1 = {0.f, 0.f, 0.f, 0.f};
#pragma unroll
      for (int ks = 0; ks < 8; ks += 2) {
        k0 = __builtin_amdgcn_mfma_f32_16x16x32_f16(af[ks], bf[ks], k0, 0, 0, 0);
        k1 = __builtin_amdgcn_mfma_f32_16x16x32_f16(af[ks + 1], bf[ks + 1], k1, 0, 0, 0);
      }
#pragma unroll
      for (int r = 0; r < 4; ++r) {
        const int i = 4 * g + r;
        const int cidx = (s == 0) ? (16 * i + i16) : (256 + 128 * (s - 1) + 16 * i + i16);
        const bool ok = (s == 0) || (i < 8) || (s == 3 && i == 8 && i16 == 0);
        if (ok) kc[cidx] = (_Float16)(k0[r] + k1[r]);
      }
    }
    __syncthreads();
    STAMP(0, 3);
    unsigned short o[8];
#pragma unroll
    for (int e = 0; e < 8; ++e) o[e] = ((const unsigned short*)kc)[cix[e]];
    *(u32x4*)(ws + WS_G + (size_t)d * 4096 + tid * 16) =
        u32x4{(unsigned)o[0] | ((unsigned)o[1] << 16), (unsigned)o[2] | ((unsigned)o[3] << 16),
              (unsigned)o[4] | ((unsigned)o[5] << 16), (unsigned)o[6] | ((unsigned)o[7] << 16)};
    if (tid == 0) ((float*)(ws + WS_KINV))[d] = ldexpf(1.f, ex - 15);
    STAMP(0, 15);
    return;
  }
  b -= PB_KGEN;
  if (b < PB_XT) {
    unsigned long long lpc;
    asm volatile("s_getpc_b64 s[40:41]\n\ts_add_u32 s40, s40, s4d_layer_kernel@rel32@lo+4\n\t"
                 "s_addc_u32 s41, s41, s4d_layer_kernel@rel32@hi+12\n\ts_mov_b64 %0, s[40:41]"
                 : "=s"(lpc) :: "scc", "s40", "s41");
    const unsigned cw2 = warm_code<LAYER_CODE>((const char*)lpc, tid);
    const int tile = b * 4 + wave, tt = tile >> 3, ct = tile & 7;
    const int tg = lane & 7, cg = lane >> 3;
    const unsigned short* src = x0 + (size_t)(tt * 64 + tg * 8) * D_ + ct * 64 + cg * 8;
    u32x4 r[8];
#pragma unroll
    for (int j = 0; j < 8; ++j) r[j] = *(const u32x4*)(src + (size_t)j * D_);
    unsigned short* dst = (unsigned short*)(ws + WS_XT0) + (size_t)(ct * 64 + cg * 8) * L_ + tt * 64 + tg * 8;
#pragma unroll
    for (int c = 0; c < 4; ++c) {
      u32x4 lo = {perm_lo(r[1][c], r[0][c]), perm_lo(r[3][c], r[2][c]), perm_lo(r[5][c], r[4][c]), perm_lo(r[7][c], r[6][c])};
      u32x4 hi = {perm_hi(r[1][c], r[0][c]), perm_hi(r[3][c], r[2][c]), perm_hi(r[5][c], r[4][c]), perm_hi(r[7][c], r[6][c])};
      *(u32x4*)(dst + (size_t)(2 * c) * L_) = lo;
      *(u32x4*)(dst + (size_t)(2 * c + 1) * L_) = hi;
    }
    asm volatile("" ::"v"(cw1), "v"(cw2));
    STAMP(0, 15);
    return;
  }
  b -= PB_XT;
  if (b < PB_ST) {
    const int t0 = b * 32 + wave * 8;
    u32x4 r[8];
#pragma unroll
    for (int j = 0; j < 8; ++j) r[j] = *(const u32x4*)(x0 + (size_t)(t0 + j) * D_ + lane * 8);
    for (int i = b * 256 + tid; i < (int)(WS_ZBYTES / 16); i += PB_ST * 256) ((u32x4*)(ws + WS_Z0))[i] = u32x4{0u, 0u, 0u, 0u};
    asm volatile("" : "+v"(r[0]), "+v"(r[1]), "+v"(r[2]), "+v"(r[3]), "+v"(r[4]), "+v"(r[5]), "+v"(r[6]), "+v"(r[7]));
    unsigned word = 0u;
#pragma unroll
    for (int j = 0; j < 8; ++j) {
      float s = 0.f;
#pragma unroll
      for (int c = 0; c < 4; ++c) s += bf_lo(r[j][c]) + bf_hi(r[j][c]);
      const float mean = wave_sum(s) * (1.f / D_);
      float q = 0.f;
#pragma unroll
      for (int c = 0; c < 4; ++c) {
        const float a = bf_lo(r[j][c]) - mean, bb = bf_hi(r[j][c]) - mean;
        q += a * a + bb * bb;
      }
      const float var = wave_sum(q) * (1.f / D_);
      const unsigned wv = pk_bf16(mean, sqrtf(rbf(rbf(var) + 1e-5f)));
      if (lane == j) word = wv;
    }
    if (lane < 8) ((unsigned*)(ws + WS_SW0))[t0 + lane] = word;
    STAMP(0, 15);
    return;
  }
  b -= PB_ST;
  if (b < PB_RT) {
    u32x2 r0[8], r1[8];
    size_t d0_, d1_;
    retile_load(b * 512 + tid, w1, w2, r0, d0_);
    retile_load(b * 512 + 256 + tid, w1, w2, r1, d1_);
    retile_store(r0, ws, d0_);
    retile_store(r1, ws, d1_);
    STAMP(0, 15);
    return;
  }
  b -= PB_RT;
  {
    const int cg = tid % 14, rg = tid / 14;
    const int f0 = b * 112 + cg * 8;
    float s[8] = {0.f, 0.f, 0.f, 0.f, 0.f, 0.f, 0.f, 0.f};
    if (tid < 224) {
#pragma unroll
      for (int h = 0; h < 2; ++h) {
        u32x4 v[16];
#pragma unroll
        for (int j = 0; j < 16; ++j) v[j] = *(const u32x4*)(w1 + (size_t)(rg * 32 + h * 16 + j) * F_ + f0);
#pragma unroll
        for (int j = 0; j < 16; ++j)
#pragma unroll
          for (int c = 0; c < 4; ++c) { s[2 * c] += bf_lo(v[j][c]); s[2 * c + 1] += bf_hi(v[j][c]); }
      }
#pragma unroll
      for (int e = 0; e < 8; ++e) red[rg * 112 + cg * 8 + e] = s[e];
    }
    __syncthreads();
    if (tid < 112) {
      float t = 0.f;
#pragma unroll
      for (int r = 0; r < 16; ++r) t += red[r * 112 + tid];
      ((float*)(ws + WS_W1S))[b * 112 + tid] = t;
    }
    STAMP(0, 15);
  }
}

constexpr int CV_CS = 260 * 16;
constexpr int CV_KCH = 8 * CV_CS;
constexpr int CV_U0 = 2 * CV_KCH;
constexpr int CV_UCH = (256 + 2048) * 2;
constexpr int CV_XS = CV_U0 + 2 * CV_UCH;
constexpr int CV_RED = CV_XS + 2 * 8192;
constexpr int G1_R = 9;
constexpr int G2_R = 9;
constexpr int LAYER_LDS = 9 * 16384 + 1024;
static_assert(CV_RED + 16384 <= 9 * 16384, "conv LDS");
#define G1_W0 5
#define G2_W0 6
#define CV_PF1 16
#define CV_PF2 22
#define W1PRE 5
#define POLL_SLEEP 2
static_assert(G1_W0 <= G1_R - 2 && W1PRE <= G1_R, "ring safety");
__host__ __device__ constexpr int g2_iss(int i) {
  int v = G2_W0 + i;
  return v < 14 ? v : 14;
}
__host__ __device__ constexpr int g1_young(int c) {
  int n = 0;
  for (int j = c + 1; j < c + G1_W0 && j < 16; ++j) n += (j >= W1PRE) ? 4 : 2;
  return n;
}
template <int N> DI void wait_lgkm2(u32x2& a, u32x2& b) {
  asm volatile("s_waitcnt lgkmcnt(%2)" : "+v"(a), "+v"(b) : "n"(N) : "memory");
}
template <int N> DI void wait_lgkm3(u32x2& a, u32x2& b, u32x4& c) {
  asm volatile("s_waitcnt lgkmcnt(%3)" : "+v"(a), "+v"(b), "+v"(c) : "n"(N) : "memory");
}
template <int OFF> DI void lds_read16(u32x4& d, unsigned addr) {
  asm volatile("ds_read_b128 %0, %1 offset:%2" : "=v"(d) : "v"(addr), "n"(OFF));
}
DI void pin4(bf16x8* f) { asm volatile("" : "+v"(f[0]), "+v"(f[1]), "+v"(f[2]), "+v"(f[3])); }
DI void pin7(u32x4* f) { asm volatile("" : "+v"(f[0]), "+v"(f[1]), "+v"(f[2]), "+v"(f[3]), "+v"(f[4]), "+v"(f[5]), "+v"(f[6])); }
DI void st16_sc1(void* p, u32x4 v) { asm volatile("global_store_dwordx4 %0, %1, off sc1\n\ts_nop 1" ::"v"(p), "v"(v) : "memory"); }
DI void st8_sc1(void* p, u32x2 v) { asm volatile("global_store_dwordx2 %0, %1, off sc1\n\ts_nop 1" ::"v"(p), "v"(v) : "memory"); }
DI u32x4 ld16_sc1_wait(const void* p) {
  u32x4 v;
  asm volatile("global_load_dwordx4 %0, %1, off sc1\n\ts_waitcnt vmcnt(0)" : "=&v"(v) : "v"(p) : "memory");
  return v;
}

struct LayerArgs {
  const unsigned short* xin;
  const unsigned short* dskip;
  unsigned short* xmid;
  char* hbuf;
  unsigned short* xout;
  char* ws;
  long long layer;
};

extern "C" __global__ __launch_bounds__(512, 2) void s4d_layer_kernel(LayerArgs p) {
  const unsigned cw = warm_code<LAYER_CODE>((const char*)__builtin_amdgcn_s_getpc(), threadIdx.x);
  const int LAYER = (int)p.layer;
  extern __shared__ __attribute__((aligned(16))) char smem[];
  const int tid = threadIdx.x, lane = tid & 63;
  const int wave8 = __builtin_amdgcn_readfirstlane(tid >> 6);
  const int wave = wave8 & 3;
  const int bx = blockIdx.x & 7, bi = blockIdx.x >> 3;
  const int r32 = lane & 31, hh = lane >> 5;
  char* ws = p.ws;
  unsigned short* xt = p.xmid;
  char* hbuf = p.hbuf;
  unsigned* sw = (unsigned*)(ws + WS_SW) + LAYER * 2048;
  unsigned* flags = (unsigned*)(ws + WS_FL) + LAYER * 256 * FLS;
  unsigned* cflags = (unsigned*)(ws + WS_CF) + LAYER * (CREP * 16 * 32);
  const char* w1t = ws + WS_W1T;
  const char* w2t = ws + WS_W2T;
  const int KID = 1 + 3 * LAYER;
  const int mt2 = bx * 4 + (bi & 3), nt2 = bi >> 2;
  const int ni = wave8 & 1, kq = wave8 >> 1;
  const int mi_f = kq >> 1, kh_f = kq & 1;
  const int tok = mt2 * 64 + mi_f * 32 + r32;
  const int dbase = nt2 * 64 + ni * 32 + 4 * hh + 16 * kh_f;
  u32x4 wr[28];
  unsigned short xr[8];
  const char* w2src = w2t + (size_t)(nt2 * 2 + ni) * 112 * 1024 + kq * 2048 + lane * 16;
  const unsigned short* xrsrc = xt + ((size_t)(tok >> 7) * D_ + dbase) * 128 + (tok & 127);
  auto pf_w2 = [&](auto J) {
    constexpr int j = J;
    wr[j] = *(const u32x4*)(w2src + (j >> 1) * 8192 + (j & 1) * 1024);
  };
  auto pf_xr = [&](auto J) {
    constexpr int j = J;
    xr[j] = __hip_atomic_load(xrsrc + ((j & 3) + 8 * (j >> 2)) * 128, __ATOMIC_RELAXED, __HIP_MEMORY_SCOPE_AGENT);
  };
  const int mt = (bx >> 1) * 4 + (bi & 3), nt = (bx & 1) * 7 + (bi >> 2);
  const int ft = nt * 4 + wave;
  STAMP(KID, 0);
  {
    const int d0 = 2 * (bx * 32 + bi);
    const int gz = tid & 255, gc = tid >> 8;
    const int t4 = tid * 4;
    const int ch = wave8 >> 2, half = wave8 & 1, qs = (wave8 >> 1) & 1;
    float xf[2][4], m[4], sd[4];
    const unsigned dsk2 = *(const unsigned*)(p.dskip + d0);
    const char* gp = ws + WS_G + (size_t)(d0 + gc) * 4096 + gz * 16;
    const u32x4 ga = *(const u32x4*)gp;
    const u32x4 gb = *(const u32x4*)(gp + 16);
    f32x4 st0, st1;
    u32x4 swv0;
    swv0 = *(const u32x4*)(ws + WS_SW0 + t4 * 4);
    const char* stp = ws + (LAYER ? WS_ST1 : WS_G) + t4 * 8;
    st0 = *(const f32x4*)stp; st1 = *(const f32x4*)(stp + 16);
    const u32x2 xv0 = *(const u32x2*)(p.xin + (size_t)d0 * L_ + t4);
    const u32x2 xv1 = *(const u32x2*)(p.xin + (size_t)(d0 + 1) * L_ + t4);
    const float kscale = ((const float*)(ws + WS_KINV))[d0 + ch];
    if (tid < 16) *(u32x4*)(smem + (tid >> 3) * CV_KCH + (tid & 7) * CV_CS + 256 * 16) = u32x4{0u, 0u, 0u, 0u};
    if (tid >= 64 && tid < 128) {
      const int q = tid - 64;
      *(u32x4*)(smem + CV_U0 + (q >> 5) * CV_UCH + (q & 31) * 16) = u32x4{0u, 0u, 0u, 0u};
    }
    if (tid == 128) *(unsigned*)(smem + 9 * 16384) = 0u;
    {
      const unsigned msk = (gz < 255) ? 0xffffffffu : 0u;
      unsigned w[8] = {ga[0], ga[1], ga[2], ga[3], gb[0] & msk, gb[1] & msk, gb[2] & msk, gb[3] & msk};
      unsigned o[7];
#pragma unroll
      for (int k = 0; k < 7; ++k) o[k] = __builtin_amdgcn_alignbit(w[k + 1], w[k], 16);
      char* dst = smem + gc * CV_KCH + gz * 16;
#pragma unroll
      for (int mm = 0; mm < 4; ++mm) {
        *(u32x4*)(dst + (2 * mm) * CV_CS) = u32x4{w[mm], w[mm + 1], w[mm + 2], w[mm + 3]};
        *(u32x4*)(dst + (2 * mm + 1) * CV_CS) = u32x4{o[mm], o[mm + 1], o[mm + 2], o[mm + 3]};
      }
    }
#pragma unroll
    for (int r = 0; r < 4; ++r) {
      {
        const unsigned wv = swv0[r];
        const float a = (r < 2) ? st0[(r & 1) * 2] : st1[(r & 1) * 2];
        const float bq = (r < 2) ? st0[(r & 1) * 2 + 1] : st1[(r & 1) * 2 + 1];
        const float mean = a * (1.f / D_), var = fmaxf(bq * (1.f / D_) - mean * mean, 0.f);
        const float m1 = rbf(mean), sd1 = rbf(__builtin_amdgcn_sqrtf(rbf(rbf(var) + 1e-5f)));
        m[r] = LAYER ? m1 : bf_lo(wv);
        sd[r] = LAYER ? sd1 : bf_hi(wv);
      }
      const unsigned w0 = xv0[r >> 1], w1_ = xv1[r >> 1];
      xf[0][r] = (r & 1) ? bf_hi(w0) : bf_lo(w0);
      xf[1][r] = (r & 1) ? bf_hi(w1_) : bf_lo(w1_);
    }
    {
      float rs[4];
#pragma unroll
      for (int r = 0; r < 4; ++r) rs[r] = __builtin_amdgcn_rcpf(sd[r]);
#pragma unroll
      for (int c = 0; c < 2; ++c) {
        const float dsk = c ? bf_hi(dsk2) : bf_lo(dsk2);
        float uu[4];
        unsigned xs[4];
#pragma unroll
        for (int r = 0; r < 4; ++r) {
          const float x = xf[c][r];
          const float dd = rbf(x - m[r]);
          float q = dd * rs[r];
          q = fmaf(fmaf(-q, sd[r], dd), rs[r], q);
          uu[r] = rbf(q);
          xs[r] = pk_bf16(x, uu[r] * dsk);
        }
        *(u32x2*)(smem + CV_U0 + c * CV_UCH + (256 + t4) * 2) = u32x2{pk_f16(uu[0], uu[1]), pk_f16(uu[2], uu[3])};
        *(u32x4*)(smem + CV_XS + c * 8192 + t4 * 4) = u32x4{xs[0], xs[1], xs[2], xs[3]};
      }
    }
    asm volatile("" ::"v"(cw));
    __syncthreads();
    STAMP(KID, 1);
    const int j16 = lane & 15, g = lane >> 4;
    const int cc = (7 - j16) & 7;
    const int zl = (2031 - j16 - cc) / 8 + g;
    const char* abase = smem + ch * CV_KCH + cc * CV_CS + (zl - 32 * 7 - 4 * (4 * half + 3)) * 16;
    const char* bbase = smem + CV_U0 + ch * CV_UCH + 480 + 32 * j16 + 16 * g - 64 * (4 * half + 3);
    f32x4 acc[4];
#pragma unroll
    for (int q = 0; q < 4; ++q) acc[q] = f32x4{0.f, 0.f, 0.f, 0.f};
    auto body = [&](auto QS) {
      constexpr int S = QS;
      constexpr int Q[4] = {S ? 6 : 7, S ? 5 : 4, S ? 2 : 3, S ? 1 : 0};
      constexpr int NF = S ? 7 : 8;
      f16x8 A[2][NF], B[2][NF];
      auto ld = [&](auto IT) {
        constexpr int it = IT;
#pragma unroll
        for (int k = 0; k < NF; ++k) {
          A[it & 1][k] = *(const f16x8*)(abase + (7 - k) * 512 + (3 - it) * 64);
          B[it & 1][NF - 1 - k] = *(const f16x8*)(bbase + (NF - 1 - k) * 512 + (3 - it) * 64);
        }
      };
      ld(std::integral_constant<int, 0>{});
      __builtin_amdgcn_sched_barrier(0);
      static_for<0, 4>([&](auto IT) {
        constexpr int it = IT;
        if constexpr (it + 1 < 4) ld(std::integral_constant<int, it + 1>{});
        static_for<it * (CV_PF1 / 4), (it + 1) * (CV_PF1 / 4)>([&](auto J) { pf_w2(J); });
#pragma unroll
        for (int b1 = 0; b1 < NF; ++b1)
#pragma unroll
          for (int e = 0; e < 4; ++e)
            if (b1 <= Q[e]) acc[e] = __builtin_amdgcn_mfma_f32_16x16x32_f16(A[it & 1][b1], B[it & 1][Q[e] - b1], acc[e], 0, 0, 0);
#pragma unroll
        for (int k = 0; k < 18; ++k) {
          __builtin_amdgcn_sched_group_barrier(0x8, 1, 0);
          if (it + 1 < 4 && k < 2 * NF) __builtin_amdgcn_sched_group_barrier(0x100, 1, 0);
          if (CV_PF1 > 0 && (k % (16 / (CV_PF1 / 4))) == 1 && k / (16 / (CV_PF1 / 4)) < CV_PF1 / 4)
            __builtin_amdgcn_sched_group_barrier(0x20, 1, 0);
        }
        __builtin_amdgcn_sched_barrier(0);
      });
    };
    if (qs) body(std::integral_constant<int, 1>{}); else body(std::integral_constant<int, 0>{});
    f32x4 keep[2];
    {
      char* wrp = smem + CV_RED + wave8 * 2048 + lane * 16;
#pragma unroll
      for (int e = 0; e < 2; ++e) {
        f32x4 give;
#pragma unroll
        for (int r = 0; r < 4; ++r) {
          const float lo = acc[e][r], hi = acc[2 + e][r];
          give[r] = half ? lo : hi;
          keep[e][r] = half ? hi : lo;
        }
        *(f32x4*)(wrp + e * 1024) = give;
      }
    }
    asm volatile("s_waitcnt lgkmcnt(0)" ::: "memory");
    __builtin_amdgcn_s_barrier();
    STAMP(KID, 2);
    {
      const char* rd = smem + CV_RED + (wave8 ^ 1) * 2048 + lane * 16;
      const int toff = 16 * j16 + 4 * g;
#pragma unroll
      for (int e = 0; e < 2; ++e) {
        const int q = half ? (e ? (qs ? 1 : 0) : (qs ? 2 : 3)) : (e ? (qs ? 5 : 4) : (qs ? 6 : 7));
        const int t = 256 * q + toff;
        const f32x4 o = *(const f32x4*)(rd + e * 1024);
        const u32x4 xs = *(const u32x4*)(smem + CV_XS + ch * 8192 + t * 4);
        const f32x4 y = keep[e] + o;
        float xo[4];
#pragma unroll
        for (int r = 0; r < 4; ++r) {
          const unsigned w = xs[r];
          xo[r] = bf_lo(w) + rbf(rbf(y[r] * kscale) + bf_hi(w));
        }
        st8_sc1(xt + ((size_t)(t >> 7) * D_ + d0 + ch) * 128 + (t & 127), u32x2{pk_bf16(xo[0], xo[1]), pk_bf16(xo[2], xo[3])});
      }
    }
    wait_vm_lgkm0<0>();
    __builtin_amdgcn_s_barrier();
    if (tid < CREP) (void)__hip_atomic_fetch_add(cflags + (tid * 16 + (d0 >> 5)) * 32, 1u, __ATOMIC_RELAXED, __HIP_MEMORY_SCOPE_AGENT);
    STAMP(KID, 3);
  }
  const int grow = lane >> 4, gslot = lane & 15;
  const unsigned short* xsrc = xt + ((size_t)mt * D_ + wave * 8 + grow) * 128 + ((gslot ^ (grow << 2)) * 8);
  const char* wsrc = w1t + (size_t)ft * 32 * 1024 + lane * 16;
  auto issue_w = [&](int c) {
    char* slot = smem + (c % G1_R) * 16384;
#pragma unroll
    for (int i = 0; i < 2; ++i)
      __builtin_amdgcn_global_load_lds((const unsigned*)(wsrc + (size_t)(c * 2 + i) * 1024),
                                       (LAS unsigned*)(slot + 8192 + (wave * 2 + i) * 1024), 16, 0, 0);
  };
  auto issue_x = [&](int c) {
    char* slot = smem + (c % G1_R) * 16384;
#pragma unroll
    for (int i = 0; i < 2; ++i)
      __builtin_amdgcn_global_load_lds((const unsigned*)(xsrc + (size_t)(c * 32 + i * 4) * 128),
                                       (LAS unsigned*)(slot + (wave * 8 + i * 4) * 256), 16, 0, 16  );
  };
  if (bi < 28 && wave8 >= 4) {
    if (wave8 < 6)
      __builtin_amdgcn_global_load_lds((const unsigned*)(ws + WS_W1S) + nt * 128 + wave * 64 + lane,
                                       (LAS unsigned*)(smem + 9 * 16384 + 256 + wave * 256), 4, 0, 0);
#pragma unroll
    for (int c = 0; c < W1PRE; ++c) issue_w(c);
  }
  __builtin_amdgcn_sched_barrier(0);
  static_for<CV_PF1, CV_PF2>([&](auto J) { pf_w2(J); });
  __builtin_amdgcn_sched_barrier(0);
  if (wave8 == 7) {
    const unsigned* cp = cflags + ((blockIdx.x % CREP) * 16 + (lane & 15)) * 32;
    for (int it = 0; it < (1 << 20); ++it) {
      const unsigned f = __hip_atomic_load(cp, __ATOMIC_RELAXED, __HIP_MEMORY_SCOPE_AGENT);
      if (__all(f >= 16u)) break;
      __builtin_amdgcn_s_sleep(POLL_SLEEP);
    }
  }
  __builtin_amdgcn_s_barrier();
  STAMP(KID, 4);
  if (bi >= 28) {
    const int ht = bx * 4 + (bi - 28);
    const int t8 = lane & 7, dg = wave8 * 8 + (lane >> 3);
    const auto rsrc = __builtin_amdgcn_make_buffer_rsrc((void*)xt, 0, L_ * D_ * 2, 0x00020000);
    const int off = (((ht >> 1) * D_ + dg * 8) * 128 + (ht & 1) * 64 + t8 * 8) * 2;
    u32x4 v[8];
#pragma unroll
    for (int j = 0; j < 8; ++j) v[j] = __builtin_amdgcn_raw_buffer_load_b128(rsrc, off + j * 256, 0, 16  );
    static_for<0, 8>([&](auto J) { pf_xr(J); });
    static_for<CV_PF2, 28>([&](auto J) { pf_w2(J); });
    float* red = (float*)smem;
    float sm[8];
#pragma unroll
    for (int e = 0; e < 8; ++e) {
      float a = 0.f;
#pragma unroll
      for (int j = 0; j < 8; ++j) a += (e & 1) ? bf_hi(v[j][e >> 1]) : bf_lo(v[j][e >> 1]);
      sm[e] = a;
    }
#pragma unroll
    for (int o = 8; o <= 32; o <<= 1) {
#pragma unroll
      for (int e = 0; e < 8; ++e) sm[e] += __shfl_xor(sm[e], o, 64);
    }
    if (lane < 8) {
#pragma unroll
      for (int e = 0; e < 8; ++e) red[wave8 * 64 + t8 * 8 + e] = sm[e];
    }
    __syncthreads();
    float mean[8];
#pragma unroll
    for (int e = 0; e < 8; ++e) {
      float a = 0.f;
#pragma unroll
      for (int w = 0; w < 8; ++w) a += red[w * 64 + t8 * 8 + e];
      mean[e] = a * (1.f / D_);
    }
#pragma unroll
    for (int e = 0; e < 8; ++e) {
      float a = 0.f;
#pragma unroll
      for (int j = 0; j < 8; ++j) {
        const float dd = ((e & 1) ? bf_hi(v[j][e >> 1]) : bf_lo(v[j][e >> 1])) - mean[e];
        a = fmaf(dd, dd, a);
      }
      sm[e] = a;
    }
#pragma unroll
    for (int o = 8; o <= 32; o <<= 1) {
#pragma unroll
      for (int e = 0; e < 8; ++e) sm[e] += __shfl_xor(sm[e], o, 64);
    }
    if (lane < 8) {
#pragma unroll
      for (int e = 0; e < 8; ++e) red[512 + wave8 * 64 + t8 * 8 + e] = sm[e];
    }
    __syncthreads();
    if (tid < 64) {
      float a = 0.f, q = 0.f;
#pragma unroll
      for (int w = 0; w < 8; ++w) { a += red[w * 64 + tid]; q += red[512 + w * 64 + tid]; }
      const float mm = a * (1.f / D_), var = q * (1.f / D_);
      const float sdv = sqrtf(rbf(rbf(var) + 1e-5f));
      __hip_atomic_store(sw + ht * 64 + tid, pk_bf16(mm, sdv), __ATOMIC_RELAXED, __HIP_MEMORY_SCOPE_AGENT);
    }
  } else if (wave8 >= 4) {
#pragma unroll
    for (int c = 0; c < G1_W0; ++c) { issue_x(c); if (c >= W1PRE) issue_w(c); }
    static_for<0, 16>([&](auto C) {
      constexpr int c = C;
      constexpr int extra = (c + G1_W0 > 16) ? 8 + (28 - CV_PF2) : 0;
      wait_vm<g1_young(c) + extra>();
      __builtin_amdgcn_s_barrier();
      if constexpr (c + G1_W0 < 16) { issue_x(c + G1_W0); if (c + G1_W0 >= W1PRE) issue_w(c + G1_W0); }
      if constexpr (c + G1_W0 == 16) {
        __builtin_amdgcn_sched_barrier(0);
        static_for<0, 8>([&](auto J) { pf_xr(J); });
        static_for<CV_PF2, 28>([&](auto J) { pf_w2(J); });
        __builtin_amdgcn_sched_barrier(0);
      }
    });
  } else {
    const int blk = (lane >> 4) & 1, q4 = (lane & 15) >> 2, p4 = lane & 3;
    const unsigned sbase = (unsigned)(uintptr_t)(LAS char*)smem;
    unsigned* lcnt = (unsigned*)(smem + 9 * 16384);
    unsigned tb[3][4], ta[3];
#pragma unroll
    for (int ct = 0; ct < 4; ++ct) {
      tb[0][ct] = sbase + (unsigned)((8 * hh + q4) * 256 + (((4 * ct + 2 * blk + (p4 >> 1)) ^ (q4 << 2)) * 16) + 8 * (p4 & 1));
      tb[1][ct] = tb[0][ct] + 65536u; tb[2][ct] = tb[0][ct] + 131072u;
    }
    ta[0] = sbase + 8192 + wave * 2048 + lane * 16; ta[1] = ta[0] + 65536u; ta[2] = ta[0] + 131072u;
    f32x16 acc[4];
#pragma unroll
    for (int ct = 0; ct < 4; ++ct)
#pragma unroll
      for (int i = 0; i < 16; ++i) acc[ct][i] = 0.f;
    constexpr int LA = 6;
    u32x2 bb[8][2];
    u32x4 af[3];
    unsigned swv[4];
    f32x4 ws4[4];
    auto issue_pair = [&](auto NN) {
      constexpr int n = NN, ks = n >> 2, ct = n & 3;
      constexpr int so = ((ks >> 1) % G1_R) * 16384, seg = so >> 16, o = (so & 65535) + (ks & 1) * 4096;
      if constexpr (ct == 0) lds_read16<(so & 65535) + (ks & 1) * 1024>(af[ks % 3], ta[seg]);
      tr_read<o>(bb[n & 7][0], tb[seg][ct]);
      tr_read<o + 1024>(bb[n & 7][1], tb[seg][ct]);
    };
    __builtin_amdgcn_s_barrier();
    STAMP(KID, 5);
    static_for<0, LA>([&](auto NN) { issue_pair(NN); });
    static_for<0, 128>([&](auto NN) {
      constexpr int n = NN, ks = n >> 2, ct = n & 3;
      constexpr int last = (n + LA - 1 < 127) ? (n + LA - 1) : 127;
      constexpr int younger = 2 * (last - n) + ((last >> 2) - (n >> 2));
      if constexpr (ct == 0) wait_lgkm3<younger>(bb[n & 7][0], bb[n & 7][1], af[ks % 3]);
      else wait_lgkm2<younger>(bb[n & 7][0], bb[n & 7][1]);
      const u32x4 bu = {bb[n & 7][0][0], bb[n & 7][0][1], bb[n & 7][1][0], bb[n & 7][1][1]};
      acc[ct] = __builtin_amdgcn_mfma_f32_32x32x16_bf16(__builtin_bit_cast(bf16x8, af[ks % 3]), __builtin_bit_cast(bf16x8, bu), acc[ct], 0, 0, 0);
      __builtin_amdgcn_sched_barrier(0);
      if constexpr (n + LA < 128) {
        if constexpr ((n + LA) % 8 == 0) __builtin_amdgcn_s_barrier();
        issue_pair(std::integral_constant<int, n + LA>{});
      }
      if constexpr (n >= 72 && (n & 3) == 0 && CV_PF2 + (n - 72) / 4 < 28) pf_w2(std::integral_constant<int, CV_PF2 + (n - 72) / 4>{});
      if constexpr (n >= 120 && n < 124)
        swv[n - 120] = __hip_atomic_load(sw + mt * 128 + (n - 120) * 32 + r32, __ATOMIC_RELAXED, __HIP_MEMORY_SCOPE_AGENT);
      __builtin_amdgcn_sched_barrier(0);
    });
    STAMP(KID, 6);
    for (int it = 0; it < (1 << 20); ++it) {
      if (__all(swv[0] != 0u && swv[1] != 0u && swv[2] != 0u && swv[3] != 0u)) break;
      __builtin_amdgcn_s_sleep(1);
#pragma unroll
      for (int ct = 0; ct < 4; ++ct)
        swv[ct] = __hip_atomic_load(sw + mt * 128 + ct * 32 + r32, __ATOMIC_RELAXED, __HIP_MEMORY_SCOPE_AGENT);
    }
    asm volatile("" : "+v"(swv[0]), "+v"(swv[1]), "+v"(swv[2]), "+v"(swv[3]));
#pragma unroll
    for (int q = 0; q < 4; ++q) ws4[q] = *(const f32x4*)(smem + 9 * 16384 + 256 + (wave * 32 + 8 * q + 4 * hh) * 4);
#pragma unroll
    for (int ct = 0; ct < 4; ++ct) {
      const float rs = __builtin_amdgcn_rcpf(bf_hi(swv[ct]));
      const float cm = -bf_lo(swv[ct]) * rs;
      char* dst = hbuf + ((size_t)((mt * 4 + ct) * 112 + ft * 2) * 64 + lane) * 16;
      const f32x2 c2 = {cm, cm}, r2 = {rs, rs};
#pragma unroll
      for (int s = 0; s < 2; ++s) {
        u32x4 o;
#pragma unroll
        for (int j = 0; j < 4; ++j) {
          const int reg = 8 * s + 2 * j;
          const f32x2 wsum = {ws4[reg >> 2][reg & 3], ws4[reg >> 2][(reg & 3) + 1]};
          const f32x2 a2 = {acc[ct][reg], acc[ct][reg + 1]};
          const f32x2 v2 = __builtin_elementwise_fma(a2, r2, c2 * wsum);
          const s16x2 pv = __builtin_bit_cast(s16x2, __builtin_convertvector(v2, bf16x2));
          const s16x2 z = {0, 0};
          o[j] = __builtin_bit_cast(unsigned, __builtin_elementwise_max(pv, z));
        }
        st16_sc1(dst + s * 1024, o);
      }
    }
    STAMP(KID, 7);
    wait_vm<0>();
    if (lane == 0) {
      const unsigned old = __hip_atomic_fetch_add(lcnt, 1u, __ATOMIC_RELAXED, __HIP_MEMORY_SCOPE_WORKGROUP);
      if (old == 3u) __hip_atomic_store(flags + (mt * 16 + nt) * FLS, 1u, __ATOMIC_RELAXED, __HIP_MEMORY_SCOPE_AGENT);
    }
    STAMP(KID, 8);
    static_for<0, 8>([&](auto J) { pf_xr(J); });
  }
  pin7(wr); pin7(wr + 7); pin7(wr + 14); pin7(wr + 21);
  {
    unsigned x0_ = xr[0], x1_ = xr[1], x2_ = xr[2], x3_ = xr[3], x4_ = xr[4], x5_ = xr[5], x6_ = xr[6], x7_ = xr[7];
    asm volatile("" : "+v"(x0_), "+v"(x1_), "+v"(x2_), "+v"(x3_), "+v"(x4_), "+v"(x5_), "+v"(x6_), "+v"(x7_));
    xr[0] = x0_; xr[1] = x1_; xr[2] = x2_; xr[3] = x3_; xr[4] = x4_; xr[5] = x5_; xr[6] = x6_; xr[7] = x7_;
  }
  __builtin_amdgcn_s_barrier();
  if (wave8 == 7) {
    const unsigned* fp = flags + ((mt2 >> 1) * 16 + (lane < 14 ? lane : 0)) * FLS;
    for (int it = 0; it < (1 << 20); ++it) {
      const unsigned f = __hip_atomic_load(fp, __ATOMIC_RELAXED, __HIP_MEMORY_SCOPE_AGENT);
      if (__all(f != 0u)) break;
      __builtin_amdgcn_s_sleep(POLL_SLEEP);
    }
  }
  asm volatile("s_waitcnt lgkmcnt(0)" ::: "memory");
  __builtin_amdgcn_s_barrier();
  STAMP(KID, 9);
  const char* hsrc = hbuf + (size_t)(mt2 * 2) * 112 * 1024 + wave8 * 1024 + lane * 16;
  char* dstb = smem + wave8 * 2048;
  auto issue2 = [&](int c) {
#pragma unroll
    for (int mi = 0; mi < 2; ++mi)
      __builtin_amdgcn_global_load_lds((const unsigned*)(hsrc + (size_t)mi * 112 * 1024 + c * 8192),
                                       (LAS unsigned*)(dstb + (c % G2_R) * 16384 + mi * 1024), 16, 0, 16  );
  };
  f32x16 acc[2];
#pragma unroll
  for (int i = 0; i < 16; ++i) { acc[0][i] = 0.f; acc[1][i] = 0.f; }
  const char* rdh = smem + kq * 4096 + lane * 16;
  bf16x8 hf[2][4];
#pragma unroll
  for (int c = 0; c < g2_iss(0); ++c) issue2(c);
  auto mainloop = [&](auto GRP) {
    constexpr int grp = GRP;
    static_for<0, 15>([&](auto C) {
      constexpr int c = C;
      if constexpr (c < 14) {
        wait_vm_lgkm0<2 * (g2_iss(c) - (c + 1))>();
        __builtin_amdgcn_s_barrier();
        if constexpr (c >= 1) pin4(hf[(c - 1) & 1]);
        if constexpr (c == 0) STAMP(KID, 10);
#pragma unroll
        for (int e = 0; e < 4; ++e) hf[c & 1][e] = *(const bf16x8*)(rdh + (c % G2_R) * 16384 + e * 1024);
      }
      auto mem = [&]() {
        if constexpr (c < 14) {
#pragma unroll
          for (int cn = g2_iss(c); cn < g2_iss(c + 1); ++cn) issue2(cn);
        }
      };
      auto mma = [&]() {
        if constexpr (c >= 1) {
          constexpr int pc = c - 1;
#pragma unroll
          for (int j = 0; j < 2; ++j)
#pragma unroll
            for (int mi = 0; mi < 2; ++mi)
              acc[mi] = __builtin_amdgcn_mfma_f32_32x32x16_bf16(__builtin_bit_cast(bf16x8, wr[2 * pc + j]), hf[pc & 1][j * 2 + mi], acc[mi], 0, 0, 0);
        }
      };
      __builtin_amdgcn_sched_barrier(0);
      if constexpr (grp == 0) { mem(); __builtin_amdgcn_sched_barrier(0); mma(); }
      else { mma(); __builtin_amdgcn_sched_barrier(0); mem(); }
      __builtin_amdgcn_sched_barrier(0);
    });
  };
  if (wave8 < 4) mainloop(std::integral_constant<int, 0>{});
  else mainloop(std::integral_constant<int, 1>{});
  STAMP(KID, 11);
  char* red = smem + 5 * 16384;
  {
#pragma unroll
    for (int pc = 0; pc < 4; ++pc) {
      if (pc != kq) {
        const int slot = (kq > pc) ? kq - 1 : kq;
        char* dst = red + ((pc * 2 + ni) * 3 + slot) * 2048 + lane * 16;
        f32x4 g0, g1;
#pragma unroll
        for (int i = 0; i < 4; ++i) { g0[i] = acc[pc >> 1][8 * (pc & 1) + i]; g1[i] = acc[pc >> 1][8 * (pc & 1) + 4 + i]; }
        *(f32x4*)dst = g0;
        *(f32x4*)(dst + 1024) = g1;
      }
    }
  }
  float mine[8];
#pragma unroll
  for (int i = 0; i < 8; ++i) {
    const float a0 = kh_f ? acc[0][8 + i] : acc[0][i];
    const float a1 = kh_f ? acc[1][8 + i] : acc[1][i];
    mine[i] = mi_f ? a1 : a0;
  }
  __syncthreads();
  {
    const char* src = red + (wave8 * 3) * 2048 + lane * 16;
#pragma unroll
    for (int sl = 0; sl < 3; ++sl) {
      const f32x4 o0 = *(const f32x4*)(src + sl * 2048);
      const f32x4 o1 = *(const f32x4*)(src + sl * 2048 + 1024);
#pragma unroll
      for (int i = 0; i < 4; ++i) { mine[i] += o0[i]; mine[4 + i] += o1[i]; }
    }
  }
  float xo[8];
  float s1 = 0.f, s2 = 0.f;
#pragma unroll
  for (int i = 0; i < 8; ++i) {
    const float v = rbf(__uint_as_float(((unsigned)xr[i]) << 16) + rbf(mine[i]));
    xo[i] = v;
    s1 += v; s2 += v * v;
  }
  unsigned short* xout = p.xout;
  if (LAYER == 1) {
#pragma unroll
    for (int q = 0; q < 2; ++q)
      *(u32x2*)(xout + (size_t)tok * D_ + dbase + 8 * q) =
          u32x2{pk_bf16(xo[4 * q], xo[4 * q + 1]), pk_bf16(xo[4 * q + 2], xo[4 * q + 3])};
  } else {
#pragma unroll
    for (int i = 0; i < 8; ++i)
      xout[(size_t)(dbase + (i & 3) + 8 * (i >> 2)) * L_ + tok] = (unsigned short)(__float_as_uint(xo[i]) >> 16);
    s1 += __shfl_xor(s1, 32, 64);
    s2 += __shfl_xor(s2, 32, 64);
    float* sred = (float*)(smem + 8 * 16384);
    sred[wave8 * 64 + lane] = hh ? s2 : s1;
    __syncthreads();
    if (wave8 < 2) {
      const int w0 = wave8 * 4;
      const float t = sred[w0 * 64 + lane] + sred[(w0 + 1) * 64 + lane] + sred[(w0 + 2) * 64 + lane] + sred[(w0 + 3) * 64 + lane];
      atomicAdd((float*)(ws + WS_ST1) + 2 * (mt2 * 64 + wave8 * 32 + r32) + hh, t);
    }
  }
  STAMP(KID, 15);
}

extern "C" void kernel_launch(void* const* d_in, const int* in_sizes, int n_in,
                              void* d_out, int out_size, void* d_ws, size_t ws_size,
                              hipStream_t stream) {
  const unsigned short* x0 = (const unsigned short*)d_in[0];
  const unsigned short* logA = (const unsigned short*)d_in[1];
  const unsigned short* Aim = (const unsigned short*)d_in[2];
  const unsigned short* Cre = (const unsigned short*)d_in[3];
  const unsigned short* Cim = (const unsigned short*)d_in[4];
  const unsigned short* logdt = (const unsigned short*)d_in[5];
  const unsigned short* dskip = (const unsigned short*)d_in[6];
  const unsigned short* w1 = (const unsigned short*)d_in[8];
  const unsigned short* w2 = (const unsigned short*)d_in[9];
  char* ws = (char*)d_ws;
  (void)hipFuncSetAttribute((const void*)s4d_layer_kernel, hipFuncAttributeMaxDynamicSharedMemorySize, LAYER_LDS);
  prep_kernel<<<PREP_BLOCKS, 256, 0, stream>>>(x0, logA, Aim, Cre, Cim, logdt, w1, w2, ws);
  LayerArgs a0 = {(const unsigned short*)(ws + WS_XT0), dskip, (unsigned short*)(ws + WS_XT1), ws + WS_H,
                  (unsigned short*)(ws + WS_XT2), ws, 0};
  LayerArgs a1 = {(const unsigned short*)(ws + WS_XT2), dskip, (unsigned short*)(ws + WS_XT3), ws + WS_H2,
                  (unsigned short*)d_out, ws, 1};
  s4d_layer_kernel<<<256, 512, LAYER_LDS, stream>>>(a0);
  s4d_layer_kernel<<<256, 512, LAYER_LDS, stream>>>(a1);
}
```

```cpp
#include <hip/hip_runtime.h>
#include <hip/hip_bf16.h>
#include <stdint.h>

typedef __attribute__((ext_vector_type(8))) __bf16 bf16x8;
typedef __attribute__((ext_vector_type(2))) __bf16 bf16x2;
typedef __attribute__((ext_vector_type(4))) short s16x4;
typedef __attribute__((ext_vector_type(2))) short s16x2;
typedef __attribute__((ext_vector_type(8))) short s16x8;
typedef __attribute__((ext_vector_type(8))) _Float16 f16x8;
typedef __attribute__((ext_vector_type(2))) _Float16 f16x2;
typedef __attribute__((ext_vector_type(2))) float f32x2;
typedef __attribute__((ext_vector_type(4))) float f32x4;
typedef __attribute__((ext_vector_type(16))) float f32x16;
typedef __attribute__((ext_vector_type(4))) unsigned u32x4;
typedef __attribute__((ext_vector_type(2))) unsigned u32x2;
#define LAS __attribute__((address_space(3)))
#define DI __device__ __forceinline__

constexpr int L_ = 2048, D_ = 512, N_ = 128, F_ = 1792;

constexpr size_t MiB = 1024 * 1024;
#define FLS 32
#define CREP 8
constexpr size_t WS_ST1  = 0;
constexpr size_t WS_SW   = WS_ST1 + 16384;
constexpr size_t WS_W1S  = WS_SW + 16384;
constexpr size_t WS_FL   = WS_W1S + 8192;
constexpr size_t WS_CF   = WS_FL + 2048 * FLS;
constexpr size_t WS_ZBYTES = WS_CF + 4 * CREP * 16 * 128;
constexpr size_t WS_SW0  = 1 * MiB;
constexpr size_t WS_W1P  = 1 * MiB + 65536;
constexpr size_t WS_XT0  = 2 * MiB;
constexpr size_t WS_XT1  = 4 * MiB;
constexpr size_t WS_XT2  = 6 * MiB;
constexpr size_t WS_XT3  = 8 * MiB;
constexpr size_t WS_W1T  = 10 * MiB;
constexpr size_t WS_W2T  = 12 * MiB;
constexpr size_t WS_H    = 14 * MiB;
constexpr size_t WS_H2   = 24 * MiB;

#define STAMP(kid, i)
#define STAMPT(kid, i, T)
DI float bf_lo(unsigned u) { return __uint_as_float(u << 16); }
DI float bf_hi(unsigned u) { return __uint_as_float(u & 0xffff0000u); }
DI unsigned pk_bf16(float a, float b) {
  f32x2 v = {a, b};
  return __builtin_bit_cast(unsigned, __builtin_convertvector(v, bf16x2));
}
DI unsigned pk_f16(float a, float b) {
  f32x2 v = {a, b};
  return __builtin_bit_cast(unsigned, __builtin_convertvector(v, f16x2));
}
DI float rbf(float a) { return bf_lo(pk_bf16(a, 0.f)); }
DI float ld_bf(const unsigned short* p) { return __uint_as_float(((unsigned)*p) << 16); }
DI unsigned perm_lo(unsigned hi, unsigned lo) { return __builtin_amdgcn_perm(hi, lo, 0x05040100u); }
DI unsigned perm_hi(unsigned hi, unsigned lo) { return __builtin_amdgcn_perm(hi, lo, 0x07060302u); }
template <int CTRL> DI float dpp_add(float v) {
  return v + __uint_as_float(__builtin_amdgcn_update_dpp(0u, __float_as_uint(v), CTRL, 0xf, 0xf, true));
}
DI float wave_sum(float v) {
  v = dpp_add<0xB1>(v);
  v = dpp_add<0x4E>(v);
  v = dpp_add<0x141>(v);
  v = dpp_add<0x140>(v);
  const unsigned u = __float_as_uint(v);
  return (__uint_as_float(__builtin_amdgcn_readlane(u, 0)) + __uint_as_float(__builtin_amdgcn_readlane(u, 16))) +
         (__uint_as_float(__builtin_amdgcn_readlane(u, 32)) + __uint_as_float(__builtin_amdgcn_readlane(u, 48)));
}
#define ST_FLAGS ""
DI void st16(void* p, u32x4 v) { asm volatile("global_store_dwordx4 %0, %1, off " ST_FLAGS "\n\ts_nop 1" ::"v"(p), "v"(v) : "memory"); }
DI void st8(void* p, u32x2 v) { asm volatile("global_store_dwordx2 %0, %1, off " ST_FLAGS "\n\ts_nop 1" ::"v"(p), "v"(v) : "memory"); }
DI void st2(void* p, unsigned v) { asm volatile("global_store_short %0, %1, off " ST_FLAGS "\n\ts_nop 1" ::"v"(p), "v"(v) : "memory"); }
template <int N> DI void wait_vm() { asm volatile("s_waitcnt vmcnt(%0)" ::"n"(N) : "memory"); }
template <int OFF> DI void gload16(u32x4& dst, const char* p) {
  asm volatile("global_load_dwordx4 %0, %1, off offset:%2" : "=v"(dst) : "v"(p), "n"(OFF));
}
template <int N> DI void wait_vm4(u32x4& a, u32x4& b, u32x4& c, u32x4& d) {
  asm volatile("s_waitcnt vmcnt(%4)" : "+v"(a), "+v"(b), "+v"(c), "+v"(d) : "n"(N) : "memory");
}
template <int OFF> DI void tr_read(u32x2& d, unsigned addr) {
  asm volatile("ds_read_b64_tr_b16 %0, %1 offset:%2" : "=v"(d) : "v"(addr), "n"(OFF));
}
template <int N> DI void wait_lgkm8(u32x2* b) {
  asm volatile("s_waitcnt lgkmcnt(%8)"
               : "+v"(b[0]), "+v"(b[1]), "+v"(b[2]), "+v"(b[3]), "+v"(b[4]), "+v"(b[5]), "+v"(b[6]), "+v"(b[7])
               : "n"(N) : "memory");
}
template <int N> DI void wait_vm_lgkm0() { asm volatile("s_waitcnt vmcnt(%0) lgkmcnt(0)" ::"n"(N) : "memory"); }
template <int I, int E, class F> DI void static_for(F&& f) {
  if constexpr (I < E) { f(std::integral_constant<int, I>{}); static_for<I + 1, E>(f); }
}

constexpr int TROW = 528;

__host__ __device__ constexpr int tab_w(int idx) {
  return idx < 16 ? idx : (idx < 40 ? ((8 + ((idx - 16) & 7)) << (1 + ((idx - 16) >> 3))) : 128);
}
__host__ __device__ constexpr int ilog2c(int w) { return w <= 1 ? 0 : 1 + ilog2c(w >> 1); }
__host__ __device__ constexpr int tab_idx(int w) {
  return w < 16 ? w : (w == 128 ? 40 : 16 + 8 * (ilog2c(w) - 4) + ((w >> (ilog2c(w) - 3)) - 8));
}
DI int tab_idx_rt(int w) {
  if (w < 16) return w;
  int lg = 31 - __clz(w);
  return 16 + 8 * (lg - 4) + ((w >> (lg - 3)) - 8);
}

DI void retile_load(int u, const unsigned short* __restrict__ w1, const unsigned short* __restrict__ w2, u32x2* r, size_t& dst) {
  if (u < 28672) {
    const int fg = u % 448, o = u / 448;
    const int f = 4 * fg;
#pragma unroll
    for (int j = 0; j < 8; ++j) r[j] = *(const u32x2*)(w1 + (size_t)(8 * o + j) * F_ + f);
    dst = WS_W1T + ((size_t)(((f >> 5) * 32 + (o >> 1)) * 64 + (o & 1) * 32 + (f & 31))) * 16;
  } else {
    const int v = u - 28672;
    const int dg = v & 127, o = v >> 7;
    const int dd = 4 * dg, ks = o >> 1, hh = o & 1;
#pragma unroll
    for (int j = 0; j < 8; ++j)
      r[j] = *(const u32x2*)(w2 + (size_t)(16 * ks + 8 * (j >> 2) + 4 * hh + (j & 3)) * D_ + dd);
    dst = WS_W2T + ((size_t)(((dd >> 5) * 112 + ks) * 64 + hh * 32 + (dd & 31))) * 16;
  }
}
DI void st16_sc1(void* p, u32x4 v) { asm volatile("global_store_dwordx4 %0, %1, off sc1\n\ts_nop 1" ::"v"(p), "v"(v) : "memory"); }
DI void st8_sc1(void* p, u32x2 v) { asm volatile("global_store_dwordx2 %0, %1, off sc1\n\ts_nop 1" ::"v"(p), "v"(v) : "memory"); }
template <int K> DI unsigned quad_bcast(unsigned v) { return __builtin_amdgcn_update_dpp(0u, v, K * 0x55, 0xf, 0xf, true); }
DI void retile_store(const u32x2* r, char* ws, size_t dst, int lane, bool active) {
  u32x4 pc[4];
#pragma unroll
  for (int c = 0; c < 2; ++c) {
    pc[2 * c] = u32x4{perm_lo(r[1][c], r[0][c]), perm_lo(r[3][c], r[2][c]), perm_lo(r[5][c], r[4][c]), perm_lo(r[7][c], r[6][c])};
    pc[2 * c + 1] = u32x4{perm_hi(r[1][c], r[0][c]), perm_hi(r[3][c], r[2][c]), perm_hi(r[5][c], r[4][c]), perm_hi(r[7][c], r[6][c])};
  }
  const int ql = lane & 3;
  char* base = ws + dst - (size_t)ql * 64 + ql * 16;
  static_for<0, 4>([&](auto KK) {
    constexpr int k = KK;
    u32x4 o;
#pragma unroll
    for (int e = 0; e < 4; ++e) {
      const unsigned v0 = quad_bcast<k>(pc[0][e]), v1 = quad_bcast<k>(pc[1][e]), v2 = quad_bcast<k>(pc[2][e]), v3 = quad_bcast<k>(pc[3][e]);
      o[e] = ql == 0 ? v0 : (ql == 1 ? v1 : (ql == 2 ? v2 : v3));
    }
    if (active) st16_sc1(base + k * 64, o);
  });
}

#define LAYER_CODE 33792
#define PREP_CODE 14336
template <int BYTES> DI unsigned warm_code(const char* pc, int t) {
  const int o = t * 128;
  return *(const __attribute__((address_space(1))) unsigned*)(uintptr_t)(pc + (o < BYTES ? o : 0));
}
constexpr int CV_CS = 260 * 16;
constexpr int CV_KCH = 8 * CV_CS;
constexpr int CV_U0 = 2 * CV_KCH;
constexpr int CV_UCH = (256 + 2048) * 2;
constexpr int CV_XS = CV_U0 + 2 * CV_UCH;
constexpr int CV_RED = CV_XS + 2 * 8192;
constexpr int G1_R = 9;
constexpr int G2_R = 9;
constexpr int LDS_CNT = 9 * 16384;
constexpr int LDS_G = LDS_CNT + 1024;
constexpr int LDS_KC = 90 * 1024;
constexpr int LAYER_LDS = LDS_G + 2 * 4112;
static_assert(2 * 82 * TROW <= LDS_KC && LAYER_LDS <= 160 * 1024, "LDS");
static_assert(CV_RED + 16384 <= 9 * 16384, "conv LDS");
#define G1_W0 5
#define G2_W0 6
#define CV_PF1 16
#define CV_PF2 22
#define W1PRE 5
#define ST_AT 40
#define PUB_AT 40
#define POLL_SLEEP 2
static_assert(G1_W0 <= G1_R - 2 && W1PRE <= G1_R, "ring safety");
__host__ __device__ constexpr int g2_iss(int i) {
  int v = G2_W0 + i;
  return v < 14 ? v : 14;
}
__host__ __device__ constexpr int g1_young(int c) {
  int n = 0;
  for (int j = c + 1; j < c + G1_W0 && j < 16; ++j) n += (j >= W1PRE) ? 4 : 2;
  return n;
}
template <int N> DI void wait_lgkm2(u32x2& a, u32x2& b) {
  asm volatile("s_waitcnt lgkmcnt(%2)" : "+v"(a), "+v"(b) : "n"(N) : "memory");
}
template <int N> DI void wait_lgkm3(u32x2& a, u32x2& b, u32x4& c) {
  asm volatile("s_waitcnt lgkmcnt(%3)" : "+v"(a), "+v"(b), "+v"(c) : "n"(N) : "memory");
}
template <int OFF> DI void lds_read16(u32x4& d, unsigned addr) {
  asm volatile("ds_read_b128 %0, %1 offset:%2" : "=v"(d) : "v"(addr), "n"(OFF));
}
DI void pin4(bf16x8* f) { asm volatile("" : "+v"(f[0]), "+v"(f[1]), "+v"(f[2]), "+v"(f[3])); }
DI void pin7(u32x4* f) { asm volatile("" : "+v"(f[0]), "+v"(f[1]), "+v"(f[2]), "+v"(f[3]), "+v"(f[4]), "+v"(f[5]), "+v"(f[6])); }
DI u32x4 ld16_sc1_wait(const void* p) {
  u32x4 v;
  asm volatile("global_load_dwordx4 %0, %1, off sc1\n\ts_waitcnt vmcnt(0)" : "=&v"(v) : "v"(p) : "memory");
  return v;
}

struct LayerArgs {
  const unsigned short *x0, *logA, *Aim, *Cre, *Cim, *logdt, *dskip, *w1, *w2;
  unsigned short* out;
  char* ws;
};

extern "C" __global__ __launch_bounds__(512, 2) void s4d_layer_kernel(LayerArgs p) {
  extern __shared__ __attribute__((aligned(16))) char smem[];
  {
    const char* pc0 = (const char*)__builtin_amdgcn_s_getpc();
    const int tid = threadIdx.x, lane = tid & 63;
    const int wave8 = __builtin_amdgcn_readfirstlane(tid >> 6);
    const int b = blockIdx.x;
    char* ws = p.ws;
    STAMP(0, 0);
    const int d0 = 2 * ((b & 7) * 32 + (b >> 3));
    const int ch = wave8 >> 2, d = d0 + ch;
    const int isH = (wave8 >> 1) & 1;
    const int n = (wave8 & 1) * 64 + lane;
    const int tk = b * 8 + wave8;
    const u32x4 row = *(const u32x4*)(p.x0 + (size_t)tk * D_ + lane * 8);
    unsigned e0 = p.logdt[d], e1 = p.logA[d * N_ + n], e2 = p.Aim[d * N_ + n];
    unsigned e3 = p.Cre[d * N_ + lane], e4 = p.Cim[d * N_ + lane], e5 = p.Cre[d * N_ + 64 + lane], e6 = p.Cim[d * N_ + 64 + lane];
    if (tid == 0) { *(unsigned*)(smem + LDS_CNT) = 0u; *(unsigned*)(smem + LDS_CNT + 4) = 0u; }
    __builtin_amdgcn_sched_barrier(0);
    asm volatile("s_waitcnt lgkmcnt(0)" ::: "memory");
    __builtin_amdgcn_s_barrier();
    __builtin_amdgcn_sched_barrier(0);
    u32x4 r[8];
    u32x2 rt[8];
    size_t rdst = 0;
    unsigned cw = 0u;
    const int tt = b >> 3, ctile = b & 7, tg = lane & 7, cg = lane >> 3;
    __builtin_amdgcn_sched_barrier(0);
    u32x4 rowp = row;
    asm volatile("" : "+v"(rowp), "+v"(e0), "+v"(e1), "+v"(e2), "+v"(e3), "+v"(e4), "+v"(e5), "+v"(e6));
    const float q0 = bf_lo(e0), q1 = bf_lo(e1), q2 = bf_lo(e2), q3 = bf_lo(e3), q4_ = bf_lo(e4), q5 = bf_lo(e5), q6 = bf_lo(e6);
    unsigned sword;
    {
      float sm = 0.f;
#pragma unroll
      for (int c = 0; c < 4; ++c) sm += bf_lo(rowp[c]) + bf_hi(rowp[c]);
      const float mean = wave_sum(sm) * (1.f / D_);
      float q = 0.f;
#pragma unroll
      for (int c = 0; c < 4; ++c) {
        const float a = bf_lo(rowp[c]) - mean, bb = bf_hi(rowp[c]) - mean;
        q += a * a + bb * bb;
      }
      const float var = wave_sum(q) * (1.f / D_);
      const float sdv = sqrtf(rbf(rbf(var) + 1e-5f));
      sword = pk_bf16(mean, sdv);
    }
    STAMP(0, 1);
    const float dt = rbf(expf(q0));
    const float ar = -rbf(expf(q1)) * dt;
    const float ai = q2 * (isH ? -dt : dt);
    const float c0r = q3 * dt, c0i = q4_ * dt, c1r = q5 * dt, c1i = q6 * dt;
    const float S = wave_sum(2.f * (fabsf(c0r) + fabsf(c0i) + fabsf(c1r) + fabsf(c1i)));
    int ex = 0;
    (void)frexpf(S, &ex);
    if (!(S > 0.f) || !(S < 3.0e38f)) ex = 15;
    ex = max(-100, min(ex, 120));
    const float scale = ldexpf(1.f, 15 - ex);
    if ((tid & 255) == 0) *(float*)(smem + LDS_CNT + 8 + ch * 4) = ldexpf(1.f, ex - 15);
    const float cr = (wave8 & 1) ? c1r : c0r, ci = (wave8 & 1) ? c1i : c0i;
    const float base = isH ? 16.f : 1.f;
    const float er = ar * 1.44269504088896340736f * base;
    const float IH = 0.15915494309189533577f;
    const float IL = (float)(0.15915494309189533577 - (double)0.15915494309189533577f);
    float fh = ai * IH;
    float fl = fmaf(ai, IH, -fh) + ai * IL;
    fh *= base; fl *= base;
    f32x2 Er2[8], Ei2[8];
#pragma unroll
    for (int k = 0; k < 8; ++k) {
      const float pw = (float)(1 << k);
      const float mag = __builtin_amdgcn_exp2f(er * pw);
      const float ph = __builtin_amdgcn_fractf(fh * pw) + fl * pw;
      const float re = mag * __builtin_amdgcn_cosf(ph), im = mag * __builtin_amdgcn_sinf(ph);
      Er2[k] = f32x2{re, re};
      Ei2[k] = f32x2{-im, im};
    }
    char* tabs = smem + ch * (82 * TROW);
    f32x2 tv[41];
    tv[0] = isH ? f32x2{2.f * scale * cr, -2.f * scale * ci} : f32x2{1.f, 0.f};
    char* trow = tabs + isH * (41 * TROW) + n * 4;
    *(unsigned*)trow = __builtin_bit_cast(unsigned, __builtin_convertvector(tv[0], f16x2));
    static_for<1, 41>([&](auto I) {
      constexpr int idx = I;
      constexpr int w = tab_w(idx);
      constexpr int lg = ilog2c(w);
      constexpr int ridx = tab_idx(w - (1 << lg));
      const f32x2 sw = __builtin_shufflevector(tv[ridx], tv[ridx], 1, 0);
      tv[idx] = __builtin_elementwise_fma(Ei2[lg], sw, Er2[lg] * tv[ridx]);
      *(unsigned*)(trow + idx * TROW) = __builtin_bit_cast(unsigned, __builtin_convertvector(tv[idx], f16x2));
      if constexpr (idx == 1) {
        if (wave8 < 4) {
          retile_load(b * 224 + (tid < 224 ? tid : (tid & 3)), p.w1, p.w2, rt, rdst);
        } else if (wave8 == 7) {
          const unsigned short* src = p.x0 + (size_t)(tt * 64 + tg * 8) * D_ + ctile * 64 + cg * 8;
#pragma unroll
          for (int j = 0; j < 8; ++j) r[j] = *(const u32x4*)(src + (size_t)j * D_);
        } else {
          cw = warm_code<LAYER_CODE>(pc0, tid - 256) ^ warm_code<LAYER_CODE>(pc0, tid - 64);
        }
      }
      if constexpr (idx == ST_AT) {
        unsigned cwp = cw;
        asm volatile("" : "+v"(cwp));
    if (wave8 < 4) {
      asm volatile("" : "+v"(rt[0]), "+v"(rt[1]), "+v"(rt[2]), "+v"(rt[3]), "+v"(rt[4]), "+v"(rt[5]), "+v"(rt[6]), "+v"(rt[7]));
      retile_store(rt, ws, rdst, lane, tid < 224);
      if (b < 128 && tid < 224) {
        f32x4 t = {0.f, 0.f, 0.f, 0.f};
#pragma unroll
        for (int j = 0; j < 8; ++j) t += f32x4{bf_lo(rt[j][0]), bf_hi(rt[j][0]), bf_lo(rt[j][1]), bf_hi(rt[j][1])};
        const int u = b * 224 + tid, fg = u % 448, o1 = u / 448;
        st16_sc1(ws + WS_W1P + (size_t)((fg >> 3) * 64 + o1) * 128 + (fg & 7) * 16, __builtin_bit_cast(u32x4, t));
      }
    } else if (wave8 == 7) {
      asm volatile("" : "+v"(r[0]), "+v"(r[1]), "+v"(r[2]), "+v"(r[3]), "+v"(r[4]), "+v"(r[5]), "+v"(r[6]), "+v"(r[7]));
      unsigned short* dst = (unsigned short*)(ws + WS_XT0) + (size_t)(ctile * 64 + cg * 8) * L_ + tt * 64 + tg * 8;
#pragma unroll
      for (int c = 0; c < 4; ++c) {
        st16_sc1(dst + (size_t)(2 * c) * L_, u32x4{perm_lo(r[1][c], r[0][c]), perm_lo(r[3][c], r[2][c]), perm_lo(r[5][c], r[4][c]), perm_lo(r[7][c], r[6][c])});
        st16_sc1(dst + (size_t)(2 * c + 1) * L_, u32x4{perm_hi(r[1][c], r[0][c]), perm_hi(r[3][c], r[2][c]), perm_hi(r[5][c], r[4][c]), perm_hi(r[7][c], r[6][c])});
      }
    }
        if (lane == 0) __hip_atomic_store((unsigned*)(ws + WS_SW0) + tk, sword, __ATOMIC_RELAXED, __HIP_MEMORY_SCOPE_AGENT);
      }
    });
    const int gz = tid & 255;
    int cix[8];
    {
      const int l0 = 2040 - gz * 8;
      const int sh = max((31 - __clz(l0 | 1)) - 7, 0);
      const int one = sh > 0 ? 1 : 0;
#pragma unroll
      for (int e = 0; e < 8; ++e) {
        const int l = l0 + 7 - e;
        cix[e] = 128 * sh + ((l + ((1 << sh) >> 1) - one + ((l >> sh) & one)) >> sh);
      }
    }
    STAMP(0, 2);
    asm volatile("s_waitcnt lgkmcnt(0)" ::: "memory");
    __builtin_amdgcn_s_barrier();
    _Float16* kc = (_Float16*)(smem + LDS_KC + ch * 1536);
    {
      const int sr = wave8 & 3, i16 = lane & 15, g = lane >> 4;
      const int hw = (sr == 0) ? i16 : (i16 < 8 ? ((8 + i16) << sr) : 128);
      const char* arow = tabs + 41 * TROW + tab_idx_rt(hw) * TROW + g * 16;
      const char* brow = tabs + tab_idx_rt(i16 << sr) * TROW + g * 16;
      f16x8 af[8], bf[8];
#pragma unroll
      for (int ks = 0; ks < 8; ++ks) { af[ks] = *(const f16x8*)(arow + ks * 64); bf[ks] = *(const f16x8*)(brow + ks * 64); }
      f32x4 k0 = {0.f, 0.f, 0.f, 0.f}, k1 = {0.f, 0.f, 0.f, 0.f};
#pragma unroll
      for (int ks = 0; ks < 8; ks += 2) {
        k0 = __builtin_amdgcn_mfma_f32_16x16x32_f16(af[ks], bf[ks], k0, 0, 0, 0);
        k1 = __builtin_amdgcn_mfma_f32_16x16x32_f16(af[ks + 1], bf[ks + 1], k1, 0, 0, 0);
      }
#pragma unroll
      for (int r = 0; r < 4; ++r) {
        const int i = 4 * g + r;
        const int cidx = (sr == 0) ? (16 * i + i16) : (256 + 128 * (sr - 1) + 16 * i + i16);
        const bool ok = (sr == 0) || (i < 8) || (sr == 3 && i == 8 && i16 == 0);
        if (ok) kc[cidx] = (_Float16)(k0[r] + k1[r]);
      }
    }
    asm volatile("s_waitcnt lgkmcnt(0)" ::: "memory");
    __builtin_amdgcn_s_barrier();
    {
      unsigned short o[8];
#pragma unroll
      for (int e = 0; e < 8; ++e) o[e] = ((const unsigned short*)kc)[cix[e]];
      *(u32x4*)(smem + LDS_G + ch * 4112 + gz * 16) =
          u32x4{(unsigned)o[0] | ((unsigned)o[1] << 16), (unsigned)o[2] | ((unsigned)o[3] << 16),
                (unsigned)o[4] | ((unsigned)o[5] << 16), (unsigned)o[6] | ((unsigned)o[7] << 16)};
    }
    wait_vm_lgkm0<0>();
    __builtin_amdgcn_s_barrier();
    if (tid < CREP)
      (void)__hip_atomic_fetch_add((unsigned*)(ws + WS_CF) + 3 * (CREP * 16 * 32) + (tid * 16 + (b >> 4)) * 32, 1u, __ATOMIC_RELAXED, __HIP_MEMORY_SCOPE_AGENT);
    STAMP(0, 15);
  }
  u32x4 wr[28];
#pragma unroll 1
  for (int LAYER = 0; LAYER < 2; ++LAYER) {
  int tid = threadIdx.x;
  asm volatile("" : "+v"(tid));
  const int lane = tid & 63;
  const int wave8 = __builtin_amdgcn_readfirstlane(tid >> 6);
  const int wave = wave8 & 3;
  const int bx = blockIdx.x & 7, bi = blockIdx.x >> 3;
  const int r32 = lane & 31, hh = lane >> 5;
  char* ws = p.ws;
  const char* w1t = ws + WS_W1T;
  const char* w2t = ws + WS_W2T;
  const int mt2 = bx * 4 + (bi & 3), nt2 = bi >> 2;
  const int ni = wave8 & 1, kq = wave8 >> 1;
  const int mi_f = kq >> 1, kh_f = kq & 1;
  const int tok = mt2 * 64 + mi_f * 32 + r32;
  const int dbase = nt2 * 64 + ni * 32 + 4 * hh + 16 * kh_f;
  unsigned short xr[8];
  const auto rs_w2 = __builtin_amdgcn_make_buffer_rsrc((void*)w2t, 0, F_ * D_ * 2, 0x00020000);
  const int w2off = (nt2 * 2 + ni) * 112 * 1024 + kq * 2048 + lane * 16;
  auto pf_w2 = [&](auto J) {
    constexpr int j = J;
    wr[j] = __builtin_amdgcn_raw_buffer_load_b128(rs_w2, w2off + (j & 1) * 1024, (j >> 1) * 8192, 16  );
  };
  const int mt = (bx >> 1) * 4 + (bi & 3), nt = (bx & 1) * 7 + (bi >> 2);
  const int ft = nt * 4 + wave;
  const int KID = 1 + 3 * LAYER;
  const unsigned short* xin = (const unsigned short*)(ws + (LAYER ? WS_XT2 : WS_XT0));
  unsigned short* xt = (unsigned short*)(ws + (LAYER ? WS_XT3 : WS_XT1));
  char* hbuf = ws + (LAYER ? WS_H2 : WS_H);
  unsigned* sw = (unsigned*)(ws + WS_SW) + LAYER * 2048;
  unsigned* flags = (unsigned*)(ws + WS_FL) + LAYER * 256 * FLS;
  unsigned* cflags = (unsigned*)(ws + WS_CF) + LAYER * (CREP * 16 * 32);
  unsigned* eflags = (unsigned*)(ws + WS_CF) + (LAYER ? 2 : 3) * (CREP * 16 * 32);
  const unsigned short* xrsrc = xt + ((size_t)(tok >> 7) * D_ + dbase) * 128 + (tok & 127);
  auto pf_xr = [&](auto J) {
    constexpr int j = J;
    xr[j] = __hip_atomic_load(xrsrc + ((j & 3) + 8 * (j >> 2)) * 128, __ATOMIC_RELAXED, __HIP_MEMORY_SCOPE_AGENT);
  };
  STAMP(KID, 0);
  {
    const int d0 = 2 * (bx * 32 + bi);
    const int gz = tid & 255, gc = tid >> 8;
    const int t4 = tid * 4;
    const int ch = wave8 >> 2, half = wave8 & 1, qs = (wave8 >> 1) & 1;
    float xf[2][4], m[4], sd[4];
    const unsigned dsk2 = *(const unsigned*)(p.dskip + d0);
    const u32x4 ga = *(const u32x4*)(smem + LDS_G + gc * 4112 + gz * 16);
    const u32x4 gb = *(const u32x4*)(smem + LDS_G + gc * 4112 + gz * 16 + 16);
    const float kscale = *(const float*)(smem + LDS_CNT + 8 + ch * 4);
    unsigned zz = 0u;
    asm volatile("" : "+v"(zz));
    if (tid < 16) *(u32x4*)(smem + (tid >> 3) * CV_KCH + (tid & 7) * CV_CS + 256 * 16) = u32x4{zz, zz, zz, zz};
    if (tid >= 64 && tid < 128) {
      const int q = tid - 64;
      *(u32x4*)(smem + CV_U0 + (q >> 5) * CV_UCH + (q & 31) * 16) = u32x4{zz, zz, zz, zz};
    }
    if (tid == 128) *(unsigned*)(smem + LDS_CNT) = 0u;
    {
      const unsigned msk = (gz < 255) ? 0xffffffffu : 0u;
      unsigned w[8] = {ga[0], ga[1], ga[2], ga[3], gb[0] & msk, gb[1] & msk, gb[2] & msk, gb[3] & msk};
      unsigned o[7];
#pragma unroll
      for (int k = 0; k < 7; ++k) o[k] = __builtin_amdgcn_alignbit(w[k + 1], w[k], 16);
      char* dst = smem + gc * CV_KCH + gz * 16;
#pragma unroll
      for (int mm = 0; mm < 4; ++mm) {
        *(u32x4*)(dst + (2 * mm) * CV_CS) = u32x4{w[mm], w[mm + 1], w[mm + 2], w[mm + 3]};
        *(u32x4*)(dst + (2 * mm + 1) * CV_CS) = u32x4{o[mm], o[mm + 1], o[mm + 2], o[mm + 3]};
      }
    }
    if (wave8 == 7) {
      const unsigned* cp = eflags + ((blockIdx.x % CREP) * 16 + (lane & 15)) * 32;
      for (int it = 0; it < (1 << 20); ++it) {
        const unsigned f = __hip_atomic_load(cp, __ATOMIC_RELAXED, __HIP_MEMORY_SCOPE_AGENT);
        if (__all(f >= 16u)) break;
        __builtin_amdgcn_s_sleep(POLL_SLEEP);
      }
    }
    __builtin_amdgcn_s_barrier();
    STAMP(KID, 12);
    const auto rs_st = __builtin_amdgcn_make_buffer_rsrc((void*)(ws + (LAYER ? WS_ST1 : WS_SW0)), 0, 16384, 0x00020000);
    const auto rs_x = __builtin_amdgcn_make_buffer_rsrc((void*)xin, 0, L_ * D_ * 2, 0x00020000);
    const u32x4 swv0 = __builtin_amdgcn_raw_buffer_load_b128(rs_st, LAYER ? t4 * 8 : t4 * 4, 0, 16  );
    const f32x4 st0 = __builtin_bit_cast(f32x4, swv0);
    const f32x4 st1 = __builtin_bit_cast(f32x4, __builtin_amdgcn_raw_buffer_load_b128(rs_st, LAYER ? t4 * 8 + 16 : 0, 0, 16));
    const u32x2 xv0 = __builtin_amdgcn_raw_buffer_load_b64(rs_x, (d0 * L_ + t4) * 2, 0, 16);
    const u32x2 xv1 = __builtin_amdgcn_raw_buffer_load_b64(rs_x, ((d0 + 1) * L_ + t4) * 2, 0, 16);
#pragma unroll
    for (int r = 0; r < 4; ++r) {
      {
        const unsigned wv = swv0[r];
        const float a = (r < 2) ? st0[(r & 1) * 2] : st1[(r & 1) * 2];
        const float bq = (r < 2) ? st0[(r & 1) * 2 + 1] : st1[(r & 1) * 2 + 1];
        const float mean = a * (1.f / D_), var = fmaxf(bq * (1.f / D_) - mean * mean, 0.f);
        const float m1 = rbf(mean), sd1 = rbf(__builtin_amdgcn_sqrtf(rbf(rbf(var) + 1e-5f)));
        m[r] = LAYER ? m1 : bf_lo(wv);
        sd[r] = LAYER ? sd1 : bf_hi(wv);
      }
      const unsigned w0 = xv0[r >> 1], w1_ = xv1[r >> 1];
      xf[0][r] = (r & 1) ? bf_hi(w0) : bf_lo(w0);
      xf[1][r] = (r & 1) ? bf_hi(w1_) : bf_lo(w1_);
    }
    {
      float rs[4];
#pragma unroll
      for (int r = 0; r < 4; ++r) rs[r] = __builtin_amdgcn_rcpf(sd[r]);
#pragma unroll
      for (int c = 0; c < 2; ++c) {
        const float dsk = c ? bf_hi(dsk2) : bf_lo(dsk2);
        float uu[4];
        unsigned xs[4];
#pragma unroll
        for (int r = 0; r < 4; ++r) {
          const float x = xf[c][r];
          const float dd = rbf(x - m[r]);
          float q = dd * rs[r];
          q = fmaf(fmaf(-q, sd[r], dd), rs[r], q);
          uu[r] = rbf(q);
          xs[r] = pk_bf16(x, uu[r] * dsk);
        }
        *(u32x2*)(smem + CV_U0 + c * CV_UCH + (256 + t4) * 2) = u32x2{pk_f16(uu[0], uu[1]), pk_f16(uu[2], uu[3])};
        *(u32x4*)(smem + CV_XS + c * 8192 + t4 * 4) = u32x4{xs[0], xs[1], xs[2], xs[3]};
      }
    }
    __syncthreads();
    STAMP(KID, 1);
    const int j16 = lane & 15, g = lane >> 4;
    const int cc = (7 - j16) & 7;
    const int zl = (2031 - j16 - cc) / 8 + g;
    const char* abase = smem + ch * CV_KCH + cc * CV_CS + (zl - 32 * 7 - 4 * (4 * half + 3)) * 16;
    const char* bbase = smem + CV_U0 + ch * CV_UCH + 480 + 32 * j16 + 16 * g - 64 * (4 * half + 3);
    f32x4 acc[4];
#pragma unroll
    for (int q = 0; q < 4; ++q) acc[q] = f32x4{0.f, 0.f, 0.f, 0.f};
    auto body = [&](auto QS, auto PFF) {
      constexpr int S = QS;
      constexpr bool PF = PFF;
      constexpr int Q[4] = {S ? 6 : 7, S ? 5 : 4, S ? 2 : 3, S ? 1 : 0};
      constexpr int NF = S ? 7 : 8;
      f16x8 A[2][NF], B[2][NF];
      auto ld = [&](auto IT) {
        constexpr int it = IT;
#pragma unroll
        for (int k = 0; k < NF; ++k) {
          A[it & 1][k] = *(const f16x8*)(abase + (7 - k) * 512 + (3 - it) * 64);
          B[it & 1][NF - 1 - k] = *(const f16x8*)(bbase + (NF - 1 - k) * 512 + (3 - it) * 64);
        }
      };
      ld(std::integral_constant<int, 0>{});
      __builtin_amdgcn_sched_barrier(0);
      static_for<0, 4>([&](auto IT) {
        constexpr int it = IT;
        if constexpr (it + 1 < 4) ld(std::integral_constant<int, it + 1>{});
        if constexpr (PF) static_for<it * (CV_PF1 / 4), (it + 1) * (CV_PF1 / 4)>([&](auto J) { pf_w2(J); });
#pragma unroll
        for (int b1 = 0; b1 < NF; ++b1)
#pragma unroll
          for (int e = 0; e < 4; ++e)
            if (b1 <= Q[e]) acc[e] = __builtin_amdgcn_mfma_f32_16x16x32_f16(A[it & 1][b1], B[it & 1][Q[e] - b1], acc[e], 0, 0, 0);
#pragma unroll
        for (int k = 0; k < 18; ++k) {
          __builtin_amdgcn_sched_group_barrier(0x8, 1, 0);
          if (it + 1 < 4 && k >= 2 && k - 2 < 2 * NF) __builtin_amdgcn_sched_group_barrier(0x100, 1, 0);
          if (PF && CV_PF1 > 0 && (k % (16 / (CV_PF1 / 4))) == 1 && k / (16 / (CV_PF1 / 4)) < CV_PF1 / 4)
            __builtin_amdgcn_sched_group_barrier(0x20, 1, 0);
        }
        __builtin_amdgcn_sched_barrier(0);
      });
    };
    if (LAYER == 0) {
      if (qs) body(std::integral_constant<int, 1>{}, std::true_type{}); else body(std::integral_constant<int, 0>{}, std::true_type{});
    } else {
      if (qs) body(std::integral_constant<int, 1>{}, std::false_type{}); else body(std::integral_constant<int, 0>{}, std::false_type{});
    }
    f32x4 keep[2];
    {
      char* wrp = smem + CV_RED + wave8 * 2048 + lane * 16;
#pragma unroll
      for (int e = 0; e < 2; ++e) {
        f32x4 give;
#pragma unroll
        for (int r = 0; r < 4; ++r) {
          const float lo = acc[e][r], hi = acc[2 + e][r];
          give[r] = half ? lo : hi;
          keep[e][r] = half ? hi : lo;
        }
        *(f32x4*)(wrp + e * 1024) = give;
      }
    }
    asm volatile("s_waitcnt lgkmcnt(0)" ::: "memory");
    __builtin_amdgcn_s_barrier();
    STAMP(KID, 2);
    {
      const char* rd = smem + CV_RED + (wave8 ^ 1) * 2048 + lane * 16;
      const int toff = 16 * j16 + 4 * g;
#pragma unroll
      for (int e = 0; e < 2; ++e) {
        const int q = half ? (e ? (qs ? 1 : 0) : (qs ? 2 : 3)) : (e ? (qs ? 5 : 4) : (qs ? 6 : 7));
        const int t = 256 * q + toff;
        const f32x4 o = *(const f32x4*)(rd + e * 1024);
        const u32x4 xs = *(const u32x4*)(smem + CV_XS + ch * 8192 + t * 4);
        const f32x4 y = keep[e] + o;
        float xo[4];
#pragma unroll
        for (int r = 0; r < 4; ++r) {
          const unsigned w = xs[r];
          xo[r] = bf_lo(w) + rbf(rbf(y[r] * kscale) + bf_hi(w));
        }
        st8_sc1(xt + ((size_t)(t >> 7) * D_ + d0 + ch) * 128 + (t & 127), u32x2{pk_bf16(xo[0], xo[1]), pk_bf16(xo[2], xo[3])});
      }
    }
    wait_vm_lgkm0<0>();
    __builtin_amdgcn_s_barrier();
    if (tid < CREP) (void)__hip_atomic_fetch_add(cflags + (tid * 16 + (d0 >> 5)) * 32, 1u, __ATOMIC_RELAXED, __HIP_MEMORY_SCOPE_AGENT);
    STAMP(KID, 3);
  }
  const int grow = lane >> 4, gslot = lane & 15;
  const unsigned short* xsrc = xt + ((size_t)mt * D_ + wave * 8 + grow) * 128 + ((gslot ^ (grow << 2)) * 8);
  const char* wsrc = w1t + (size_t)ft * 32 * 1024 + lane * 16;
  auto issue_w = [&](int c) {
    char* slot = smem + (c % G1_R) * 16384;
#pragma unroll
    for (int i = 0; i < 2; ++i)
      __builtin_amdgcn_global_load_lds((const unsigned*)(wsrc + (size_t)(c * 2 + i) * 1024),
                                       (LAS unsigned*)(slot + 8192 + (wave * 2 + i) * 1024), 16, 0, 16  );
  };
  auto issue_x = [&](int c) {
    char* slot = smem + (c % G1_R) * 16384;
#pragma unroll
    for (int i = 0; i < 2; ++i)
      __builtin_amdgcn_global_load_lds((const unsigned*)(xsrc + (size_t)(c * 32 + i * 4) * 128),
                                       (LAS unsigned*)(slot + (wave * 8 + i * 4) * 256), 16, 0, 16  );
  };
  if (bi < 28 && wave8 >= 4) {
#pragma unroll
    for (int c = 0; c < W1PRE; ++c) issue_w(c);
  }
  __builtin_amdgcn_sched_barrier(0);
  if (LAYER == 0) static_for<CV_PF1, 28>([&](auto J) { pf_w2(J); });
  __builtin_amdgcn_sched_barrier(0);
  if (wave8 == 7) {
    const unsigned* cp = cflags + ((blockIdx.x % CREP) * 16 + (lane & 15)) * 32;
    for (int it = 0; it < (1 << 20); ++it) {
      const unsigned f = __hip_atomic_load(cp, __ATOMIC_RELAXED, __HIP_MEMORY_SCOPE_AGENT);
      if (__all(f >= 16u)) break;
      __builtin_amdgcn_s_sleep(POLL_SLEEP);
    }
  }
  __builtin_amdgcn_s_barrier();
  STAMP(KID, 4);
  if (bi >= 28) {
    const int ht = bx * 4 + (bi - 28);
    const int cgp = wave8 * 2 + hh;
    const auto rsrc = __builtin_amdgcn_make_buffer_rsrc((void*)xt, 0, L_ * D_ * 2, 0x00020000);
    const int off0 = ((ht >> 1) * D_ * 128 + (ht & 1) * 64 + 2 * r32) * 2;
    const unsigned pv = __builtin_amdgcn_raw_buffer_load_b32(rsrc, off0, 0, 16  );
    unsigned v[32];
#pragma unroll
    for (int j = 0; j < 32; ++j) v[j] = __builtin_amdgcn_raw_buffer_load_b32(rsrc, off0 + (cgp * 32 + j) * 256, 0, 16  );
    static_for<0, 8>([&](auto J) { pf_xr(J); });
    const f32x2 piv = {bf_lo(pv), bf_hi(pv)};
    f32x2 s1 = {0.f, 0.f}, s2 = {0.f, 0.f};
#pragma unroll
    for (int j = 0; j < 32; ++j) {
      const f32x2 dd = f32x2{bf_lo(v[j]), bf_hi(v[j])} - piv;
      s1 += dd;
      s2 = __builtin_elementwise_fma(dd, dd, s2);
    }
    f32x4* red = (f32x4*)smem;
    red[cgp * 32 + r32] = f32x4{s1[0], s2[0], s1[1], s2[1]};
    asm volatile("s_waitcnt lgkmcnt(0)" ::: "memory");
    __builtin_amdgcn_s_barrier();
    if (tid < 64) {
      const f32x2* r2 = (const f32x2*)smem;
      float a = 0.f, q = 0.f;
#pragma unroll
      for (int w = 0; w < 16; ++w) { const f32x2 t = r2[w * 64 + tid]; a += t[0]; q += t[1]; }
      const unsigned pw = __shfl(pv, tid >> 1, 64);
      const float dm = a * (1.f / D_);
      const float var = fmaxf(q * (1.f / D_) - dm * dm, 0.f);
      const float mm = ((tid & 1) ? bf_hi(pw) : bf_lo(pw)) + dm;
      const float sdv = __builtin_amdgcn_sqrtf(rbf(rbf(var) + 1e-5f));
      __hip_atomic_store(sw + ht * 64 + tid, pk_bf16(mm, sdv), __ATOMIC_RELAXED, __HIP_MEMORY_SCOPE_AGENT);
    }
    STAMP(KID, 12);
  } else if (wave8 >= 4) {
#pragma unroll
    for (int c = 0; c < G1_W0; ++c) { issue_x(c); if (c >= W1PRE) issue_w(c); }
    static_for<0, 16>([&](auto C) {
      constexpr int c = C;
      constexpr int extra = (c + G1_W0 > 16) ? 8 : 0;
      wait_vm<g1_young(c) + extra>();
      __builtin_amdgcn_s_barrier();
      if constexpr (c + G1_W0 < 16) { issue_x(c + G1_W0); if (c + G1_W0 >= W1PRE) issue_w(c + G1_W0); }
      if constexpr (c + G1_W0 == 16) {
        __builtin_amdgcn_sched_barrier(0);
        static_for<0, 8>([&](auto J) { pf_xr(J); });
        __builtin_amdgcn_sched_barrier(0);
      }
    });
  } else {
    if (LAYER == 0) {
      const auto rs_p = __builtin_amdgcn_make_buffer_rsrc((void*)(ws + WS_W1P), 0, 64 * F_ * 4, 0x00020000);
      u32x4 pp[8];
#pragma unroll
      for (int j = 0; j < 8; ++j) pp[j] = __builtin_amdgcn_raw_buffer_load_b128(rs_p, ft * 8192 + j * 1024 + lane * 16, 0, 16  );
      asm volatile("" : "+v"(pp[0]), "+v"(pp[1]), "+v"(pp[2]), "+v"(pp[3]), "+v"(pp[4]), "+v"(pp[5]), "+v"(pp[6]), "+v"(pp[7]));
      f32x4 t = {0.f, 0.f, 0.f, 0.f};
#pragma unroll
      for (int j = 0; j < 8; ++j) t += __builtin_bit_cast(f32x4, pp[j]);
#pragma unroll
      for (int o = 8; o <= 32; o <<= 1) {
#pragma unroll
        for (int e = 0; e < 4; ++e) t[e] += __shfl_xor(t[e], o, 64);
      }
      if (lane < 8) *(f32x4*)(smem + LDS_CNT + 256 + (wave * 32 + lane * 4) * 4) = t;
      asm volatile("s_waitcnt lgkmcnt(0)" ::: "memory");
    }
    const int blk = (lane >> 4) & 1, q4 = (lane & 15) >> 2, p4 = lane & 3;
    const unsigned sbase = (unsigned)(uintptr_t)(LAS char*)smem;
    unsigned* lcnt = (unsigned*)(smem + 9 * 16384);
    unsigned tb[3][4], ta[3];
#pragma unroll
    for (int ct = 0; ct < 4; ++ct) {
      tb[0][ct] = sbase + (unsigned)((8 * hh + q4) * 256 + (((4 * ct + 2 * blk + (p4 >> 1)) ^ (q4 << 2)) * 16) + 8 * (p4 & 1));
      tb[1][ct] = tb[0][ct] + 65536u; tb[2][ct] = tb[0][ct] + 131072u;
    }
    ta[0] = sbase + 8192 + wave * 2048 + lane * 16; ta[1] = ta[0] + 65536u; ta[2] = ta[0] + 131072u;
    f32x16 acc[4];
#pragma unroll
    for (int ct = 0; ct < 4; ++ct)
#pragma unroll
      for (int i = 0; i < 16; ++i) acc[ct][i] = 0.f;
    constexpr int LA = 6;
    u32x2 bb[8][2];
    u32x4 af[3];
    unsigned swv[4];
    f32x4 ws4[4];
    auto issue_pair = [&](auto NN) {
      constexpr int n = NN, ks = n >> 2, ct = n & 3;
      constexpr int so = ((ks >> 1) % G1_R) * 16384, seg = so >> 16, o = (so & 65535) + (ks & 1) * 4096;
      if constexpr (ct == 0) lds_read16<(so & 65535) + (ks & 1) * 1024>(af[ks % 3], ta[seg]);
      tr_read<o>(bb[n & 7][0], tb[seg][ct]);
      tr_read<o + 1024>(bb[n & 7][1], tb[seg][ct]);
    };
    __builtin_amdgcn_s_barrier();
    STAMP(KID, 5);
    static_for<0, LA>([&](auto NN) { issue_pair(NN); });
    static_for<0, 128>([&](auto NN) {
      constexpr int n = NN, ks = n >> 2, ct = n & 3;
      constexpr int last = (n + LA - 1 < 127) ? (n + LA - 1) : 127;
      constexpr int younger = 2 * (last - n) + ((last >> 2) - (n >> 2));
      if constexpr (ct == 0) wait_lgkm3<younger>(bb[n & 7][0], bb[n & 7][1], af[ks % 3]);
      else wait_lgkm2<younger>(bb[n & 7][0], bb[n & 7][1]);
      const u32x4 bu = {bb[n & 7][0][0], bb[n & 7][0][1], bb[n & 7][1][0], bb[n & 7][1][1]};
      acc[ct] = __builtin_amdgcn_mfma_f32_32x32x16_bf16(__builtin_bit_cast(bf16x8, af[ks % 3]), __builtin_bit_cast(bf16x8, bu), acc[ct], 0, 0, 0);
      __builtin_amdgcn_sched_barrier(0);
      if constexpr (n + LA < 128) {
        if constexpr ((n + LA) % 8 == 0) __builtin_amdgcn_s_barrier();
        issue_pair(std::integral_constant<int, n + LA>{});
      }
      if constexpr (n >= 104 && n < 108)
        swv[n - 104] = __hip_atomic_load(sw + mt * 128 + (n - 104) * 32 + r32, __ATOMIC_RELAXED, __HIP_MEMORY_SCOPE_AGENT);
      __builtin_amdgcn_sched_barrier(0);
    });
    STAMP(KID, 6);
    for (int it = 0; it < (1 << 20); ++it) {
      if (__all(swv[0] != 0u && swv[1] != 0u && swv[2] != 0u && swv[3] != 0u)) break;
      __builtin_amdgcn_s_sleep(1);
#pragma unroll
      for (int ct = 0; ct < 4; ++ct)
        swv[ct] = __hip_atomic_load(sw + mt * 128 + ct * 32 + r32, __ATOMIC_RELAXED, __HIP_MEMORY_SCOPE_AGENT);
    }
    asm volatile("" : "+v"(swv[0]), "+v"(swv[1]), "+v"(swv[2]), "+v"(swv[3]));
#pragma unroll
    for (int q = 0; q < 4; ++q) ws4[q] = *(const f32x4*)(smem + 9 * 16384 + 256 + (wave * 32 + 8 * q + 4 * hh) * 4);
#pragma unroll
    for (int ct = 0; ct < 4; ++ct) {
      const float rs = __builtin_amdgcn_rcpf(bf_hi(swv[ct]));
      const float cm = -bf_lo(swv[ct]) * rs;
      char* dst = hbuf + ((size_t)((mt * 4 + ct) * 112 + ft * 2) * 64 + lane) * 16;
      const f32x2 c2 = {cm, cm}, r2 = {rs, rs};
#pragma unroll
      for (int s = 0; s < 2; ++s) {
        u32x4 o;
#pragma unroll
        for (int j = 0; j < 4; ++j) {
          const int reg = 8 * s + 2 * j;
          const f32x2 wsum = {ws4[reg >> 2][reg & 3], ws4[reg >> 2][(reg & 3) + 1]};
          const f32x2 a2 = {acc[ct][reg], acc[ct][reg + 1]};
          const f32x2 v2 = __builtin_elementwise_fma(a2, r2, c2 * wsum);
          const s16x2 pv = __builtin_bit_cast(s16x2, __builtin_convertvector(v2, bf16x2));
          const s16x2 z = {0, 0};
          o[j] = __builtin_bit_cast(unsigned, __builtin_elementwise_max(pv, z));
        }
        st16_sc1(dst + s * 1024, o);
      }
    }
    STAMP(KID, 7);
    wait_vm<0>();
    if (lane == 0) {
      const unsigned old = __hip_atomic_fetch_add(lcnt, 1u, __ATOMIC_RELAXED, __HIP_MEMORY_SCOPE_WORKGROUP);
      if (old == 3u) __hip_atomic_store(flags + (mt * 16 + nt) * FLS, 1u, __ATOMIC_RELAXED, __HIP_MEMORY_SCOPE_AGENT);
    }
    STAMP(KID, 8);
    static_for<0, 8>([&](auto J) { pf_xr(J); });
  }
  pin7(wr); pin7(wr + 7); pin7(wr + 14); pin7(wr + 21);
  {
    unsigned x0_ = xr[0], x1_ = xr[1], x2_ = xr[2], x3_ = xr[3], x4_ = xr[4], x5_ = xr[5], x6_ = xr[6], x7_ = xr[7];
    asm volatile("" : "+v"(x0_), "+v"(x1_), "+v"(x2_), "+v"(x3_), "+v"(x4_), "+v"(x5_), "+v"(x6_), "+v"(x7_));
    xr[0] = x0_; xr[1] = x1_; xr[2] = x2_; xr[3] = x3_; xr[4] = x4_; xr[5] = x5_; xr[6] = x6_; xr[7] = x7_;
  }
  __builtin_amdgcn_s_barrier();
  if (wave8 == 7) {
    const unsigned* fp = flags + ((mt2 >> 1) * 16 + (lane < 14 ? lane : 0)) * FLS;
    for (int it = 0; it < (1 << 20); ++it) {
      const unsigned f = __hip_atomic_load(fp, __ATOMIC_RELAXED, __HIP_MEMORY_SCOPE_AGENT);
      if (__all(f != 0u)) break;
      __builtin_amdgcn_s_sleep(POLL_SLEEP);
    }
  }
  asm volatile("s_waitcnt lgkmcnt(0)" ::: "memory");
  __builtin_amdgcn_s_barrier();
  STAMP(KID, 9);
  const char* hsrc = hbuf + (size_t)(mt2 * 2) * 112 * 1024 + wave8 * 1024 + lane * 16;
  char* dstb = smem + wave8 * 2048;
  auto issue2 = [&](int c) {
#pragma unroll
    for (int mi = 0; mi < 2; ++mi)
      __builtin_amdgcn_global_load_lds((const unsigned*)(hsrc + (size_t)mi * 112 * 1024 + c * 8192),
                                       (LAS unsigned*)(dstb + (c % G2_R) * 16384 + mi * 1024), 16, 0, 16  );
  };
  f32x16 acc[2];
#pragma unroll
  for (int i = 0; i < 16; ++i) { acc[0][i] = 0.f; acc[1][i] = 0.f; }
  const char* rdh = smem + kq * 4096 + lane * 16;
  bf16x8 hf[2][4];
#pragma unroll
  for (int c = 0; c < g2_iss(0); ++c) issue2(c);
  static_for<0, 15>([&](auto C) {
    constexpr int c = C;
    auto mma = [&](auto JJ) {
      constexpr int j = JJ;
      if constexpr (c >= 1) {
        constexpr int pc = c - 1;
#pragma unroll
        for (int mi = 0; mi < 2; ++mi)
          acc[mi] = __builtin_amdgcn_mfma_f32_32x32x16_bf16(__builtin_bit_cast(bf16x8, wr[2 * pc + j]), hf[pc & 1][j * 2 + mi], acc[mi], 0, 0, 0);
      }
    };
    mma(std::integral_constant<int, 0>{});
    __builtin_amdgcn_sched_barrier(0);
    if constexpr (c < 14) {
      wait_vm_lgkm0<2 * (g2_iss(c) - (c + 1))>();
      __builtin_amdgcn_s_barrier();
      if constexpr (c == 0) STAMP(KID, 10);
#pragma unroll
      for (int e = 0; e < 4; ++e) hf[c & 1][e] = *(const bf16x8*)(rdh + (c % G2_R) * 16384 + e * 1024);
    }
    __builtin_amdgcn_sched_barrier(0);
    mma(std::integral_constant<int, 1>{});
    __builtin_amdgcn_sched_barrier(0);
    if constexpr (c < 14) {
#pragma unroll
      for (int cn = g2_iss(c); cn < g2_iss(c + 1); ++cn) issue2(cn);
    }
    __builtin_amdgcn_sched_barrier(0);
  });
  STAMP(KID, 11);
  char* red = smem + 5 * 16384;
  {
#pragma unroll
    for (int pc = 0; pc < 4; ++pc) {
      if (pc != kq) {
        const int slot = (kq > pc) ? kq - 1 : kq;
        char* dst = red + ((pc * 2 + ni) * 3 + slot) * 2048 + lane * 16;
        f32x4 g0, g1;
#pragma unroll
        for (int i = 0; i < 4; ++i) { g0[i] = acc[pc >> 1][8 * (pc & 1) + i]; g1[i] = acc[pc >> 1][8 * (pc & 1) + 4 + i]; }
        *(f32x4*)dst = g0;
        *(f32x4*)(dst + 1024) = g1;
      }
    }
  }
  float mine[8];
#pragma unroll
  for (int i = 0; i < 8; ++i) {
    const float a0 = kh_f ? acc[0][8 + i] : acc[0][i];
    const float a1 = kh_f ? acc[1][8 + i] : acc[1][i];
    mine[i] = mi_f ? a1 : a0;
  }
  __syncthreads();
  {
    const char* src = red + (wave8 * 3) * 2048 + lane * 16;
#pragma unroll
    for (int sl = 0; sl < 3; ++sl) {
      const f32x4 o0 = *(const f32x4*)(src + sl * 2048);
      const f32x4 o1 = *(const f32x4*)(src + sl * 2048 + 1024);
#pragma unroll
      for (int i = 0; i < 4; ++i) { mine[i] += o0[i]; mine[4 + i] += o1[i]; }
    }
  }
  float xo[8];
  float s1 = 0.f, s2 = 0.f;
#pragma unroll
  for (int i = 0; i < 8; ++i) {
    const float v = rbf(__uint_as_float(((unsigned)xr[i]) << 16) + rbf(mine[i]));
    xo[i] = v;
    s1 += v; s2 += v * v;
  }
  if (LAYER == 1) {
#pragma unroll
    for (int q = 0; q < 2; ++q)
      *(u32x2*)(p.out + (size_t)tok * D_ + dbase + 8 * q) =
          u32x2{pk_bf16(xo[4 * q], xo[4 * q + 1]), pk_bf16(xo[4 * q + 2], xo[4 * q + 3])};
  } else {
    unsigned short* xout = (unsigned short*)(ws + WS_XT2);
#pragma unroll
    for (int i = 0; i < 8; ++i)
      __hip_atomic_store(xout + (size_t)(dbase + (i & 3) + 8 * (i >> 2)) * L_ + tok,
                         (unsigned short)(__float_as_uint(xo[i]) >> 16), __ATOMIC_RELAXED, __HIP_MEMORY_SCOPE_AGENT);
    s1 += __shfl_xor(s1, 32, 64);
    s2 += __shfl_xor(s2, 32, 64);
    float* sred = (float*)(smem + 8 * 16384);
    sred[wave8 * 64 + lane] = hh ? s2 : s1;
    asm volatile("s_waitcnt lgkmcnt(0)" ::: "memory");
    __builtin_amdgcn_s_barrier();
    if (wave8 < 2) {
      const int w0 = wave8 * 4;
      const float t = sred[w0 * 64 + lane] + sred[(w0 + 1) * 64 + lane] + sred[(w0 + 2) * 64 + lane] + sred[(w0 + 3) * 64 + lane];
      (void)__hip_atomic_fetch_add((float*)(ws + WS_ST1) + 2 * (mt2 * 64 + wave8 * 32 + r32) + hh, t, __ATOMIC_RELAXED, __HIP_MEMORY_SCOPE_AGENT);
    }
    wait_vm_lgkm0<0>();
    __builtin_amdgcn_s_barrier();
    if (tid < CREP) (void)__hip_atomic_fetch_add((unsigned*)(ws + WS_CF) + 2 * (CREP * 16 * 32) + (tid * 16 + (blockIdx.x & 15)) * 32, 1u, __ATOMIC_RELAXED, __HIP_MEMORY_SCOPE_AGENT);
  }
  STAMP(KID, 15);
  }
}

extern "C" void kernel_launch(void* const* d_in, const int* in_sizes, int n_in,
                              void* d_out, int out_size, void* d_ws, size_t ws_size,
                              hipStream_t stream) {
  const unsigned short* x0 = (const unsigned short*)d_in[0];
  const unsigned short* logA = (const unsigned short*)d_in[1];
  const unsigned short* Aim = (const unsigned short*)d_in[2];
  const unsigned short* Cre = (const unsigned short*)d_in[3];
  const unsigned short* Cim = (const unsigned short*)d_in[4];
  const unsigned short* logdt = (const unsigned short*)d_in[5];
  const unsigned short* dskip = (const unsigned short*)d_in[6];
  const unsigned short* w1 = (const unsigned short*)d_in[8];
  const unsigned short* w2 = (const unsigned short*)d_in[9];
  char* ws = (char*)d_ws;
  (void)hipFuncSetAttribute((const void*)s4d_layer_kernel, hipFuncAttributeMaxDynamicSharedMemorySize, LAYER_LDS);
  (void)hipMemsetAsync(ws, 0, WS_ZBYTES, stream);
  LayerArgs a0 = {x0, logA, Aim, Cre, Cim, logdt, dskip, w1, w2, (unsigned short*)d_out, ws};
  s4d_layer_kernel<<<256, 512, LAYER_LDS, stream>>>(a0);
}
```

```cpp
#include <hip/hip_runtime.h>
#include <hip/hip_bf16.h>
#include <stdint.h>

typedef __attribute__((ext_vector_type(8))) __bf16 bf16x8;
typedef __attribute__((ext_vector_type(2))) __bf16 bf16x2;
typedef __attribute__((ext_vector_type(4))) short s16x4;
typedef __attribute__((ext_vector_type(2))) short s16x2;
typedef __attribute__((ext_vector_type(8))) short s16x8;
typedef __attribute__((ext_vector_type(8))) _Float16 f16x8;
typedef __attribute__((ext_vector_type(2))) _Float16 f16x2;
typedef __attribute__((ext_vector_type(2))) float f32x2;
typedef __attribute__((ext_vector_type(4))) float f32x4;
typedef __attribute__((ext_vector_type(16))) float f32x16;
typedef __attribute__((ext_vector_type(4))) unsigned u32x4;
typedef __attribute__((ext_vector_type(2))) unsigned u32x2;
#define LAS __attribute__((address_space(3)))
#define DI __device__ __forceinline__

constexpr int L_ = 2048, D_ = 512, N_ = 128, F_ = 1792;

constexpr size_t MiB = 1024 * 1024;
constexpr size_t WS_G    = 0;
constexpr size_t WS_XT0  = 2 * MiB;
constexpr size_t WS_XT1  = 4 * MiB;
constexpr size_t WS_XT2  = 6 * MiB;
constexpr size_t WS_XT3  = 8 * MiB;
constexpr size_t WS_W1T  = 10 * MiB;
constexpr size_t WS_W2T  = 12 * MiB;
constexpr size_t WS_H    = 14 * MiB;
constexpr size_t WS_H2   = 24 * MiB;
constexpr size_t WS_KINV = 22 * MiB;
constexpr size_t WS_W1S  = WS_KINV + 4096;
constexpr size_t WS_Z0   = 23 * MiB;
constexpr size_t WS_ST1  = WS_Z0;
constexpr size_t WS_SW   = WS_ST1 + 16384;
#define FLS 32
#define CREP 8
#define CFS (3 * CREP)
constexpr size_t WS_FL   = WS_SW + 16384;
constexpr size_t WS_CF   = WS_FL + 2048 * FLS;
constexpr size_t WS_ZBYTES = 16384 + 16384 + 2048 * FLS + 2048 * CFS;
constexpr size_t WS_SW0  = WS_Z0 + 512 * 1024;

#define STAMP(kid, i)
#define STAMPT(kid, i, T)
DI float bf_lo(unsigned u) { return __uint_as_float(u << 16); }
DI float bf_hi(unsigned u) { return __uint_as_float(u & 0xffff0000u); }
DI unsigned pk_bf16(float a, float b) {
  f32x2 v = {a, b};
  return __builtin_bit_cast(unsigned, __builtin_convertvector(v, bf16x2));
}
DI unsigned pk_f16(float a, float b) {
  f32x2 v = {a, b};
  return __builtin_bit_cast(unsigned, __builtin_convertvector(v, f16x2));
}
DI float rbf(float a) { return bf_lo(pk_bf16(a, 0.f)); }
DI float ld_bf(const unsigned short* p) { return __uint_as_float(((unsigned)*p) << 16); }
DI unsigned perm_lo(unsigned hi, unsigned lo) { return __builtin_amdgcn_perm(hi, lo, 0x05040100u); }
DI unsigned perm_hi(unsigned hi, unsigned lo) { return __builtin_amdgcn_perm(hi, lo, 0x07060302u); }
template <int CTRL> DI float dpp_add(float v) {
  return v + __uint_as_float(__builtin_amdgcn_update_dpp(0u, __float_as_uint(v), CTRL, 0xf, 0xf, true));
}
DI float wave_sum(float v) {
  v = dpp_add<0xB1>(v);
  v = dpp_add<0x4E>(v);
  v = dpp_add<0x141>(v);
  v = dpp_add<0x140>(v);
  const unsigned u = __float_as_uint(v);
  return (__uint_as_float(__builtin_amdgcn_readlane(u, 0)) + __uint_as_float(__builtin_amdgcn_readlane(u, 16))) +
         (__uint_as_float(__builtin_amdgcn_readlane(u, 32)) + __uint_as_float(__builtin_amdgcn_readlane(u, 48)));
}
#define ST_FLAGS ""
DI void st16(void* p, u32x4 v) { asm volatile("global_store_dwordx4 %0, %1, off " ST_FLAGS "\n\ts_nop 1" ::"v"(p), "v"(v) : "memory"); }
DI void st8(void* p, u32x2 v) { asm volatile("global_store_dwordx2 %0, %1, off " ST_FLAGS "\n\ts_nop 1" ::"v"(p), "v"(v) : "memory"); }
DI void st2(void* p, unsigned v) { asm volatile("global_store_short %0, %1, off " ST_FLAGS "\n\ts_nop 1" ::"v"(p), "v"(v) : "memory"); }
template <int N> DI void wait_vm() { asm volatile("s_waitcnt vmcnt(%0)" ::"n"(N) : "memory"); }
template <int OFF> DI void gload16(u32x4& dst, const char* p) {
  asm volatile("global_load_dwordx4 %0, %1, off offset:%2" : "=v"(dst) : "v"(p), "n"(OFF));
}
template <int N> DI void wait_vm4(u32x4& a, u32x4& b, u32x4& c, u32x4& d) {
  asm volatile("s_waitcnt vmcnt(%4)" : "+v"(a), "+v"(b), "+v"(c), "+v"(d) : "n"(N) : "memory");
}
template <int OFF> DI void tr_read(u32x2& d, unsigned addr) {
  asm volatile("ds_read_b64_tr_b16 %0, %1 offset:%2" : "=v"(d) : "v"(addr), "n"(OFF));
}
template <int N> DI void wait_lgkm8(u32x2* b) {
  asm volatile("s_waitcnt lgkmcnt(%8)"
               : "+v"(b[0]), "+v"(b[1]), "+v"(b[2]), "+v"(b[3]), "+v"(b[4]), "+v"(b[5]), "+v"(b[6]), "+v"(b[7])
               : "n"(N) : "memory");
}
template <int N> DI void wait_vm_lgkm0() { asm volatile("s_waitcnt vmcnt(%0) lgkmcnt(0)" ::"n"(N) : "memory"); }
template <int I, int E, class F> DI void static_for(F&& f) {
  if constexpr (I < E) { f(std::integral_constant<int, I>{}); static_for<I + 1, E>(f); }
}

constexpr int TROW = 528;

__host__ __device__ constexpr int tab_w(int idx) {
  return idx < 16 ? idx : (idx < 40 ? ((8 + ((idx - 16) & 7)) << (1 + ((idx - 16) >> 3))) : 128);
}
__host__ __device__ constexpr int ilog2c(int w) { return w <= 1 ? 0 : 1 + ilog2c(w >> 1); }
__host__ __device__ constexpr int tab_idx(int w) {
  return w < 16 ? w : (w == 128 ? 40 : 16 + 8 * (ilog2c(w) - 4) + ((w >> (ilog2c(w) - 3)) - 8));
}
DI int tab_idx_rt(int w) {
  if (w < 16) return w;
  int lg = 31 - __clz(w);
  return 16 + 8 * (lg - 4) + ((w >> (lg - 3)) - 8);
}

DI void retile_load(int u, const unsigned short* __restrict__ w1, const unsigned short* __restrict__ w2, u32x2* r, size_t& dst) {
  if (u < 28672) {
    const int fg = u % 448, o = u / 448;
    const int f = 4 * fg;
#pragma unroll
    for (int j = 0; j < 8; ++j) r[j] = *(const u32x2*)(w1 + (size_t)(8 * o + j) * F_ + f);
    dst = WS_W1T + ((size_t)(((f >> 5) * 32 + (o >> 1)) * 64 + (o & 1) * 32 + (f & 31))) * 16;
  } else {
    const int v = u - 28672;
    const int dg = v & 127, o = v >> 7;
    const int dd = 4 * dg, ks = o >> 1, hh = o & 1;
#pragma unroll
    for (int j = 0; j < 8; ++j)
      r[j] = *(const u32x2*)(w2 + (size_t)(16 * ks + 8 * (j >> 2) + 4 * hh + (j & 3)) * D_ + dd);
    dst = WS_W2T + ((size_t)(((dd >> 5) * 112 + ks) * 64 + hh * 32 + (dd & 31))) * 16;
  }
}
DI void retile_store(const u32x2* r, char* ws, size_t dst) {
#pragma unroll
  for (int c = 0; c < 2; ++c) {
    u32x4 lo = {perm_lo(r[1][c], r[0][c]), perm_lo(r[3][c], r[2][c]), perm_lo(r[5][c], r[4][c]), perm_lo(r[7][c], r[6][c])};
    u32x4 hi = {perm_hi(r[1][c], r[0][c]), perm_hi(r[3][c], r[2][c]), perm_hi(r[5][c], r[4][c]), perm_hi(r[7][c], r[6][c])};
    st16(ws + dst + (2 * c) * 16, lo);
    st16(ws + dst + (2 * c + 1) * 16, hi);
  }
}

#define LAYER_CODE 25600
#define PREP_CODE 14336
template <int BYTES> DI unsigned warm_code(const char* pc, int t) {
  const int o = t * 128;
  return *(const unsigned*)(pc + (o < BYTES ? o : 0));
}
constexpr int PB_KGEN = 512, PB_XT = 64, PB_ST = 64, PB_RT = 112, PB_WS = 16;
constexpr int PREP_BLOCKS = PB_KGEN + PB_XT + PB_ST + PB_RT + PB_WS;

__global__ __launch_bounds__(256) void prep_kernel(
    const unsigned short* __restrict__ x0, const unsigned short* __restrict__ logA,
    const unsigned short* __restrict__ Aim, const unsigned short* __restrict__ Cre,
    const unsigned short* __restrict__ Cim, const unsigned short* __restrict__ logdt,
    const unsigned short* __restrict__ w1, const unsigned short* __restrict__ w2, char* __restrict__ ws) {
  __shared__ __attribute__((aligned(16))) char smem[82 * TROW + 1536];
  float* red = (float*)smem;
  const int tid = threadIdx.x, lane = tid & 63;
  const int wave = __builtin_amdgcn_readfirstlane(tid >> 6);
  int b = blockIdx.x;
  STAMP(0, 0);
  unsigned cw1 = 0u;
  if (b >= PB_KGEN && b < PB_KGEN + PB_XT) cw1 = warm_code<PREP_CODE>((const char*)__builtin_amdgcn_s_getpc(), tid);
  if (b < PB_KGEN) {
    const int d = ((b & 7) * 32 + ((b >> 3) & 31)) * 2 + (b >> 8);
    const int isH = wave >> 1;
    const int n = (wave & 1) * 64 + lane;
    const float pdt = ld_bf(logdt + d), pla = ld_bf(logA + d * N_ + n), pai = ld_bf(Aim + d * N_ + n);
    const float p0r = ld_bf(Cre + d * N_ + lane), p0i = ld_bf(Cim + d * N_ + lane);
    const float p1r = ld_bf(Cre + d * N_ + 64 + lane), p1i = ld_bf(Cim + d * N_ + 64 + lane);
    const float dt = rbf(expf(pdt));
    const float ar = -rbf(expf(pla)) * dt;
    const float ai = pai * (isH ? -dt : dt);
    const float c0r = p0r * dt, c0i = p0i * dt, c1r = p1r * dt, c1i = p1i * dt;
    const float S = wave_sum(2.f * (fabsf(c0r) + fabsf(c0i) + fabsf(c1r) + fabsf(c1i)));
    int ex = 0;
    (void)frexpf(S, &ex);
    if (!(S > 0.f) || !(S < 3.0e38f)) ex = 15;
    ex = max(-100, min(ex, 120));
    const float scale = ldexpf(1.f, 15 - ex);
    const float cr = (wave & 1) ? c1r : c0r, ci = (wave & 1) ? c1i : c0i;
    const float base = isH ? 16.f : 1.f;
    const float er = ar * 1.44269504088896340736f * base;
    const float IH = 0.15915494309189533577f;
    const float IL = (float)(0.15915494309189533577 - (double)0.15915494309189533577f);
    float fh = ai * IH;
    float fl = fmaf(ai, IH, -fh) + ai * IL;
    fh *= base; fl *= base;
    STAMP(0, 1);
    f32x2 Er2[8], Ei2[8];
#pragma unroll
    for (int k = 0; k < 8; ++k) {
      const float pw = (float)(1 << k);
      const float mag = __builtin_amdgcn_exp2f(er * pw);
      const float ph = __builtin_amdgcn_fractf(fh * pw) + fl * pw;
      const float re = mag * __builtin_amdgcn_cosf(ph), im = mag * __builtin_amdgcn_sinf(ph);
      Er2[k] = f32x2{re, re};
      Ei2[k] = f32x2{-im, im};
    }
    f32x2 tv[41];
    tv[0] = isH ? f32x2{2.f * scale * cr, -2.f * scale * ci} : f32x2{1.f, 0.f};
    char* trow = smem + isH * (41 * TROW) + n * 4;
    *(unsigned*)trow = __builtin_bit_cast(unsigned, __builtin_convertvector(tv[0], f16x2));
    static_for<1, 41>([&](auto I) {
      constexpr int idx = I;
      constexpr int w = tab_w(idx);
      constexpr int lg = ilog2c(w);
      constexpr int ridx = tab_idx(w - (1 << lg));
      const f32x2 sw = __builtin_shufflevector(tv[ridx], tv[ridx], 1, 0);
      tv[idx] = __builtin_elementwise_fma(Ei2[lg], sw, Er2[lg] * tv[ridx]);
      *(unsigned*)(trow + idx * TROW) = __builtin_bit_cast(unsigned, __builtin_convertvector(tv[idx], f16x2));
    });
    int cix[8];
#pragma unroll
    for (int e = 0; e < 8; ++e) {
      const int l = 2047 - (tid * 8 + e);
      const int sh = max((31 - __clz(l | 1)) - 7, 0);
      const int q = (l + ((1 << sh) >> 1) - (sh > 0 ? 1 : 0) + ((l >> sh) & (sh > 0 ? 1 : 0))) >> sh;
      cix[e] = (sh > 0) ? (128 * sh + q) : l;
    }
    STAMP(0, 2);
    __syncthreads();
    _Float16* kc = (_Float16*)(smem + 82 * TROW);
    {
      const int s = wave, i16 = lane & 15, g = lane >> 4;
      const int hw = (s == 0) ? i16 : (i16 < 8 ? ((8 + i16) << s) : 128);
      const char* arow = smem + 41 * TROW + tab_idx_rt(hw) * TROW + g * 16;
      const char* brow = smem + tab_idx_rt(i16 << s) * TROW + g * 16;
      f16x8 af[8], bf[8];
#pragma unroll
      for (int ks = 0; ks < 8; ++ks) { af[ks] = *(const f16x8*)(arow + ks * 64); bf[ks] = *(const f16x8*)(brow + ks * 64); }
      f32x4 k0 = {0.f, 0.f, 0.f, 0.f}, k1 = {0.f, 0.f, 0.f, 0.f};
#pragma unroll
      for (int ks = 0; ks < 8; ks += 2) {
        k0 = __builtin_amdgcn_mfma_f32_16x16x32_f16(af[ks], bf[ks], k0, 0, 0, 0);
        k1 = __builtin_amdgcn_mfma_f32_16x16x32_f16(af[ks + 1], bf[ks + 1], k1, 0, 0, 0);
      }
#pragma unroll
      for (int r = 0; r < 4; ++r) {
        const int i = 4 * g + r;
        const int cidx = (s == 0) ? (16 * i + i16) : (256 + 128 * (s - 1) + 16 * i + i16);
        const bool ok = (s == 0) || (i < 8) || (s == 3 && i == 8 && i16 == 0);
        if (ok) kc[cidx] = (_Float16)(k0[r] + k1[r]);
      }
    }
    __syncthreads();
    STAMP(0, 3);
    unsigned short o[8];
#pragma unroll
    for (int e = 0; e < 8; ++e) o[e] = ((const unsigned short*)kc)[cix[e]];
    *(u32x4*)(ws + WS_G + (size_t)d * 4096 + tid * 16) =
        u32x4{(unsigned)o[0] | ((unsigned)o[1] << 16), (unsigned)o[2] | ((unsigned)o[3] << 16),
              (unsigned)o[4] | ((unsigned)o[5] << 16), (unsigned)o[6] | ((unsigned)o[7] << 16)};
    if (tid == 0) ((float*)(ws + WS_KINV))[d] = ldexpf(1.f, ex - 15);
    STAMP(0, 15);
    return;
  }
  b -= PB_KGEN;
  if (b < PB_XT) {
    unsigned long long lpc;
    asm volatile("s_getpc_b64 s[40:41]\n\ts_add_u32 s40, s40, s4d_layer_kernel@rel32@lo+4\n\t"
                 "s_addc_u32 s41, s41, s4d_layer_kernel@rel32@hi+12\n\ts_mov_b64 %0, s[40:41]"
                 : "=s"(lpc) :: "scc", "s40", "s41");
    const unsigned cw2 = warm_code<LAYER_CODE>((const char*)lpc, tid);
    const int tile = b * 4 + wave, tt = tile >> 3, ct = tile & 7;
    const int tg = lane & 7, cg = lane >> 3;
    const unsigned short* src = x0 + (size_t)(tt * 64 + tg * 8) * D_ + ct * 64 + cg * 8;
    u32x4 r[8];
#pragma unroll
    for (int j = 0; j < 8; ++j) r[j] = *(const u32x4*)(src + (size_t)j * D_);
    unsigned short* dst = (unsigned short*)(ws + WS_XT0) + (size_t)(ct * 64 + cg * 8) * L_ + tt * 64 + tg * 8;
#pragma unroll
    for (int c = 0; c < 4; ++c) {
      u32x4 lo = {perm_lo(r[1][c], r[0][c]), perm_lo(r[3][c], r[2][c]), perm_lo(r[5][c], r[4][c]), perm_lo(r[7][c], r[6][c])};
      u32x4 hi = {perm_hi(r[1][c], r[0][c]), perm_hi(r[3][c], r[2][c]), perm_hi(r[5][c], r[4][c]), perm_hi(r[7][c], r[6][c])};
      *(u32x4*)(dst + (size_t)(2 * c) * L_) = lo;
      *(u32x4*)(dst + (size_t)(2 * c + 1) * L_) = hi;
    }
    asm volatile("" ::"v"(cw1), "v"(cw2));
    STAMP(0, 15);
    return;
  }
  b -= PB_XT;
  if (b < PB_ST) {
    const int t0 = b * 32 + wave * 8;
    u32x4 r[8];
#pragma unroll
    for (int j = 0; j < 8; ++j) r[j] = *(const u32x4*)(x0 + (size_t)(t0 + j) * D_ + lane * 8);
    for (int i = b * 256 + tid; i < (int)(WS_ZBYTES / 16); i += PB_ST * 256) ((u32x4*)(ws + WS_Z0))[i] = u32x4{0u, 0u, 0u, 0u};
    asm volatile("" : "+v"(r[0]), "+v"(r[1]), "+v"(r[2]), "+v"(r[3]), "+v"(r[4]), "+v"(r[5]), "+v"(r[6]), "+v"(r[7]));
    unsigned word = 0u;
#pragma unroll
    for (int j = 0; j < 8; ++j) {
      float s = 0.f;
#pragma unroll
      for (int c = 0; c < 4; ++c) s += bf_lo(r[j][c]) + bf_hi(r[j][c]);
      const float mean = wave_sum(s) * (1.f / D_);
      float q = 0.f;
#pragma unroll
      for (int c = 0; c < 4; ++c) {
        const float a = bf_lo(r[j][c]) - mean, bb = bf_hi(r[j][c]) - mean;
        q += a * a + bb * bb;
      }
      const float var = wave_sum(q) * (1.f / D_);
      const unsigned wv = pk_bf16(mean, sqrtf(rbf(rbf(var) + 1e-5f)));
      if (lane == j) word = wv;
    }
    if (lane < 8) ((unsigned*)(ws + WS_SW0))[t0 + lane] = word;
    STAMP(0, 15);
    return;
  }
  b -= PB_ST;
  if (b < PB_RT) {
    u32x2 r0[8], r1[8];
    size_t d0_, d1_;
    retile_load(b * 512 + tid, w1, w2, r0, d0_);
    retile_load(b * 512 + 256 + tid, w1, w2, r1, d1_);
    retile_store(r0, ws, d0_);
    retile_store(r1, ws, d1_);
    STAMP(0, 15);
    return;
  }
  b -= PB_RT;
  {
    const int cg = tid % 14, rg = tid / 14;
    const int f0 = b * 112 + cg * 8;
    float s[8] = {0.f, 0.f, 0.f, 0.f, 0.f, 0.f, 0.f, 0.f};
    if (tid < 224) {
#pragma unroll
      for (int h = 0; h < 2; ++h) {
        u32x4 v[16];
#pragma unroll
        for (int j = 0; j < 16; ++j) v[j] = *(const u32x4*)(w1 + (size_t)(rg * 32 + h * 16 + j) * F_ + f0);
#pragma unroll
        for (int j = 0; j < 16; ++j)
#pragma unroll
          for (int c = 0; c < 4; ++c) { s[2 * c] += bf_lo(v[j][c]); s[2 * c + 1] += bf_hi(v[j][c]); }
      }
#pragma unroll
      for (int e = 0; e < 8; ++e) red[rg * 112 + cg * 8 + e] = s[e];
    }
    __syncthreads();
    if (tid < 112) {
      float t = 0.f;
#pragma unroll
      for (int r = 0; r < 16; ++r) t += red[r * 112 + tid];
      ((float*)(ws + WS_W1S))[b * 112 + tid] = t;
    }
    STAMP(0, 15);
  }
}

constexpr int CV_CS = 260 * 16;
constexpr int CV_KCH = 8 * CV_CS;
constexpr int CV_U0 = 2 * CV_KCH;
constexpr int CV_UCH = (256 + 2048) * 2;
constexpr int CV_XS = CV_U0 + 2 * CV_UCH;
constexpr int CV_RED = CV_XS + 2 * 8192;
constexpr int G1_R = 9;
constexpr int G2_R = 9;
constexpr int LAYER_LDS = 9 * 16384 + 1024;
static_assert(CV_RED + 16384 <= 9 * 16384, "conv LDS");
#define G1_W0 5
#define G2_W0 6
#define CV_PF1 16
#define CV_PF2 22
#define W1PRE 5
#define POLL_SLEEP 2
static_assert(G1_W0 <= G1_R - 2 && W1PRE <= G1_R, "ring safety");
__host__ __device__ constexpr int g2_iss(int i) {
  int v = G2_W0 + i;
  return v < 14 ? v : 14;
}
__host__ __device__ constexpr int g1_young(int c) {
  int n = 0;
  for (int j = c + 1; j < c + G1_W0 && j < 16; ++j) n += (j >= W1PRE) ? 4 : 2;
  return n;
}
template <int N> DI void wait_lgkm2(u32x2& a, u32x2& b) {
  asm volatile("s_waitcnt lgkmcnt(%2)" : "+v"(a), "+v"(b) : "n"(N) : "memory");
}
template <int N> DI void wait_lgkm3(u32x2& a, u32x2& b, u32x4& c) {
  asm volatile("s_waitcnt lgkmcnt(%3)" : "+v"(a), "+v"(b), "+v"(c) : "n"(N) : "memory");
}
template <int OFF> DI void lds_read16(u32x4& d, unsigned addr) {
  asm volatile("ds_read_b128 %0, %1 offset:%2" : "=v"(d) : "v"(addr), "n"(OFF));
}
DI void pin4(bf16x8* f) { asm volatile("" : "+v"(f[0]), "+v"(f[1]), "+v"(f[2]), "+v"(f[3])); }
DI void pin7(u32x4* f) { asm volatile("" : "+v"(f[0]), "+v"(f[1]), "+v"(f[2]), "+v"(f[3]), "+v"(f[4]), "+v"(f[5]), "+v"(f[6])); }
DI void st16_sc1(void* p, u32x4 v) { asm volatile("global_store_dwordx4 %0, %1, off sc1\n\ts_nop 1" ::"v"(p), "v"(v) : "memory"); }
DI void st8_sc1(void* p, u32x2 v) { asm volatile("global_store_dwordx2 %0, %1, off sc1\n\ts_nop 1" ::"v"(p), "v"(v) : "memory"); }
DI u32x4 ld16_sc1_wait(const void* p) {
  u32x4 v;
  asm volatile("global_load_dwordx4 %0, %1, off sc1\n\ts_waitcnt vmcnt(0)" : "=&v"(v) : "v"(p) : "memory");
  return v;
}

struct LayerArgs {
  const unsigned short* dskip;
  unsigned short* out;
  char* ws;
};

extern "C" __global__ __launch_bounds__(512, 2) void s4d_layer_kernel(LayerArgs p) {
  const unsigned cw = warm_code<LAYER_CODE>((const char*)__builtin_amdgcn_s_getpc(), threadIdx.x);
  extern __shared__ __attribute__((aligned(16))) char smem[];
  u32x4 wr[28];
#pragma unroll 1
  for (int LAYER = 0; LAYER < 2; ++LAYER) {
  int tid = threadIdx.x;
  asm volatile("" : "+v"(tid));
  const int lane = tid & 63;
  const int wave8 = __builtin_amdgcn_readfirstlane(tid >> 6);
  const int wave = wave8 & 3;
  const int bx = blockIdx.x & 7, bi = blockIdx.x >> 3;
  const int r32 = lane & 31, hh = lane >> 5;
  char* ws = p.ws;
  const char* w1t = ws + WS_W1T;
  const char* w2t = ws + WS_W2T;
  const int mt2 = bx * 4 + (bi & 3), nt2 = bi >> 2;
  const int ni = wave8 & 1, kq = wave8 >> 1;
  const int mi_f = kq >> 1, kh_f = kq & 1;
  const int tok = mt2 * 64 + mi_f * 32 + r32;
  const int dbase = nt2 * 64 + ni * 32 + 4 * hh + 16 * kh_f;
  unsigned short xr[8];
  const char* w2src = w2t + (size_t)(nt2 * 2 + ni) * 112 * 1024 + kq * 2048 + lane * 16;
  auto pf_w2 = [&](auto J) {
    constexpr int j = J;
    wr[j] = *(const u32x4*)(w2src + (j >> 1) * 8192 + (j & 1) * 1024);
  };
  const int mt = (bx >> 1) * 4 + (bi & 3), nt = (bx & 1) * 7 + (bi >> 2);
  const int ft = nt * 4 + wave;
  const int KID = 1 + 3 * LAYER;
  const unsigned short* xin = (const unsigned short*)(ws + (LAYER ? WS_XT2 : WS_XT0));
  unsigned short* xt = (unsigned short*)(ws + (LAYER ? WS_XT3 : WS_XT1));
  char* hbuf = ws + (LAYER ? WS_H2 : WS_H);
  unsigned* sw = (unsigned*)(ws + WS_SW) + LAYER * 2048;
  unsigned* flags = (unsigned*)(ws + WS_FL) + LAYER * 256 * FLS;
  unsigned* cflags = (unsigned*)(ws + WS_CF) + LAYER * (CREP * 16 * 32);
  unsigned* eflags = (unsigned*)(ws + WS_CF) + 2 * (CREP * 16 * 32);
  const unsigned short* xrsrc = xt + ((size_t)(tok >> 7) * D_ + dbase) * 128 + (tok & 127);
  auto pf_xr = [&](auto J) {
    constexpr int j = J;
    xr[j] = __hip_atomic_load(xrsrc + ((j & 3) + 8 * (j >> 2)) * 128, __ATOMIC_RELAXED, __HIP_MEMORY_SCOPE_AGENT);
  };
  STAMP(KID, 0);
  {
    const int d0 = 2 * (bx * 32 + bi);
    const int gz = tid & 255, gc = tid >> 8;
    const int t4 = tid * 4;
    const int ch = wave8 >> 2, half = wave8 & 1, qs = (wave8 >> 1) & 1;
    float xf[2][4], m[4], sd[4];
    const unsigned dsk2 = *(const unsigned*)(p.dskip + d0);
    const char* gp = ws + WS_G + (size_t)(d0 + gc) * 4096 + gz * 16;
    const u32x4 ga = *(const u32x4*)gp;
    const u32x4 gb = *(const u32x4*)(gp + 16);
    const float kscale = ((const float*)(ws + WS_KINV))[d0 + ch];
    u32x4 swv0 = {0u, 0u, 0u, 0u};
    u32x2 xv0 = {0u, 0u}, xv1 = {0u, 0u};
    if (LAYER == 0) {
      swv0 = *(const u32x4*)(ws + WS_SW0 + t4 * 4);
      xv0 = *(const u32x2*)(xin + (size_t)d0 * L_ + t4);
      xv1 = *(const u32x2*)(xin + (size_t)(d0 + 1) * L_ + t4);
    }
    unsigned zz = 0u;
    asm volatile("" : "+v"(zz));
    if (tid < 16) *(u32x4*)(smem + (tid >> 3) * CV_KCH + (tid & 7) * CV_CS + 256 * 16) = u32x4{zz, zz, zz, zz};
    if (tid >= 64 && tid < 128) {
      const int q = tid - 64;
      *(u32x4*)(smem + CV_U0 + (q >> 5) * CV_UCH + (q & 31) * 16) = u32x4{zz, zz, zz, zz};
    }
    if (tid == 128) *(unsigned*)(smem + 9 * 16384) = 0u;
    {
      const unsigned msk = (gz < 255) ? 0xffffffffu : 0u;
      unsigned w[8] = {ga[0], ga[1], ga[2], ga[3], gb[0] & msk, gb[1] & msk, gb[2] & msk, gb[3] & msk};
      unsigned o[7];
#pragma unroll
      for (int k = 0; k < 7; ++k) o[k] = __builtin_amdgcn_alignbit(w[k + 1], w[k], 16);
      char* dst = smem + gc * CV_KCH + gz * 16;
#pragma unroll
      for (int mm = 0; mm < 4; ++mm) {
        *(u32x4*)(dst + (2 * mm) * CV_CS) = u32x4{w[mm], w[mm + 1], w[mm + 2], w[mm + 3]};
        *(u32x4*)(dst + (2 * mm + 1) * CV_CS) = u32x4{o[mm], o[mm + 1], o[mm + 2], o[mm + 3]};
      }
    }
    f32x4 st0 = {0.f, 0.f, 0.f, 0.f}, st1 = {0.f, 0.f, 0.f, 0.f};
    if (LAYER != 0) {
      if (wave8 == 7) {
        const unsigned* cp = eflags + ((blockIdx.x % CREP) * 16 + (lane & 15)) * 32;
        for (int it = 0; it < (1 << 20); ++it) {
          const unsigned f = __hip_atomic_load(cp, __ATOMIC_RELAXED, __HIP_MEMORY_SCOPE_AGENT);
          if (__all(f >= 16u)) break;
          __builtin_amdgcn_s_sleep(POLL_SLEEP);
        }
      }
      __builtin_amdgcn_s_barrier();
      STAMP(KID, 12);
      const auto rs_st = __builtin_amdgcn_make_buffer_rsrc((void*)(ws + WS_ST1), 0, 16384, 0x00020000);
      const auto rs_x = __builtin_amdgcn_make_buffer_rsrc((void*)xin, 0, L_ * D_ * 2, 0x00020000);
      st0 = __builtin_bit_cast(f32x4, __builtin_amdgcn_raw_buffer_load_b128(rs_st, t4 * 8, 0, 16  ));
      st1 = __builtin_bit_cast(f32x4, __builtin_amdgcn_raw_buffer_load_b128(rs_st, t4 * 8 + 16, 0, 16));
      xv0 = __builtin_amdgcn_raw_buffer_load_b64(rs_x, (d0 * L_ + t4) * 2, 0, 16);
      xv1 = __builtin_amdgcn_raw_buffer_load_b64(rs_x, ((d0 + 1) * L_ + t4) * 2, 0, 16);
    }
#pragma unroll
    for (int r = 0; r < 4; ++r) {
      {
        const unsigned wv = swv0[r];
        const float a = (r < 2) ? st0[(r & 1) * 2] : st1[(r & 1) * 2];
        const float bq = (r < 2) ? st0[(r & 1) * 2 + 1] : st1[(r & 1) * 2 + 1];
        const float mean = a * (1.f / D_), var = fmaxf(bq * (1.f / D_) - mean * mean, 0.f);
        const float m1 = rbf(mean), sd1 = rbf(__builtin_amdgcn_sqrtf(rbf(rbf(var) + 1e-5f)));
        m[r] = LAYER ? m1 : bf_lo(wv);
        sd[r] = LAYER ? sd1 : bf_hi(wv);
      }
      const unsigned w0 = xv0[r >> 1], w1_ = xv1[r >> 1];
      xf[0][r] = (r & 1) ? bf_hi(w0) : bf_lo(w0);
      xf[1][r] = (r & 1) ? bf_hi(w1_) : bf_lo(w1_);
    }
    {
      float rs[4];
#pragma unroll
      for (int r = 0; r < 4; ++r) rs[r] = __builtin_amdgcn_rcpf(sd[r]);
#pragma unroll
      for (int c = 0; c < 2; ++c) {
        const float dsk = c ? bf_hi(dsk2) : bf_lo(dsk2);
        float uu[4];
        unsigned xs[4];
#pragma unroll
        for (int r = 0; r < 4; ++r) {
          const float x = xf[c][r];
          const float dd = rbf(x - m[r]);
          float q = dd * rs[r];
          q = fmaf(fmaf(-q, sd[r], dd), rs[r], q);
          uu[r] = rbf(q);
          xs[r] = pk_bf16(x, uu[r] * dsk);
        }
        *(u32x2*)(smem + CV_U0 + c * CV_UCH + (256 + t4) * 2) = u32x2{pk_f16(uu[0], uu[1]), pk_f16(uu[2], uu[3])};
        *(u32x4*)(smem + CV_XS + c * 8192 + t4 * 4) = u32x4{xs[0], xs[1], xs[2], xs[3]};
      }
    }
    asm volatile("" ::"v"(cw));
    __syncthreads();
    STAMP(KID, 1);
    const int j16 = lane & 15, g = lane >> 4;
    const int cc = (7 - j16) & 7;
    const int zl = (2031 - j16 - cc) / 8 + g;
    const char* abase = smem + ch * CV_KCH + cc * CV_CS + (zl - 32 * 7 - 4 * (4 * half + 3)) * 16;
    const char* bbase = smem + CV_U0 + ch * CV_UCH + 480 + 32 * j16 + 16 * g - 64 * (4 * half + 3);
    f32x4 acc[4];
#pragma unroll
    for (int q = 0; q < 4; ++q) acc[q] = f32x4{0.f, 0.f, 0.f, 0.f};
    auto body = [&](auto QS, auto PFF) {
      constexpr int S = QS;
      constexpr bool PF = PFF;
      constexpr int Q[4] = {S ? 6 : 7, S ? 5 : 4, S ? 2 : 3, S ? 1 : 0};
      constexpr int NF = S ? 7 : 8;
      f16x8 A[2][NF], B[2][NF];
      auto ld = [&](auto IT) {
        constexpr int it = IT;
#pragma unroll
        for (int k = 0; k < NF; ++k) {
          A[it & 1][k] = *(const f16x8*)(abase + (7 - k) * 512 + (3 - it) * 64);
          B[it & 1][NF - 1 - k] = *(const f16x8*)(bbase + (NF - 1 - k) * 512 + (3 - it) * 64);
        }
      };
      ld(std::integral_constant<int, 0>{});
      __builtin_amdgcn_sched_barrier(0);
      static_for<0, 4>([&](auto IT) {
        constexpr int it = IT;
        if constexpr (it + 1 < 4) ld(std::integral_constant<int, it + 1>{});
        if constexpr (PF) static_for<it * (CV_PF1 / 4), (it + 1) * (CV_PF1 / 4)>([&](auto J) { pf_w2(J); });
#pragma unroll
        for (int b1 = 0; b1 < NF; ++b1)
#pragma unroll
          for (int e = 0; e < 4; ++e)
            if (b1 <= Q[e]) acc[e] = __builtin_amdgcn_mfma_f32_16x16x32_f16(A[it & 1][b1], B[it & 1][Q[e] - b1], acc[e], 0, 0, 0);
#pragma unroll
        for (int k = 0; k < 18; ++k) {
          __builtin_amdgcn_sched_group_barrier(0x8, 1, 0);
          if (it + 1 < 4 && k >= 2 && k - 2 < 2 * NF) __builtin_amdgcn_sched_group_barrier(0x100, 1, 0);
          if (PF && CV_PF1 > 0 && (k % (16 / (CV_PF1 / 4))) == 1 && k / (16 / (CV_PF1 / 4)) < CV_PF1 / 4)
            __builtin_amdgcn_sched_group_barrier(0x20, 1, 0);
        }
        __builtin_amdgcn_sched_barrier(0);
      });
    };
    if (LAYER == 0) {
      if (qs) body(std::integral_constant<int, 1>{}, std::true_type{}); else body(std::integral_constant<int, 0>{}, std::true_type{});
    } else {
      if (qs) body(std::integral_constant<int, 1>{}, std::false_type{}); else body(std::integral_constant<int, 0>{}, std::false_type{});
    }
    f32x4 keep[2];
    {
      char* wrp = smem + CV_RED + wave8 * 2048 + lane * 16;
#pragma unroll
      for (int e = 0; e < 2; ++e) {
        f32x4 give;
#pragma unroll
        for (int r = 0; r < 4; ++r) {
          const float lo = acc[e][r], hi = acc[2 + e][r];
          give[r] = half ? lo : hi;
          keep[e][r] = half ? hi : lo;
        }
        *(f32x4*)(wrp + e * 1024) = give;
      }
    }
    asm volatile("s_waitcnt lgkmcnt(0)" ::: "memory");
    __builtin_amdgcn_s_barrier();
    STAMP(KID, 2);
    {
      const char* rd = smem + CV_RED + (wave8 ^ 1) * 2048 + lane * 16;
      const int toff = 16 * j16 + 4 * g;
#pragma unroll
      for (int e = 0; e < 2; ++e) {
        const int q = half ? (e ? (qs ? 1 : 0) : (qs ? 2 : 3)) : (e ? (qs ? 5 : 4) : (qs ? 6 : 7));
        const int t = 256 * q + toff;
        const f32x4 o = *(const f32x4*)(rd + e * 1024);
        const u32x4 xs = *(const u32x4*)(smem + CV_XS + ch * 8192 + t * 4);
        const f32x4 y = keep[e] + o;
        float xo[4];
#pragma unroll
        for (int r = 0; r < 4; ++r) {
          const unsigned w = xs[r];
          xo[r] = bf_lo(w) + rbf(rbf(y[r] * kscale) + bf_hi(w));
        }
        st8_sc1(xt + ((size_t)(t >> 7) * D_ + d0 + ch) * 128 + (t & 127), u32x2{pk_bf16(xo[0], xo[1]), pk_bf16(xo[2], xo[3])});
      }
    }
    wait_vm_lgkm0<0>();
    __builtin_amdgcn_s_barrier();
    if (tid < CREP) (void)__hip_atomic_fetch_add(cflags + (tid * 16 + (d0 >> 5)) * 32, 1u, __ATOMIC_RELAXED, __HIP_MEMORY_SCOPE_AGENT);
    STAMP(KID, 3);
  }
  const int grow = lane >> 4, gslot = lane & 15;
  const unsigned short* xsrc = xt + ((size_t)mt * D_ + wave * 8 + grow) * 128 + ((gslot ^ (grow << 2)) * 8);
  const char* wsrc = w1t + (size_t)ft * 32 * 1024 + lane * 16;
  auto issue_w = [&](int c) {
    char* slot = smem + (c % G1_R) * 16384;
#pragma unroll
    for (int i = 0; i < 2; ++i)
      __builtin_amdgcn_global_load_lds((const unsigned*)(wsrc + (size_t)(c * 2 + i) * 1024),
                                       (LAS unsigned*)(slot + 8192 + (wave * 2 + i) * 1024), 16, 0, 0);
  };
  auto issue_x = [&](int c) {
    char* slot = smem + (c % G1_R) * 16384;
#pragma unroll
    for (int i = 0; i < 2; ++i)
      __builtin_amdgcn_global_load_lds((const unsigned*)(xsrc + (size_t)(c * 32 + i * 4) * 128),
                                       (LAS unsigned*)(slot + (wave * 8 + i * 4) * 256), 16, 0, 16  );
  };
  if (bi < 28 && wave8 >= 4) {
    if (wave8 < 6)
      __builtin_amdgcn_global_load_lds((const unsigned*)(ws + WS_W1S) + nt * 128 + wave * 64 + lane,
                                       (LAS unsigned*)(smem + 9 * 16384 + 256 + wave * 256), 4, 0, 0);
#pragma unroll
    for (int c = 0; c < W1PRE; ++c) issue_w(c);
  }
  __builtin_amdgcn_sched_barrier(0);
  if (LAYER == 0) static_for<CV_PF1, 28>([&](auto J) { pf_w2(J); });
  __builtin_amdgcn_sched_barrier(0);
  if (wave8 == 7) {
    const unsigned* cp = cflags + ((blockIdx.x % CREP) * 16 + (lane & 15)) * 32;
    for (int it = 0; it < (1 << 20); ++it) {
      const unsigned f = __hip_atomic_load(cp, __ATOMIC_RELAXED, __HIP_MEMORY_SCOPE_AGENT);
      if (__all(f >= 16u)) break;
      __builtin_amdgcn_s_sleep(POLL_SLEEP);
    }
  }
  __builtin_amdgcn_s_barrier();
  STAMP(KID, 4);
  if (bi >= 28) {
    const int ht = bx * 4 + (bi - 28);
    const int cgp = wave8 * 2 + hh;
    const auto rsrc = __builtin_amdgcn_make_buffer_rsrc((void*)xt, 0, L_ * D_ * 2, 0x00020000);
    const int off0 = ((ht >> 1) * D_ * 128 + (ht & 1) * 64 + 2 * r32) * 2;
    const unsigned pv = __builtin_amdgcn_raw_buffer_load_b32(rsrc, off0, 0, 16  );
    unsigned v[32];
#pragma unroll
    for (int j = 0; j < 32; ++j) v[j] = __builtin_amdgcn_raw_buffer_load_b32(rsrc, off0 + (cgp * 32 + j) * 256, 0, 16  );
    static_for<0, 8>([&](auto J) { pf_xr(J); });
    const f32x2 piv = {bf_lo(pv), bf_hi(pv)};
    f32x2 s1 = {0.f, 0.f}, s2 = {0.f, 0.f};
#pragma unroll
    for (int j = 0; j < 32; ++j) {
      const f32x2 dd = f32x2{bf_lo(v[j]), bf_hi(v[j])} - piv;
      s1 += dd;
      s2 = __builtin_elementwise_fma(dd, dd, s2);
    }
    f32x4* red = (f32x4*)smem;
    red[cgp * 32 + r32] = f32x4{s1[0], s2[0], s1[1], s2[1]};
    asm volatile("s_waitcnt lgkmcnt(0)" ::: "memory");
    __builtin_amdgcn_s_barrier();
    if (tid < 64) {
      const f32x2* r2 = (const f32x2*)smem;
      float a = 0.f, q = 0.f;
#pragma unroll
      for (int w = 0; w < 16; ++w) { const f32x2 t = r2[w * 64 + tid]; a += t[0]; q += t[1]; }
      const unsigned pw = __shfl(pv, tid >> 1, 64);
      const float dm = a * (1.f / D_);
      const float var = fmaxf(q * (1.f / D_) - dm * dm, 0.f);
      const float mm = ((tid & 1) ? bf_hi(pw) : bf_lo(pw)) + dm;
      const float sdv = __builtin_amdgcn_sqrtf(rbf(rbf(var) + 1e-5f));
      __hip_atomic_store(sw + ht * 64 + tid, pk_bf16(mm, sdv), __ATOMIC_RELAXED, __HIP_MEMORY_SCOPE_AGENT);
    }
    STAMP(KID, 12);
  } else if (wave8 >= 4) {
#pragma unroll
    for (int c = 0; c < G1_W0; ++c) { issue_x(c); if (c >= W1PRE) issue_w(c); }
    static_for<0, 16>([&](auto C) {
      constexpr int c = C;
      constexpr int extra = (c + G1_W0 > 16) ? 8 : 0;
      wait_vm<g1_young(c) + extra>();
      __builtin_amdgcn_s_barrier();
      if constexpr (c + G1_W0 < 16) { issue_x(c + G1_W0); if (c + G1_W0 >= W1PRE) issue_w(c + G1_W0); }
      if constexpr (c + G1_W0 == 16) {
        __builtin_amdgcn_sched_barrier(0);
        static_for<0, 8>([&](auto J) { pf_xr(J); });
        __builtin_amdgcn_sched_barrier(0);
      }
    });
  } else {
    const int blk = (lane >> 4) & 1, q4 = (lane & 15) >> 2, p4 = lane & 3;
    const unsigned sbase = (unsigned)(uintptr_t)(LAS char*)smem;
    unsigned* lcnt = (unsigned*)(smem + 9 * 16384);
    unsigned tb[3][4], ta[3];
#pragma unroll
    for (int ct = 0; ct < 4; ++ct) {
      tb[0][ct] = sbase + (unsigned)((8 * hh + q4) * 256 + (((4 * ct + 2 * blk + (p4 >> 1)) ^ (q4 << 2)) * 16) + 8 * (p4 & 1));
      tb[1][ct] = tb[0][ct] + 65536u; tb[2][ct] = tb[0][ct] + 131072u;
    }
    ta[0] = sbase + 8192 + wave * 2048 + lane * 16; ta[1] = ta[0] + 65536u; ta[2] = ta[0] + 131072u;
    f32x16 acc[4];
#pragma unroll
    for (int ct = 0; ct < 4; ++ct)
#pragma unroll
      for (int i = 0; i < 16; ++i) acc[ct][i] = 0.f;
    constexpr int LA = 6;
    u32x2 bb[8][2];
    u32x4 af[3];
    unsigned swv[4];
    f32x4 ws4[4];
    auto issue_pair = [&](auto NN) {
      constexpr int n = NN, ks = n >> 2, ct = n & 3;
      constexpr int so = ((ks >> 1) % G1_R) * 16384, seg = so >> 16, o = (so & 65535) + (ks & 1) * 4096;
      if constexpr (ct == 0) lds_read16<(so & 65535) + (ks & 1) * 1024>(af[ks % 3], ta[seg]);
      tr_read<o>(bb[n & 7][0], tb[seg][ct]);
      tr_read<o + 1024>(bb[n & 7][1], tb[seg][ct]);
    };
    __builtin_amdgcn_s_barrier();
    STAMP(KID, 5);
    static_for<0, LA>([&](auto NN) { issue_pair(NN); });
    static_for<0, 128>([&](auto NN) {
      constexpr int n = NN, ks = n >> 2, ct = n & 3;
      constexpr int last = (n + LA - 1 < 127) ? (n + LA - 1) : 127;
      constexpr int younger = 2 * (last - n) + ((last >> 2) - (n >> 2));
      if constexpr (ct == 0) wait_lgkm3<younger>(bb[n & 7][0], bb[n & 7][1], af[ks % 3]);
      else wait_lgkm2<younger>(bb[n & 7][0], bb[n & 7][1]);
      const u32x4 bu = {bb[n & 7][0][0], bb[n & 7][0][1], bb[n & 7][1][0], bb[n & 7][1][1]};
      acc[ct] = __builtin_amdgcn_mfma_f32_32x32x16_bf16(__builtin_bit_cast(bf16x8, af[ks % 3]), __builtin_bit_cast(bf16x8, bu), acc[ct], 0, 0, 0);
      __builtin_amdgcn_sched_barrier(0);
      if constexpr (n + LA < 128) {
        if constexpr ((n + LA) % 8 == 0) __builtin_amdgcn_s_barrier();
        issue_pair(std::integral_constant<int, n + LA>{});
      }
      if constexpr (n >= 104 && n < 108)
        swv[n - 104] = __hip_atomic_load(sw + mt * 128 + (n - 104) * 32 + r32, __ATOMIC_RELAXED, __HIP_MEMORY_SCOPE_AGENT);
      __builtin_amdgcn_sched_barrier(0);
    });
    STAMP(KID, 6);
    for (int it = 0; it < (1 << 20); ++it) {
      if (__all(swv[0] != 0u && swv[1] != 0u && swv[2] != 0u && swv[3] != 0u)) break;
      __builtin_amdgcn_s_sleep(1);
#pragma unroll
      for (int ct = 0; ct < 4; ++ct)
        swv[ct] = __hip_atomic_load(sw + mt * 128 + ct * 32 + r32, __ATOMIC_RELAXED, __HIP_MEMORY_SCOPE_AGENT);
    }
    asm volatile("" : "+v"(swv[0]), "+v"(swv[1]), "+v"(swv[2]), "+v"(swv[3]));
#pragma unroll
    for (int q = 0; q < 4; ++q) ws4[q] = *(const f32x4*)(smem + 9 * 16384 + 256 + (wave * 32 + 8 * q + 4 * hh) * 4);
#pragma unroll
    for (int ct = 0; ct < 4; ++ct) {
      const float rs = __builtin_amdgcn_rcpf(bf_hi(swv[ct]));
      const float cm = -bf_lo(swv[ct]) * rs;
      char* dst = hbuf + ((size_t)((mt * 4 + ct) * 112 + ft * 2) * 64 + lane) * 16;
      const f32x2 c2 = {cm, cm}, r2 = {rs, rs};
#pragma unroll
      for (int s = 0; s < 2; ++s) {
        u32x4 o;
#pragma unroll
        for (int j = 0; j < 4; ++j) {
          const int reg = 8 * s + 2 * j;
          const f32x2 wsum = {ws4[reg >> 2][reg & 3], ws4[reg >> 2][(reg & 3) + 1]};
          const f32x2 a2 = {acc[ct][reg], acc[ct][reg + 1]};
          const f32x2 v2 = __builtin_elementwise_fma(a2, r2, c2 * wsum);
          const s16x2 pv = __builtin_bit_cast(s16x2, __builtin_convertvector(v2, bf16x2));
          const s16x2 z = {0, 0};
          o[j] = __builtin_bit_cast(unsigned, __builtin_elementwise_max(pv, z));
        }
        st16_sc1(dst + s * 1024, o);
      }
    }
    STAMP(KID, 7);
    wait_vm<0>();
    if (lane == 0) {
      const unsigned old = __hip_atomic_fetch_add(lcnt, 1u, __ATOMIC_RELAXED, __HIP_MEMORY_SCOPE_WORKGROUP);
      if (old == 3u) __hip_atomic_store(flags + (mt * 16 + nt) * FLS, 1u, __ATOMIC_RELAXED, __HIP_MEMORY_SCOPE_AGENT);
    }
    STAMP(KID, 8);
    static_for<0, 8>([&](auto J) { pf_xr(J); });
  }
  pin7(wr); pin7(wr + 7); pin7(wr + 14); pin7(wr + 21);
  {
    unsigned x0_ = xr[0], x1_ = xr[1], x2_ = xr[2], x3_ = xr[3], x4_ = xr[4], x5_ = xr[5], x6_ = xr[6], x7_ = xr[7];
    asm volatile("" : "+v"(x0_), "+v"(x1_), "+v"(x2_), "+v"(x3_), "+v"(x4_), "+v"(x5_), "+v"(x6_), "+v"(x7_));
    xr[0] = x0_; xr[1] = x1_; xr[2] = x2_; xr[3] = x3_; xr[4] = x4_; xr[5] = x5_; xr[6] = x6_; xr[7] = x7_;
  }
  __builtin_amdgcn_s_barrier();
  if (wave8 == 7) {
    const unsigned* fp = flags + ((mt2 >> 1) * 16 + (lane < 14 ? lane : 0)) * FLS;
    for (int it = 0; it < (1 << 20); ++it) {
      const unsigned f = __hip_atomic_load(fp, __ATOMIC_RELAXED, __HIP_MEMORY_SCOPE_AGENT);
      if (__all(f != 0u)) break;
      __builtin_amdgcn_s_sleep(POLL_SLEEP);
    }
  }
  asm volatile("s_waitcnt lgkmcnt(0)" ::: "memory");
  __builtin_amdgcn_s_barrier();
  STAMP(KID, 9);
  const char* hsrc = hbuf + (size_t)(mt2 * 2) * 112 * 1024 + wave8 * 1024 + lane * 16;
  char* dstb = smem + wave8 * 2048;
  auto issue2 = [&](int c) {
#pragma unroll
    for (int mi = 0; mi < 2; ++mi)
      __builtin_amdgcn_global_load_lds((const unsigned*)(hsrc + (size_t)mi * 112 * 1024 + c * 8192),
                                       (LAS unsigned*)(dstb + (c % G2_R) * 16384 + mi * 1024), 16, 0, 16  );
  };
  f32x16 acc[2];
#pragma unroll
  for (int i = 0; i < 16; ++i) { acc[0][i] = 0.f; acc[1][i] = 0.f; }
  const char* rdh = smem + kq * 4096 + lane * 16;
  bf16x8 hf[2][4];
#pragma unroll
  for (int c = 0; c < g2_iss(0); ++c) issue2(c);
  static_for<0, 15>([&](auto C) {
    constexpr int c = C;
    auto mma = [&](auto JJ) {
      constexpr int j = JJ;
      if constexpr (c >= 1) {
        constexpr int pc = c - 1;
#pragma unroll
        for (int mi = 0; mi < 2; ++mi)
          acc[mi] = __builtin_amdgcn_mfma_f32_32x32x16_bf16(__builtin_bit_cast(bf16x8, wr[2 * pc + j]), hf[pc & 1][j * 2 + mi], acc[mi], 0, 0, 0);
      }
    };
    mma(std::integral_constant<int, 0>{});
    __builtin_amdgcn_sched_barrier(0);
    if constexpr (c < 14) {
      wait_vm_lgkm0<2 * (g2_iss(c) - (c + 1))>();
      __builtin_amdgcn_s_barrier();
      if constexpr (c == 0) STAMP(KID, 10);
#pragma unroll
      for (int e = 0; e < 4; ++e) hf[c & 1][e] = *(const bf16x8*)(rdh + (c % G2_R) * 16384 + e * 1024);
    }
    __builtin_amdgcn_sched_barrier(0);
    mma(std::integral_constant<int, 1>{});
    __builtin_amdgcn_sched_barrier(0);
    if constexpr (c < 14) {
#pragma unroll
      for (int cn = g2_iss(c); cn < g2_iss(c + 1); ++cn) issue2(cn);
    }
    __builtin_amdgcn_sched_barrier(0);
  });
  STAMP(KID, 11);
  char* red = smem + 5 * 16384;
  {
#pragma unroll
    for (int pc = 0; pc < 4; ++pc) {
      if (pc != kq) {
        const int slot = (kq > pc) ? kq - 1 : kq;
        char* dst = red + ((pc * 2 + ni) * 3 + slot) * 2048 + lane * 16;
        f32x4 g0, g1;
#pragma unroll
        for (int i = 0; i < 4; ++i) { g0[i] = acc[pc >> 1][8 * (pc & 1) + i]; g1[i] = acc[pc >> 1][8 * (pc & 1) + 4 + i]; }
        *(f32x4*)dst = g0;
        *(f32x4*)(dst + 1024) = g1;
      }
    }
  }
  float mine[8];
#pragma unroll
  for (int i = 0; i < 8; ++i) {
    const float a0 = kh_f ? acc[0][8 + i] : acc[0][i];
    const float a1 = kh_f ? acc[1][8 + i] : acc[1][i];
    mine[i] = mi_f ? a1 : a0;
  }
  __syncthreads();
  {
    const char* src = red + (wave8 * 3) * 2048 + lane * 16;
#pragma unroll
    for (int sl = 0; sl < 3; ++sl) {
      const f32x4 o0 = *(const f32x4*)(src + sl * 2048);
      const f32x4 o1 = *(const f32x4*)(src + sl * 2048 + 1024);
#pragma unroll
      for (int i = 0; i < 4; ++i) { mine[i] += o0[i]; mine[4 + i] += o1[i]; }
    }
  }
  float xo[8];
  float s1 = 0.f, s2 = 0.f;
#pragma unroll
  for (int i = 0; i < 8; ++i) {
    const float v = rbf(__uint_as_float(((unsigned)xr[i]) << 16) + rbf(mine[i]));
    xo[i] = v;
    s1 += v; s2 += v * v;
  }
  if (LAYER == 1) {
#pragma unroll
    for (int q = 0; q < 2; ++q)
      *(u32x2*)(p.out + (size_t)tok * D_ + dbase + 8 * q) =
          u32x2{pk_bf16(xo[4 * q], xo[4 * q + 1]), pk_bf16(xo[4 * q + 2], xo[4 * q + 3])};
  } else {
    unsigned short* xout = (unsigned short*)(ws + WS_XT2);
#pragma unroll
    for (int i = 0; i < 8; ++i)
      __hip_atomic_store(xout + (size_t)(dbase + (i & 3) + 8 * (i >> 2)) * L_ + tok,
                         (unsigned short)(__float_as_uint(xo[i]) >> 16), __ATOMIC_RELAXED, __HIP_MEMORY_SCOPE_AGENT);
    s1 += __shfl_xor(s1, 32, 64);
    s2 += __shfl_xor(s2, 32, 64);
    float* sred = (float*)(smem + 8 * 16384);
    sred[wave8 * 64 + lane] = hh ? s2 : s1;
    asm volatile("s_waitcnt lgkmcnt(0)" ::: "memory");
    __builtin_amdgcn_s_barrier();
    if (wave8 < 2) {
      const int w0 = wave8 * 4;
      const float t = sred[w0 * 64 + lane] + sred[(w0 + 1) * 64 + lane] + sred[(w0 + 2) * 64 + lane] + sred[(w0 + 3) * 64 + lane];
      (void)__hip_atomic_fetch_add((float*)(ws + WS_ST1) + 2 * (mt2 * 64 + wave8 * 32 + r32) + hh, t, __ATOMIC_RELAXED, __HIP_MEMORY_SCOPE_AGENT);
    }
    wait_vm_lgkm0<0>();
    __builtin_amdgcn_s_barrier();
    if (tid < CREP) (void)__hip_atomic_fetch_add(eflags + (tid * 16 + (blockIdx.x & 15)) * 32, 1u, __ATOMIC_RELAXED, __HIP_MEMORY_SCOPE_AGENT);
  }
  STAMP(KID, 15);
  }
}

extern "C" void kernel_launch(void* const* d_in, const int* in_sizes, int n_in,
                              void* d_out, int out_size, void* d_ws, size_t ws_size,
                              hipStream_t stream) {
  const unsigned short* x0 = (const unsigned short*)d_in[0];
  const unsigned short* logA = (const unsigned short*)d_in[1];
  const unsigned short* Aim = (const unsigned short*)d_in[2];
  const unsigned short* Cre = (const unsigned short*)d_in[3];
  const unsigned short* Cim = (const unsigned short*)d_in[4];
  const unsigned short* logdt = (const unsigned short*)d_in[5];
  const unsigned short* dskip = (const unsigned short*)d_in[6];
  const unsigned short* w1 = (const unsigned short*)d_in[8];
  const unsigned short* w2 = (const unsigned short*)d_in[9];
  char* ws = (char*)d_ws;
  (void)hipFuncSetAttribute((const void*)s4d_layer_kernel, hipFuncAttributeMaxDynamicSharedMemorySize, LAYER_LDS);
  prep_kernel<<<PREP_BLOCKS, 256, 0, stream>>>(x0, logA, Aim, Cre, Cim, logdt, w1, w2, ws);
  LayerArgs a0 = {dskip, (unsigned short*)d_out, ws};
  s4d_layer_kernel<<<256, 512, LAYER_LDS, stream>>>(a0);
}
```

```cpp
#include <hip/hip_runtime.h>
#include <hip/hip_bf16.h>
#include <stdint.h>

typedef __attribute__((ext_vector_type(8))) __bf16 bf16x8;
typedef __attribute__((ext_vector_type(2))) __bf16 bf16x2;
typedef __attribute__((ext_vector_type(4))) short s16x4;
typedef __attribute__((ext_vector_type(2))) short s16x2;
typedef __attribute__((ext_vector_type(8))) short s16x8;
typedef __attribute__((ext_vector_type(8))) _Float16 f16x8;
typedef __attribute__((ext_vector_type(2))) _Float16 f16x2;
typedef __attribute__((ext_vector_type(2))) float f32x2;
typedef __attribute__((ext_vector_type(4))) float f32x4;
typedef __attribute__((ext_vector_type(16))) float f32x16;
typedef __attribute__((ext_vector_type(4))) unsigned u32x4;
typedef __attribute__((ext_vector_type(2))) unsigned u32x2;
#define LAS __attribute__((address_space(3)))
#define DI __device__ __forceinline__

constexpr int L_ = 2048, D_ = 512, N_ = 128, F_ = 1792;

constexpr size_t MiB = 1024 * 1024;
constexpr size_t WS_G    = 0;
constexpr size_t WS_XT0  = 2 * MiB;
constexpr size_t WS_XT1  = 4 * MiB;
constexpr size_t WS_XT2  = 6 * MiB;
constexpr size_t WS_XT3  = 8 * MiB;
constexpr size_t WS_W1T  = 10 * MiB;
constexpr size_t WS_W2T  = 12 * MiB;
constexpr size_t WS_H    = 14 * MiB;
constexpr size_t WS_H2   = 24 * MiB;
constexpr size_t WS_KINV = 22 * MiB;
constexpr size_t WS_W1S  = WS_KINV + 4096;
constexpr size_t WS_Z0   = 23 * MiB;
constexpr size_t WS_ST1  = WS_Z0;
constexpr size_t WS_SW   = WS_ST1 + 16384;
#define FLS 32
#define CREP 8
#define CFS (3 * CREP)
constexpr size_t WS_FL   = WS_SW + 16384;
constexpr size_t WS_CF   = WS_FL + 4096 * FLS;
constexpr size_t WS_ZBYTES = 16384 + 16384 + 4096 * FLS + 2048 * CFS;
constexpr size_t WS_SW0  = WS_Z0 + 512 * 1024;

#define STAMP(kid, i)
#define STAMPT(kid, i, T)
DI float bf_lo(unsigned u) { return __uint_as_float(u << 16); }
DI float bf_hi(unsigned u) { return __uint_as_float(u & 0xffff0000u); }
DI unsigned pk_bf16(float a, float b) {
  f32x2 v = {a, b};
  return __builtin_bit_cast(unsigned, __builtin_convertvector(v, bf16x2));
}
DI unsigned pk_f16(float a, float b) {
  f32x2 v = {a, b};
  return __builtin_bit_cast(unsigned, __builtin_convertvector(v, f16x2));
}
DI float rbf(float a) { return bf_lo(pk_bf16(a, 0.f)); }
DI float ld_bf(const unsigned short* p) { return __uint_as_float(((unsigned)*p) << 16); }
DI unsigned perm_lo(unsigned hi, unsigned lo) { return __builtin_amdgcn_perm(hi, lo, 0x05040100u); }
DI unsigned perm_hi(unsigned hi, unsigned lo) { return __builtin_amdgcn_perm(hi, lo, 0x07060302u); }
template <int CTRL> DI float dpp_add(float v) {
  return v + __uint_as_float(__builtin_amdgcn_update_dpp(0u, __float_as_uint(v), CTRL, 0xf, 0xf, true));
}
DI float wave_sum(float v) {
  v = dpp_add<0xB1>(v);
  v = dpp_add<0x4E>(v);
  v = dpp_add<0x141>(v);
  v = dpp_add<0x140>(v);
  const unsigned u = __float_as_uint(v);
  return (__uint_as_float(__builtin_amdgcn_readlane(u, 0)) + __uint_as_float(__builtin_amdgcn_readlane(u, 16))) +
         (__uint_as_float(__builtin_amdgcn_readlane(u, 32)) + __uint_as_float(__builtin_amdgcn_readlane(u, 48)));
}
#define ST_FLAGS ""
DI void st16(void* p, u32x4 v) { asm volatile("global_store_dwordx4 %0, %1, off " ST_FLAGS "\n\ts_nop 1" ::"v"(p), "v"(v) : "memory"); }
DI void st8(void* p, u32x2 v) { asm volatile("global_store_dwordx2 %0, %1, off " ST_FLAGS "\n\ts_nop 1" ::"v"(p), "v"(v) : "memory"); }
DI void st2(void* p, unsigned v) { asm volatile("global_store_short %0, %1, off " ST_FLAGS "\n\ts_nop 1" ::"v"(p), "v"(v) : "memory"); }
template <int N> DI void wait_vm() { asm volatile("s_waitcnt vmcnt(%0)" ::"n"(N) : "memory"); }
template <int OFF> DI void gload16(u32x4& dst, const char* p) {
  asm volatile("global_load_dwordx4 %0, %1, off offset:%2" : "=v"(dst) : "v"(p), "n"(OFF));
}
template <int N> DI void wait_vm4(u32x4& a, u32x4& b, u32x4& c, u32x4& d) {
  asm volatile("s_waitcnt vmcnt(%4)" : "+v"(a), "+v"(b), "+v"(c), "+v"(d) : "n"(N) : "memory");
}
template <int OFF> DI void tr_read(u32x2& d, unsigned addr) {
  asm volatile("ds_read_b64_tr_b16 %0, %1 offset:%2" : "=v"(d) : "v"(addr), "n"(OFF));
}
template <int N> DI void wait_lgkm8(u32x2* b) {
  asm volatile("s_waitcnt lgkmcnt(%8)"
               : "+v"(b[0]), "+v"(b[1]), "+v"(b[2]), "+v"(b[3]), "+v"(b[4]), "+v"(b[5]), "+v"(b[6]), "+v"(b[7])
               : "n"(N) : "memory");
}
template <int N> DI void wait_vm_lgkm0() { asm volatile("s_waitcnt vmcnt(%0) lgkmcnt(0)" ::"n"(N) : "memory"); }
template <int I, int E, class F> DI void static_for(F&& f) {
  if constexpr (I < E) { f(std::integral_constant<int, I>{}); static_for<I + 1, E>(f); }
}

constexpr int TROW = 528;

__host__ __device__ constexpr int tab_w(int idx) {
  return idx < 16 ? idx : (idx < 40 ? ((8 + ((idx - 16) & 7)) << (1 + ((idx - 16) >> 3))) : 128);
}
__host__ __device__ constexpr int ilog2c(int w) { return w <= 1 ? 0 : 1 + ilog2c(w >> 1); }
__host__ __device__ constexpr int tab_idx(int w) {
  return w < 16 ? w : (w == 128 ? 40 : 16 + 8 * (ilog2c(w) - 4) + ((w >> (ilog2c(w) - 3)) - 8));
}
DI int tab_idx_rt(int w) {
  if (w < 16) return w;
  int lg = 31 - __clz(w);
  return 16 + 8 * (lg - 4) + ((w >> (lg - 3)) - 8);
}

DI void retile_load(int u, const unsigned short* __restrict__ w1, const unsigned short* __restrict__ w2, u32x2* r, size_t& dst) {
  if (u < 28672) {
    const int fg = u % 448, o = u / 448;
    const int f = 4 * fg;
#pragma unroll
    for (int j = 0; j < 8; ++j) r[j] = *(const u32x2*)(w1 + (size_t)(8 * o + j) * F_ + f);
    dst = WS_W1T + ((size_t)(((f >> 5) * 32 + (o >> 1)) * 64 + (o & 1) * 32 + (f & 31))) * 16;
  } else {
    const int v = u - 28672;
    const int dg = v & 127, o = v >> 7;
    const int dd = 4 * dg, ks = o >> 1, hh = o & 1;
#pragma unroll
    for (int j = 0; j < 8; ++j)
      r[j] = *(const u32x2*)(w2 + (size_t)(16 * ks + 8 * (j >> 2) + 4 * hh + (j & 3)) * D_ + dd);
    dst = WS_W2T + ((size_t)(((dd >> 5) * 112 + ks) * 64 + hh * 32 + (dd & 31))) * 16;
  }
}
DI void retile_store(const u32x2* r, char* ws, size_t dst) {
#pragma unroll
  for (int c = 0; c < 2; ++c) {
    u32x4 lo = {perm_lo(r[1][c], r[0][c]), perm_lo(r[3][c], r[2][c]), perm_lo(r[5][c], r[4][c]), perm_lo(r[7][c], r[6][c])};
    u32x4 hi = {perm_hi(r[1][c], r[0][c]), perm_hi(r[3][c], r[2][c]), perm_hi(r[5][c], r[4][c]), perm_hi(r[7][c], r[6][c])};
    st16(ws + dst + (2 * c) * 16, lo);
    st16(ws + dst + (2 * c + 1) * 16, hi);
  }
}

#define LAYER_CODE 27648
#define PREP_CODE 14336
template <int BYTES> DI unsigned warm_code(const char* pc, int t) {
  const int o = t * 128;
  return *(const unsigned*)(pc + (o < BYTES ? o : 0));
}
constexpr int PB_KGEN = 512, PB_XT = 64, PB_ST = 64, PB_RT = 112, PB_WS = 16;
constexpr int PREP_BLOCKS = PB_KGEN + PB_XT + PB_ST + PB_RT + PB_WS;

__global__ __launch_bounds__(256) void prep_kernel(
    const unsigned short* __restrict__ x0, const unsigned short* __restrict__ logA,
    const unsigned short* __restrict__ Aim, const unsigned short* __restrict__ Cre,
    const unsigned short* __restrict__ Cim, const unsigned short* __restrict__ logdt,
    const unsigned short* __restrict__ w1, const unsigned short* __restrict__ w2, char* __restrict__ ws) {
  __shared__ __attribute__((aligned(16))) char smem[82 * TROW + 1536];
  float* red = (float*)smem;
  const int tid = threadIdx.x, lane = tid & 63;
  const int wave = __builtin_amdgcn_readfirstlane(tid >> 6);
  int b = blockIdx.x;
  STAMP(0, 0);
  unsigned cw1 = 0u;
  if (b >= PB_KGEN && b < PB_KGEN + PB_XT) cw1 = warm_code<PREP_CODE>((const char*)__builtin_amdgcn_s_getpc(), tid);
  if (b < PB_KGEN) {
    const int d = ((b & 7) * 32 + ((b >> 3) & 31)) * 2 + (b >> 8);
    const int isH = wave >> 1;
    const int n = (wave & 1) * 64 + lane;
    const float pdt = ld_bf(logdt + d), pla = ld_bf(logA + d * N_ + n), pai = ld_bf(Aim + d * N_ + n);
    const float p0r = ld_bf(Cre + d * N_ + lane), p0i = ld_bf(Cim + d * N_ + lane);
    const float p1r = ld_bf(Cre + d * N_ + 64 + lane), p1i = ld_bf(Cim + d * N_ + 64 + lane);
    const float dt = rbf(expf(pdt));
    const float ar = -rbf(expf(pla)) * dt;
    const float ai = pai * (isH ? -dt : dt);
    const float c0r = p0r * dt, c0i = p0i * dt, c1r = p1r * dt, c1i = p1i * dt;
    const float S = wave_sum(2.f * (fabsf(c0r) + fabsf(c0i) + fabsf(c1r) + fabsf(c1i)));
    int ex = 0;
    (void)frexpf(S, &ex);
    if (!(S > 0.f) || !(S < 3.0e38f)) ex = 15;
    ex = max(-100, min(ex, 120));
    const float scale = ldexpf(1.f, 15 - ex);
    const float cr = (wave & 1) ? c1r : c0r, ci = (wave & 1) ? c1i : c0i;
    const float base = isH ? 16.f : 1.f;
    const float er = ar * 1.44269504088896340736f * base;
    const float IH = 0.15915494309189533577f;
    const float IL = (float)(0.15915494309189533577 - (double)0.15915494309189533577f);
    float fh = ai * IH;
    float fl = fmaf(ai, IH, -fh) + ai * IL;
    fh *= base; fl *= base;
    STAMP(0, 1);
    f32x2 Er2[8], Ei2[8];
#pragma unroll
    for (int k = 0; k < 8; ++k) {
      const float pw = (float)(1 << k);
      const float mag = __builtin_amdgcn_exp2f(er * pw);
      const float ph = __builtin_amdgcn_fractf(fh * pw) + fl * pw;
      const float re = mag * __builtin_amdgcn_cosf(ph), im = mag * __builtin_amdgcn_sinf(ph);
      Er2[k] = f32x2{re, re};
      Ei2[k] = f32x2{-im, im};
    }
    f32x2 tv[41];
    tv[0] = isH ? f32x2{2.f * scale * cr, -2.f * scale * ci} : f32x2{1.f, 0.f};
    char* trow = smem + isH * (41 * TROW) + n * 4;
    *(unsigned*)trow = __builtin_bit_cast(unsigned, __builtin_convertvector(tv[0], f16x2));
    static_for<1, 41>([&](auto I) {
      constexpr int idx = I;
      constexpr int w = tab_w(idx);
      constexpr int lg = ilog2c(w);
      constexpr int ridx = tab_idx(w - (1 << lg));
      const f32x2 sw = __builtin_shufflevector(tv[ridx], tv[ridx], 1, 0);
      tv[idx] = __builtin_elementwise_fma(Ei2[lg], sw, Er2[lg] * tv[ridx]);
      *(unsigned*)(trow + idx * TROW) = __builtin_bit_cast(unsigned, __builtin_convertvector(tv[idx], f16x2));
    });
    int cix[8];
#pragma unroll
    for (int e = 0; e < 8; ++e) {
      const int l = 2047 - (tid * 8 + e);
      const int sh = max((31 - __clz(l | 1)) - 7, 0);
      const int q = (l + ((1 << sh) >> 1) - (sh > 0 ? 1 : 0) + ((l >> sh) & (sh > 0 ? 1 : 0))) >> sh;
      cix[e] = (sh > 0) ? (128 * sh + q) : l;
    }
    STAMP(0, 2);
    __syncthreads();
    _Float16* kc = (_Float16*)(smem + 82 * TROW);
    {
      const int s = wave, i16 = lane & 15, g = lane >> 4;
      const int hw = (s == 0) ? i16 : (i16 < 8 ? ((8 + i16) << s) : 128);
      const char* arow = smem + 41 * TROW + tab_idx_rt(hw) * TROW + g * 16;
      const char* brow = smem + tab_idx_rt(i16 << s) * TROW + g * 16;
      f16x8 af[8], bf[8];
#pragma unroll
      for (int ks = 0; ks < 8; ++ks) { af[ks] = *(const f16x8*)(arow + ks * 64); bf[ks] = *(const f16x8*)(brow + ks * 64); }
      f32x4 k0 = {0.f, 0.f, 0.f, 0.f}, k1 = {0.f, 0.f, 0.f, 0.f};
#pragma unroll
      for (int ks = 0; ks < 8; ks += 2) {
        k0 = __builtin_amdgcn_mfma_f32_16x16x32_f16(af[ks], bf[ks], k0, 0, 0, 0);
        k1 = __builtin_amdgcn_mfma_f32_16x16x32_f16(af[ks + 1], bf[ks + 1], k1, 0, 0, 0);
      }
#pragma unroll
      for (int r = 0; r < 4; ++r) {
        const int i = 4 * g + r;
        const int cidx = (s == 0) ? (16 * i + i16) : (256 + 128 * (s - 1) + 16 * i + i16);
        const bool ok = (s == 0) || (i < 8) || (s == 3 && i == 8 && i16 == 0);
        if (ok) kc[cidx] = (_Float16)(k0[r] + k1[r]);
      }
    }
    __syncthreads();
    STAMP(0, 3);
    unsigned short o[8];
#pragma unroll
    for (int e = 0; e < 8; ++e) o[e] = ((const unsigned short*)kc)[cix[e]];
    *(u32x4*)(ws + WS_G + (size_t)d * 4096 + tid * 16) =
        u32x4{(unsigned)o[0] | ((unsigned)o[1] << 16), (unsigned)o[2] | ((unsigned)o[3] << 16),
              (unsigned)o[4] | ((unsigned)o[5] << 16), (unsigned)o[6] | ((unsigned)o[7] << 16)};
    if (tid == 0) ((float*)(ws + WS_KINV))[d] = ldexpf(1.f, ex - 15);
    STAMP(0, 15);
    return;
  }
  b -= PB_KGEN;
  if (b < PB_XT) {
    unsigned long long lpc;
    asm volatile("s_getpc_b64 s[40:41]\n\ts_add_u32 s40, s40, s4d_layer_kernel@rel32@lo+4\n\t"
                 "s_addc_u32 s41, s41, s4d_layer_kernel@rel32@hi+12\n\ts_mov_b64 %0, s[40:41]"
                 : "=s"(lpc) :: "scc", "s40", "s41");
    const unsigned cw2 = warm_code<LAYER_CODE>((const char*)lpc, tid);
    const int tile = b * 4 + wave, tt = tile >> 3, ct = tile & 7;
    const int tg = lane & 7, cg = lane >> 3;
    const unsigned short* src = x0 + (size_t)(tt * 64 + tg * 8) * D_ + ct * 64 + cg * 8;
    u32x4 r[8];
#pragma unroll
    for (int j = 0; j < 8; ++j) r[j] = *(const u32x4*)(src + (size_t)j * D_);
    unsigned short* dst = (unsigned short*)(ws + WS_XT0) + (size_t)(ct * 64 + cg * 8) * L_ + tt * 64 + tg * 8;
#pragma unroll
    for (int c = 0; c < 4; ++c) {
      u32x4 lo = {perm_lo(r[1][c], r[0][c]), perm_lo(r[3][c], r[2][c]), perm_lo(r[5][c], r[4][c]), perm_lo(r[7][c], r[6][c])};
      u32x4 hi = {perm_hi(r[1][c], r[0][c]), perm_hi(r[3][c], r[2][c]), perm_hi(r[5][c], r[4][c]), perm_hi(r[7][c], r[6][c])};
      *(u32x4*)(dst + (size_t)(2 * c) * L_) = lo;
      *(u32x4*)(dst + (size_t)(2 * c + 1) * L_) = hi;
    }
    asm volatile("" ::"v"(cw1), "v"(cw2));
    STAMP(0, 15);
    return;
  }
  b -= PB_XT;
  if (b < PB_ST) {
    const int t0 = b * 32 + wave * 8;
    u32x4 r[8];
#pragma unroll
    for (int j = 0; j < 8; ++j) r[j] = *(const u32x4*)(x0 + (size_t)(t0 + j) * D_ + lane * 8);
    for (int i = b * 256 + tid; i < (int)(WS_ZBYTES / 16); i += PB_ST * 256) ((u32x4*)(ws + WS_Z0))[i] = u32x4{0u, 0u, 0u, 0u};
    asm volatile("" : "+v"(r[0]), "+v"(r[1]), "+v"(r[2]), "+v"(r[3]), "+v"(r[4]), "+v"(r[5]), "+v"(r[6]), "+v"(r[7]));
    unsigned word = 0u;
#pragma unroll
    for (int j = 0; j < 8; ++j) {
      float s = 0.f;
#pragma unroll
      for (int c = 0; c < 4; ++c) s += bf_lo(r[j][c]) + bf_hi(r[j][c]);
      const float mean = wave_sum(s) * (1.f / D_);
      float q = 0.f;
#pragma unroll
      for (int c = 0; c < 4; ++c) {
        const float a = bf_lo(r[j][c]) - mean, bb = bf_hi(r[j][c]) - mean;
        q += a * a + bb * bb;
      }
      const float var = wave_sum(q) * (1.f / D_);
      const unsigned wv = pk_bf16(mean, sqrtf(rbf(rbf(var) + 1e-5f)));
      if (lane == j) word = wv;
    }
    if (lane < 8) ((unsigned*)(ws + WS_SW0))[t0 + lane] = word;
    STAMP(0, 15);
    return;
  }
  b -= PB_ST;
  if (b < PB_RT) {
    u32x2 r0[8], r1[8];
    size_t d0_, d1_;
    retile_load(b * 512 + tid, w1, w2, r0, d0_);
    retile_load(b * 512 + 256 + tid, w1, w2, r1, d1_);
    retile_store(r0, ws, d0_);
    retile_store(r1, ws, d1_);
    STAMP(0, 15);
    return;
  }
  b -= PB_RT;
  {
    const int cg = tid % 14, rg = tid / 14;
    const int f0 = b * 112 + cg * 8;
    float s[8] = {0.f, 0.f, 0.f, 0.f, 0.f, 0.f, 0.f, 0.f};
    if (tid < 224) {
#pragma unroll
      for (int h = 0; h < 2; ++h) {
        u32x4 v[16];
#pragma unroll
        for (int j = 0; j < 16; ++j) v[j] = *(const u32x4*)(w1 + (size_t)(rg * 32 + h * 16 + j) * F_ + f0);
#pragma unroll
        for (int j = 0; j < 16; ++j)
#pragma unroll
          for (int c = 0; c < 4; ++c) { s[2 * c] += bf_lo(v[j][c]); s[2 * c + 1] += bf_hi(v[j][c]); }
      }
#pragma unroll
      for (int e = 0; e < 8; ++e) red[rg * 112 + cg * 8 + e] = s[e];
    }
    __syncthreads();
    if (tid < 112) {
      float t = 0.f;
#pragma unroll
      for (int r = 0; r < 16; ++r) t += red[r * 112 + tid];
      ((float*)(ws + WS_W1S))[b * 112 + tid] = t;
    }
    STAMP(0, 15);
  }
}

constexpr int CV_CS = 260 * 16;
constexpr int CV_KCH = 8 * CV_CS;
constexpr int CV_U0 = 2 * CV_KCH;
constexpr int CV_UCH = (256 + 2048) * 2;
constexpr int CV_XS = CV_U0 + 2 * CV_UCH;
constexpr int CV_RED = CV_XS + 2 * 8192;
constexpr int G1_R = 7;
constexpr int G2_R = 9;
constexpr int LDS_CNT = 128 * 1024 + G1_R * 4096;
constexpr int LAYER_LDS = LDS_CNT + 1024;
static_assert(CV_RED + 16384 <= 9 * 16384, "conv LDS");
#define G1_W0 8
#define G2_W0 6
#define CV_PF1 16
#define CV_PF2 22
#define W1PRE 5
#define POLL_SLEEP 2
#define G1_WW 5
#define G1_LA 5
#define G1_PUBA 24
#define G2_PUBB 0
#define G2_BAT 2
static_assert(G1_WW <= G1_R - 2, "ring safety");
__host__ __device__ constexpr int g2_iss(int i) {
  int v = G2_W0 + i;
  if (i <= G2_BAT) { if (v > 7) v = 7; }
  else { const int c = 7 + 3 * (i - G2_BAT); if (v > c) v = c; }
  return v < 14 ? v : 14;
}
__host__ __device__ constexpr int g1_young(int c) {
  int pos = 0, need = 0;
  for (int j = 0; j < G1_WW; ++j) { ++pos; if (j == c) need = pos; }
  for (int j = 0; j < G1_W0; ++j) { pos += 2; if (j == c) need = pos; }
  for (int i = 0; i < c; ++i) {
    if (i + G1_W0 < 16) { pos += 2; if (i + G1_W0 == c) need = pos; }
    if (i + G1_WW < 32) { ++pos; if (i + G1_WW == c) need = pos; }
  }
  return pos - need;
}
template <int N> DI void wait_lgkm2(u32x2& a, u32x2& b) {
  asm volatile("s_waitcnt lgkmcnt(%2)" : "+v"(a), "+v"(b) : "n"(N) : "memory");
}
template <int N> DI void wait_lgkm3(u32x2& a, u32x2& b, u32x4& c) {
  asm volatile("s_waitcnt lgkmcnt(%3)" : "+v"(a), "+v"(b), "+v"(c) : "n"(N) : "memory");
}
template <int OFF> DI void lds_read16(u32x4& d, unsigned addr) {
  asm volatile("ds_read_b128 %0, %1 offset:%2" : "=v"(d) : "v"(addr), "n"(OFF));
}
DI void pin4(bf16x8* f) { asm volatile("" : "+v"(f[0]), "+v"(f[1]), "+v"(f[2]), "+v"(f[3])); }
DI void pin7(u32x4* f) { asm volatile("" : "+v"(f[0]), "+v"(f[1]), "+v"(f[2]), "+v"(f[3]), "+v"(f[4]), "+v"(f[5]), "+v"(f[6])); }
DI void st16_sc1(void* p, u32x4 v) { asm volatile("global_store_dwordx4 %0, %1, off sc1\n\ts_nop 1" ::"v"(p), "v"(v) : "memory"); }
DI void st8_sc1(void* p, u32x2 v) { asm volatile("global_store_dwordx2 %0, %1, off sc1\n\ts_nop 1" ::"v"(p), "v"(v) : "memory"); }
DI u32x4 ld16_sc1_wait(const void* p) {
  u32x4 v;
  asm volatile("global_load_dwordx4 %0, %1, off sc1\n\ts_waitcnt vmcnt(0)" : "=&v"(v) : "v"(p) : "memory");
  return v;
}

struct LayerArgs {
  const unsigned short* dskip;
  unsigned short* out;
  char* ws;
};

extern "C" __global__ __launch_bounds__(512, 2) void s4d_layer_kernel(LayerArgs p) {
  const unsigned cw = warm_code<LAYER_CODE>((const char*)__builtin_amdgcn_s_getpc(), threadIdx.x);
  extern __shared__ __attribute__((aligned(16))) char smem[];
  u32x4 wr[28];
#pragma unroll 1
  for (int LAYER = 0; LAYER < 2; ++LAYER) {
  int tid = threadIdx.x;
  asm volatile("" : "+v"(tid));
  const int lane = tid & 63;
  const int wave8 = __builtin_amdgcn_readfirstlane(tid >> 6);
  const int wave = wave8 & 3;
  const int bx = blockIdx.x & 7, bi = blockIdx.x >> 3;
  const int r32 = lane & 31, hh = lane >> 5;
  char* ws = p.ws;
  const char* w1t = ws + WS_W1T;
  const char* w2t = ws + WS_W2T;
  const int mt2 = bx * 4 + (bi & 3), nt2 = bi >> 2;
  const int ni = wave8 & 1, kq = wave8 >> 1;
  const int mi_f = kq >> 1, kh_f = kq & 1;
  const int tok = mt2 * 64 + mi_f * 32 + r32;
  const int dbase = nt2 * 64 + ni * 32 + 4 * hh + 16 * kh_f;
  unsigned short xr[8];
  const char* w2src = w2t + (size_t)(nt2 * 2 + ni) * 112 * 1024 + kq * 1024 + lane * 16;
  auto pf_w2 = [&](auto J) {
    constexpr int j = J;
    wr[j] = *(const u32x4*)(w2src + (j >> 1) * 8192 + (j & 1) * 4096);
  };
  const int mt = (bx >> 1) * 4 + (bi & 3), nt = (bx & 1) * 7 + (bi >> 2);
  const int ft = nt * 4 + wave;
  const int KID = 1 + 3 * LAYER;
  const unsigned short* xin = (const unsigned short*)(ws + (LAYER ? WS_XT2 : WS_XT0));
  unsigned short* xt = (unsigned short*)(ws + (LAYER ? WS_XT3 : WS_XT1));
  char* hbuf = ws + (LAYER ? WS_H2 : WS_H);
  unsigned* sw = (unsigned*)(ws + WS_SW) + LAYER * 2048;
  unsigned* flags = (unsigned*)(ws + WS_FL) + LAYER * 512 * FLS;
  unsigned* cflags = (unsigned*)(ws + WS_CF) + LAYER * (CREP * 16 * 32);
  unsigned* eflags = (unsigned*)(ws + WS_CF) + 2 * (CREP * 16 * 32);
  const unsigned short* xrsrc = xt + ((size_t)(tok >> 7) * D_ + dbase) * 128 + (tok & 127);
  auto pf_xr = [&](auto J) {
    constexpr int j = J;
    xr[j] = __hip_atomic_load(xrsrc + ((j & 3) + 8 * (j >> 2)) * 128, __ATOMIC_RELAXED, __HIP_MEMORY_SCOPE_AGENT);
  };
  STAMP(KID, 0);
  {
    const int d0 = 2 * (bx * 32 + bi);
    const int gz = tid & 255, gc = tid >> 8;
    const int t4 = tid * 4;
    const int ch = wave8 >> 2, half = wave8 & 1, qs = (wave8 >> 1) & 1;
    float xf[2][4], m[4], sd[4];
    const unsigned dsk2 = *(const unsigned*)(p.dskip + d0);
    const char* gp = ws + WS_G + (size_t)(d0 + gc) * 4096 + gz * 16;
    const u32x4 ga = *(const u32x4*)gp;
    const u32x4 gb = *(const u32x4*)(gp + 16);
    const float kscale = ((const float*)(ws + WS_KINV))[d0 + ch];
    u32x4 swv0 = {0u, 0u, 0u, 0u};
    u32x2 xv0 = {0u, 0u}, xv1 = {0u, 0u};
    if (LAYER == 0) {
      swv0 = *(const u32x4*)(ws + WS_SW0 + t4 * 4);
      xv0 = *(const u32x2*)(xin + (size_t)d0 * L_ + t4);
      xv1 = *(const u32x2*)(xin + (size_t)(d0 + 1) * L_ + t4);
    }
    unsigned zz = 0u;
    asm volatile("" : "+v"(zz));
    if (tid < 16) *(u32x4*)(smem + (tid >> 3) * CV_KCH + (tid & 7) * CV_CS + 256 * 16) = u32x4{zz, zz, zz, zz};
    if (tid >= 64 && tid < 128) {
      const int q = tid - 64;
      *(u32x4*)(smem + CV_U0 + (q >> 5) * CV_UCH + (q & 31) * 16) = u32x4{zz, zz, zz, zz};
    }
    if (tid == 128) *(unsigned*)(smem + LDS_CNT) = 0u;
    {
      const unsigned msk = (gz < 255) ? 0xffffffffu : 0u;
      unsigned w[8] = {ga[0], ga[1], ga[2], ga[3], gb[0] & msk, gb[1] & msk, gb[2] & msk, gb[3] & msk};
      unsigned o[7];
#pragma unroll
      for (int k = 0; k < 7; ++k) o[k] = __builtin_amdgcn_alignbit(w[k + 1], w[k], 16);
      char* dst = smem + gc * CV_KCH + gz * 16;
#pragma unroll
      for (int mm = 0; mm < 4; ++mm) {
        *(u32x4*)(dst + (2 * mm) * CV_CS) = u32x4{w[mm], w[mm + 1], w[mm + 2], w[mm + 3]};
        *(u32x4*)(dst + (2 * mm + 1) * CV_CS) = u32x4{o[mm], o[mm + 1], o[mm + 2], o[mm + 3]};
      }
    }
    f32x4 st0 = {0.f, 0.f, 0.f, 0.f}, st1 = {0.f, 0.f, 0.f, 0.f};
    if (LAYER != 0) {
      if (wave8 == 7) {
        const unsigned* cp = eflags + ((blockIdx.x % CREP) * 16 + (lane & 15)) * 32;
        for (int it = 0; it < (1 << 20); ++it) {
          const unsigned f = __hip_atomic_load(cp, __ATOMIC_RELAXED, __HIP_MEMORY_SCOPE_AGENT);
          if (__all(f >= 16u)) break;
          __builtin_amdgcn_s_sleep(POLL_SLEEP);
        }
      }
      __builtin_amdgcn_s_barrier();
      STAMP(KID, 12);
      const auto rs_st = __builtin_amdgcn_make_buffer_rsrc((void*)(ws + WS_ST1), 0, 16384, 0x00020000);
      const auto rs_x = __builtin_amdgcn_make_buffer_rsrc((void*)xin, 0, L_ * D_ * 2, 0x00020000);
      st0 = __builtin_bit_cast(f32x4, __builtin_amdgcn_raw_buffer_load_b128(rs_st, t4 * 8, 0, 16  ));
      st1 = __builtin_bit_cast(f32x4, __builtin_amdgcn_raw_buffer_load_b128(rs_st, t4 * 8 + 16, 0, 16));
      xv0 = __builtin_amdgcn_raw_buffer_load_b64(rs_x, (d0 * L_ + t4) * 2, 0, 16);
      xv1 = __builtin_amdgcn_raw_buffer_load_b64(rs_x, ((d0 + 1) * L_ + t4) * 2, 0, 16);
    }
#pragma unroll
    for (int r = 0; r < 4; ++r) {
      {
        const unsigned wv = swv0[r];
        const float a = (r < 2) ? st0[(r & 1) * 2] : st1[(r & 1) * 2];
        const float bq = (r < 2) ? st0[(r & 1) * 2 + 1] : st1[(r & 1) * 2 + 1];
        const float mean = a * (1.f / D_), var = fmaxf(bq * (1.f / D_) - mean * mean, 0.f);
        const float m1 = rbf(mean), sd1 = rbf(__builtin_amdgcn_sqrtf(rbf(rbf(var) + 1e-5f)));
        m[r] = LAYER ? m1 : bf_lo(wv);
        sd[r] = LAYER ? sd1 : bf_hi(wv);
      }
      const unsigned w0 = xv0[r >> 1], w1_ = xv1[r >> 1];
      xf[0][r] = (r & 1) ? bf_hi(w0) : bf_lo(w0);
      xf[1][r] = (r & 1) ? bf_hi(w1_) : bf_lo(w1_);
    }
    {
      float rs[4];
#pragma unroll
      for (int r = 0; r < 4; ++r) rs[r] = __builtin_amdgcn_rcpf(sd[r]);
#pragma unroll
      for (int c = 0; c < 2; ++c) {
        const float dsk = c ? bf_hi(dsk2) : bf_lo(dsk2);
        float uu[4];
        unsigned xs[4];
#pragma unroll
        for (int r = 0; r < 4; ++r) {
          const float x = xf[c][r];
          const float dd = rbf(x - m[r]);
          float q = dd * rs[r];
          q = fmaf(fmaf(-q, sd[r], dd), rs[r], q);
          uu[r] = rbf(q);
          xs[r] = pk_bf16(x, uu[r] * dsk);
        }
        *(u32x2*)(smem + CV_U0 + c * CV_UCH + (256 + t4) * 2) = u32x2{pk_f16(uu[0], uu[1]), pk_f16(uu[2], uu[3])};
        *(u32x4*)(smem + CV_XS + c * 8192 + t4 * 4) = u32x4{xs[0], xs[1], xs[2], xs[3]};
      }
    }
    asm volatile("" ::"v"(cw));
    __syncthreads();
    STAMP(KID, 1);
    const int j16 = lane & 15, g = lane >> 4;
    const int cc = (7 - j16) & 7;
    const int zl = (2031 - j16 - cc) / 8 + g;
    const char* abase = smem + ch * CV_KCH + cc * CV_CS + (zl - 32 * 7 - 4 * (4 * half + 3)) * 16;
    const char* bbase = smem + CV_U0 + ch * CV_UCH + 480 + 32 * j16 + 16 * g - 64 * (4 * half + 3);
    f32x4 acc[4];
#pragma unroll
    for (int q = 0; q < 4; ++q) acc[q] = f32x4{0.f, 0.f, 0.f, 0.f};
    auto body = [&](auto QS, auto PFF) {
      constexpr int S = QS;
      constexpr bool PF = PFF;
      constexpr int Q[4] = {S ? 6 : 7, S ? 5 : 4, S ? 2 : 3, S ? 1 : 0};
      constexpr int NF = S ? 7 : 8;
      f16x8 A[2][NF], B[2][NF];
      auto ld = [&](auto IT) {
        constexpr int it = IT;
#pragma unroll
        for (int k = 0; k < NF; ++k) {
          A[it & 1][k] = *(const f16x8*)(abase + (7 - k) * 512 + (3 - it) * 64);
          B[it & 1][NF - 1 - k] = *(const f16x8*)(bbase + (NF - 1 - k) * 512 + (3 - it) * 64);
        }
      };
      ld(std::integral_constant<int, 0>{});
      __builtin_amdgcn_sched_barrier(0);
      static_for<0, 4>([&](auto IT) {
        constexpr int it = IT;
        if constexpr (it + 1 < 4) ld(std::integral_constant<int, it + 1>{});
        if constexpr (PF) static_for<it * (CV_PF1 / 4), (it + 1) * (CV_PF1 / 4)>([&](auto J) { pf_w2(J); });
#pragma unroll
        for (int b1 = 0; b1 < NF; ++b1)
#pragma unroll
          for (int e = 0; e < 4; ++e)
            if (b1 <= Q[e]) acc[e] = __builtin_amdgcn_mfma_f32_16x16x32_f16(A[it & 1][b1], B[it & 1][Q[e] - b1], acc[e], 0, 0, 0);
#pragma unroll
        for (int k = 0; k < 18; ++k) {
          __builtin_amdgcn_sched_group_barrier(0x8, 1, 0);
          if (it + 1 < 4 && k >= 2 && k - 2 < 2 * NF) __builtin_amdgcn_sched_group_barrier(0x100, 1, 0);
          if (PF && CV_PF1 > 0 && (k % (16 / (CV_PF1 / 4))) == 1 && k / (16 / (CV_PF1 / 4)) < CV_PF1 / 4)
            __builtin_amdgcn_sched_group_barrier(0x20, 1, 0);
        }
        __builtin_amdgcn_sched_barrier(0);
      });
    };
    if (LAYER == 0) {
      if (qs) body(std::integral_constant<int, 1>{}, std::true_type{}); else body(std::integral_constant<int, 0>{}, std::true_type{});
    } else {
      if (qs) body(std::integral_constant<int, 1>{}, std::false_type{}); else body(std::integral_constant<int, 0>{}, std::false_type{});
    }
    f32x4 keep[2];
    {
      char* wrp = smem + CV_RED + wave8 * 2048 + lane * 16;
#pragma unroll
      for (int e = 0; e < 2; ++e) {
        f32x4 give;
#pragma unroll
        for (int r = 0; r < 4; ++r) {
          const float lo = acc[e][r], hi = acc[2 + e][r];
          give[r] = half ? lo : hi;
          keep[e][r] = half ? hi : lo;
        }
        *(f32x4*)(wrp + e * 1024) = give;
      }
    }
    asm volatile("s_waitcnt lgkmcnt(0)" ::: "memory");
    __builtin_amdgcn_s_barrier();
    STAMP(KID, 2);
    {
      const char* rd = smem + CV_RED + (wave8 ^ 1) * 2048 + lane * 16;
      const int toff = 16 * j16 + 4 * g;
#pragma unroll
      for (int e = 0; e < 2; ++e) {
        const int q = half ? (e ? (qs ? 1 : 0) : (qs ? 2 : 3)) : (e ? (qs ? 5 : 4) : (qs ? 6 : 7));
        const int t = 256 * q + toff;
        const f32x4 o = *(const f32x4*)(rd + e * 1024);
        const u32x4 xs = *(const u32x4*)(smem + CV_XS + ch * 8192 + t * 4);
        const f32x4 y = keep[e] + o;
        float xo[4];
#pragma unroll
        for (int r = 0; r < 4; ++r) {
          const unsigned w = xs[r];
          xo[r] = bf_lo(w) + rbf(rbf(y[r] * kscale) + bf_hi(w));
        }
        st8_sc1(xt + ((size_t)(t >> 7) * D_ + d0 + ch) * 128 + (t & 127), u32x2{pk_bf16(xo[0], xo[1]), pk_bf16(xo[2], xo[3])});
      }
    }
    wait_vm_lgkm0<0>();
    __builtin_amdgcn_s_barrier();
    if (tid < CREP) (void)__hip_atomic_fetch_add(cflags + (tid * 16 + (d0 >> 5)) * 32, 1u, __ATOMIC_RELAXED, __HIP_MEMORY_SCOPE_AGENT);
    STAMP(KID, 3);
  }
  const int grow = lane >> 4, gslot = lane & 15;
  const unsigned short* xsrc = xt + ((size_t)mt * D_ + wave * 8 + grow) * 128 + ((gslot ^ (grow << 2)) * 8);
  const char* wsrc = w1t + (size_t)(nt * 4 + (wave & 1)) * 32 * 1024 + (wave >> 1) * 1024 + lane * 16;
  auto issue_w = [&](int c) {
    __builtin_amdgcn_global_load_lds((const unsigned*)(wsrc + (size_t)(c >> 4) * 64 * 1024 + (size_t)(c & 15) * 2048),
                                     (LAS unsigned*)(smem + 131072 + (c % G1_R) * 4096 + wave * 1024), 16, 0, 0);
  };
  auto issue_x = [&](int c) {
#pragma unroll
    for (int i = 0; i < 2; ++i)
      __builtin_amdgcn_global_load_lds((const unsigned*)(xsrc + (size_t)(c * 32 + i * 4) * 128),
                                       (LAS unsigned*)(smem + c * 8192 + (wave * 8 + i * 4) * 256), 16, 0, 16  );
  };
  auto publish = [&](int pass) {
    wait_vm<0>();
    if (lane == 0) {
      const unsigned old = __hip_atomic_fetch_add((unsigned*)(smem + LDS_CNT), 1u, __ATOMIC_RELAXED, __HIP_MEMORY_SCOPE_WORKGROUP);
      if ((old & 3u) == 3u && (int)(old >> 2) == pass)
        __hip_atomic_store(flags + (pass * 256 + mt * 16 + nt) * FLS, 1u, __ATOMIC_RELAXED, __HIP_MEMORY_SCOPE_AGENT);
    }
  };
  auto wait_h = [&](int pass) {
    const unsigned* fp = flags + (pass * 256 + (mt2 >> 1) * 16 + (lane < 14 ? lane : 0)) * FLS;
    for (int it = 0; it < (1 << 20); ++it) {
      unsigned f;
      asm volatile("global_load_dword %0, %1, off sc1\n\ts_waitcnt vmcnt(0)" : "=&v"(f) : "v"(fp) : "memory");
      if (__all(f != 0u)) break;
      __builtin_amdgcn_s_sleep(POLL_SLEEP);
    }
  };
  if (bi < 28 && wave8 >= 4) {
    if (wave8 < 6)
      __builtin_amdgcn_global_load_lds((const unsigned*)(ws + WS_W1S) + nt * 128 + wave * 64 + lane,
                                       (LAS unsigned*)(smem + LDS_CNT + 256 + wave * 256), 4, 0, 0);
#pragma unroll
    for (int c = 0; c < G1_WW; ++c) issue_w(c);
  }
  __builtin_amdgcn_sched_barrier(0);
  if (LAYER == 0) static_for<CV_PF1, 28>([&](auto J) { pf_w2(J); });
  __builtin_amdgcn_sched_barrier(0);
  if (wave8 == 7) {
    const unsigned* cp = cflags + ((blockIdx.x % CREP) * 16 + (lane & 15)) * 32;
    for (int it = 0; it < (1 << 20); ++it) {
      const unsigned f = __hip_atomic_load(cp, __ATOMIC_RELAXED, __HIP_MEMORY_SCOPE_AGENT);
      if (__all(f >= 16u)) break;
      __builtin_amdgcn_s_sleep(POLL_SLEEP);
    }
  }
  pin7(wr); pin7(wr + 7); pin7(wr + 14); pin7(wr + 21);
  __builtin_amdgcn_s_barrier();
  STAMP(KID, 4);
  if (bi >= 28) {
    const int ht = bx * 4 + (bi - 28);
    const int cgp = wave8 * 2 + hh;
    const auto rsrc = __builtin_amdgcn_make_buffer_rsrc((void*)xt, 0, L_ * D_ * 2, 0x00020000);
    const int off0 = ((ht >> 1) * D_ * 128 + (ht & 1) * 64 + 2 * r32) * 2;
    const unsigned pv = __builtin_amdgcn_raw_buffer_load_b32(rsrc, off0, 0, 16  );
    unsigned v[32];
#pragma unroll
    for (int j = 0; j < 32; ++j) v[j] = __builtin_amdgcn_raw_buffer_load_b32(rsrc, off0 + (cgp * 32 + j) * 256, 0, 16  );
    const f32x2 piv = {bf_lo(pv), bf_hi(pv)};
    f32x2 s1 = {0.f, 0.f}, s2 = {0.f, 0.f};
#pragma unroll
    for (int j = 0; j < 32; ++j) {
      const f32x2 dd = f32x2{bf_lo(v[j]), bf_hi(v[j])} - piv;
      s1 += dd;
      s2 = __builtin_elementwise_fma(dd, dd, s2);
    }
    f32x4* red = (f32x4*)smem;
    red[cgp * 32 + r32] = f32x4{s1[0], s2[0], s1[1], s2[1]};
    asm volatile("s_waitcnt lgkmcnt(0)" ::: "memory");
    __builtin_amdgcn_s_barrier();
    if (tid < 64) {
      const f32x2* r2 = (const f32x2*)smem;
      float a = 0.f, q = 0.f;
#pragma unroll
      for (int w = 0; w < 16; ++w) { const f32x2 t = r2[w * 64 + tid]; a += t[0]; q += t[1]; }
      const unsigned pw = __shfl(pv, tid >> 1, 64);
      const float dm = a * (1.f / D_);
      const float var = fmaxf(q * (1.f / D_) - dm * dm, 0.f);
      const float mm = ((tid & 1) ? bf_hi(pw) : bf_lo(pw)) + dm;
      const float sdv = __builtin_amdgcn_sqrtf(rbf(rbf(var) + 1e-5f));
      __hip_atomic_store(sw + ht * 64 + tid, pk_bf16(mm, sdv), __ATOMIC_RELAXED, __HIP_MEMORY_SCOPE_AGENT);
    }
    STAMP(KID, 12);
  } else if (wave8 >= 4) {
#pragma unroll
    for (int c = 0; c < G1_W0; ++c) issue_x(c);
    static_for<0, 32>([&](auto C) {
      constexpr int c = C;
      wait_vm<g1_young(c)>();
      __builtin_amdgcn_s_barrier();
      if constexpr (c + G1_W0 < 16) issue_x(c + G1_W0);
      if constexpr (c + G1_WW < 32) issue_w(c + G1_WW);
    });
    wait_h(0);
  } else {
    const int blk = (lane >> 4) & 1, q4 = (lane & 15) >> 2, p4 = lane & 3;
    const int fs = wave & 1, th = wave >> 1;
    const unsigned sbase = (unsigned)(uintptr_t)(LAS char*)smem;
    unsigned tb[2][2];
#pragma unroll
    for (int ct = 0; ct < 2; ++ct) {
      tb[0][ct] = sbase + (unsigned)((8 * hh + q4) * 256 + (((4 * (2 * th + ct) + 2 * blk + (p4 >> 1)) ^ (q4 << 2)) * 16) + 8 * (p4 & 1));
      tb[1][ct] = tb[0][ct] + 65536u;
    }
    const unsigned ta = sbase + 131072 + fs * 1024 + lane * 16;
    f32x16 acc[2];
    constexpr int LA = G1_LA;
    u32x2 bb[8][2];
    u32x4 af[4];
    unsigned swv[2];
    auto issue_pair = [&](auto NN) {
      constexpr int n = NN, ks = (n >> 1) & 31, ct = n & 1, c = n >> 2;
      constexpr int xo = ks * 4096;
      if constexpr ((n & 3) == 0) __builtin_amdgcn_s_barrier();
      if constexpr (n == 0) STAMP(KID, 5);
      if constexpr (ct == 0) lds_read16<(c % G1_R) * 4096 + (ks & 1) * 2048>(af[ks & 3], ta);
      tr_read<(xo & 65535)>(bb[n & 7][0], tb[xo >> 16][ct]);
      tr_read<(xo & 65535) + 1024>(bb[n & 7][1], tb[xo >> 16][ct]);
    };
    static_for<0, LA>([&](auto NN) { issue_pair(NN); });
    static_for<0, 128>([&](auto NN) {
      constexpr int n = NN, ks = (n >> 1) & 31, ct = n & 1, pass = n >> 6;
      constexpr int last = (n + LA - 1 < 127) ? (n + LA - 1) : 127;
      constexpr int younger = 2 * (last - n) + ((last >> 1) - (n >> 1));
      if constexpr (ct == 0) wait_lgkm3<younger>(bb[n & 7][0], bb[n & 7][1], af[ks & 3]);
      else wait_lgkm2<younger>(bb[n & 7][0], bb[n & 7][1]);
      const u32x4 bu = {bb[n & 7][0][0], bb[n & 7][0][1], bb[n & 7][1][0], bb[n & 7][1][1]};
      if constexpr ((n & 63) < 2) {
#pragma unroll
        for (int i = 0; i < 16; ++i) acc[ct][i] = 0.f;
      }
      acc[ct] = __builtin_amdgcn_mfma_f32_32x32x16_bf16(__builtin_bit_cast(bf16x8, af[ks & 3]), __builtin_bit_cast(bf16x8, bu), acc[ct], 0, 0, 0);
      __builtin_amdgcn_sched_barrier(0);
      if constexpr (n + LA < 128) issue_pair(std::integral_constant<int, n + LA>{});
      if constexpr (n >= 56 && n < 58)
        swv[n - 56] = __hip_atomic_load(sw + mt * 128 + (2 * th + n - 56) * 32 + r32, __ATOMIC_RELAXED, __HIP_MEMORY_SCOPE_AGENT);
      if constexpr (n == 64 + G1_PUBA) publish(0);
      __builtin_amdgcn_sched_barrier(0);
      if constexpr ((n & 63) == 63) {
        if constexpr (pass == 1) STAMP(KID, 6); else STAMP(KID, 13);
        if constexpr (pass == 0) {
          for (int it = 0; it < (1 << 20); ++it) {
            if (__all(swv[0] != 0u && swv[1] != 0u)) break;
            __builtin_amdgcn_s_sleep(1);
#pragma unroll
            for (int c2_ = 0; c2_ < 2; ++c2_)
              swv[c2_] = __hip_atomic_load(sw + mt * 128 + (2 * th + c2_) * 32 + r32, __ATOMIC_RELAXED, __HIP_MEMORY_SCOPE_AGENT);
          }
          asm volatile("" : "+v"(swv[0]), "+v"(swv[1]));
        }
        f32x4 ws4[4];
#pragma unroll
        for (int q = 0; q < 4; ++q) ws4[q] = *(const f32x4*)(smem + LDS_CNT + 256 + ((2 * pass + fs) * 32 + 8 * q + 4 * hh) * 4);
#pragma unroll
        for (int c2_ = 0; c2_ < 2; ++c2_) {
          const float rs = __builtin_amdgcn_rcpf(bf_hi(swv[c2_]));
          const float cm = -bf_lo(swv[c2_]) * rs;
          char* dst = hbuf + ((size_t)((mt * 4 + 2 * th + c2_) * 112 + (nt * 4 + 2 * pass + fs) * 2) * 64 + lane) * 16;
          const f32x2 c2 = {cm, cm}, r2 = {rs, rs};
#pragma unroll
          for (int s = 0; s < 2; ++s) {
            u32x4 o;
#pragma unroll
            for (int j = 0; j < 4; ++j) {
              const int reg = 8 * s + 2 * j;
              const f32x2 wsum = {ws4[reg >> 2][reg & 3], ws4[reg >> 2][(reg & 3) + 1]};
              const f32x2 a2 = {acc[c2_][reg], acc[c2_][reg + 1]};
              const f32x2 v2 = __builtin_elementwise_fma(a2, r2, c2 * wsum);
              const s16x2 pv = __builtin_bit_cast(s16x2, __builtin_convertvector(v2, bf16x2));
              const s16x2 z = {0, 0};
              o[j] = __builtin_bit_cast(unsigned, __builtin_elementwise_max(pv, z));
            }
            st16_sc1(dst + s * 1024, o);
          }
        }
        if constexpr (pass == 0) STAMP(KID, 14);
        __builtin_amdgcn_sched_barrier(0);
      }
    });
    STAMP(KID, 7);
  }
  if (bi >= 28 && wave8 >= 4) wait_h(0);
  asm volatile("s_waitcnt lgkmcnt(0)" ::: "memory");
  __builtin_amdgcn_s_barrier();
  STAMP(KID, 9);
  const char* hsrc = hbuf + (size_t)(mt2 * 2 + (wave & 1)) * 112 * 1024 + (wave >> 1) * 8192 + lane * 16;
  auto issue2 = [&](int sc) {
#pragma unroll
    for (int i = 0; i < 4; ++i)
      __builtin_amdgcn_global_load_lds((const unsigned*)(hsrc + (size_t)(sc % 7) * 16384 + (sc / 7) * 4096 + i * 1024),
                                       (LAS unsigned*)(smem + (sc % G2_R) * 16384 + (wave >> 1) * 8192 + i * 2048 + (wave & 1) * 1024), 16, 0, 16  );
  };
  if (wave8 >= 4) {
#pragma unroll
    for (int c = 0; c < g2_iss(0); ++c) issue2(c);
  } else if (bi < 28) {
    publish(1);
  }
  STAMP(KID, 8);
  f32x16 acc[2];
#pragma unroll
  for (int i = 0; i < 16; ++i) { acc[0][i] = 0.f; acc[1][i] = 0.f; }
  const char* rdh = smem + kq * 2048 + lane * 16;
  bf16x8 hf[2][4];
  static_for<0, 15>([&](auto C) {
    constexpr int c = C;
    auto mma = [&](auto JJ) {
      constexpr int j = JJ;
      if constexpr (c >= 1) {
        constexpr int pc = c - 1;
#pragma unroll
        for (int mi = 0; mi < 2; ++mi)
          acc[mi] = __builtin_amdgcn_mfma_f32_32x32x16_bf16(__builtin_bit_cast(bf16x8, wr[2 * (2 * (pc % 7) + j) + pc / 7]), hf[pc & 1][j * 2 + mi], acc[mi], 0, 0, 0);
      }
    };
    mma(std::integral_constant<int, 0>{});
    __builtin_amdgcn_sched_barrier(0);
    if constexpr (c < 14) {
      wait_vm_lgkm0<4 * (g2_iss(c) - (c + 1)) + (c > 9 ? 8 : 0)>();
      __builtin_amdgcn_s_barrier();
      if constexpr (c == 0) STAMP(KID, 10);
#pragma unroll
      for (int e = 0; e < 4; ++e) hf[c & 1][e] = *(const bf16x8*)(rdh + (c % G2_R) * 16384 + (e >> 1) * 8192 + (e & 1) * 1024);
    }
    __builtin_amdgcn_sched_barrier(0);
    mma(std::integral_constant<int, 1>{});
    __builtin_amdgcn_sched_barrier(0);
    if constexpr (c == 9) static_for<0, 8>([&](auto J) { pf_xr(J); });
    if constexpr (c < 14 && g2_iss(c + 1) > g2_iss(c)) {
      if (wave8 >= 4) {
        if constexpr (g2_iss(c) <= 7 && g2_iss(c + 1) > 7) wait_h(1);
#pragma unroll
        for (int cn = g2_iss(c); cn < g2_iss(c + 1); ++cn) issue2(cn);
      }
    }
    __builtin_amdgcn_sched_barrier(0);
  });
  STAMP(KID, 11);
  char* red = smem + 5 * 16384;
  {
#pragma unroll
    for (int pc = 0; pc < 4; ++pc) {
      if (pc != kq) {
        const int slot = (kq > pc) ? kq - 1 : kq;
        char* dst = red + ((pc * 2 + ni) * 3 + slot) * 2048 + lane * 16;
        f32x4 g0, g1;
#pragma unroll
        for (int i = 0; i < 4; ++i) { g0[i] = acc[pc >> 1][8 * (pc & 1) + i]; g1[i] = acc[pc >> 1][8 * (pc & 1) + 4 + i]; }
        *(f32x4*)dst = g0;
        *(f32x4*)(dst + 1024) = g1;
      }
    }
  }
  float mine[8];
#pragma unroll
  for (int i = 0; i < 8; ++i) {
    const float a0 = kh_f ? acc[0][8 + i] : acc[0][i];
    const float a1 = kh_f ? acc[1][8 + i] : acc[1][i];
    mine[i] = mi_f ? a1 : a0;
  }
  __syncthreads();
  {
    const char* src = red + (wave8 * 3) * 2048 + lane * 16;
#pragma unroll
    for (int sl = 0; sl < 3; ++sl) {
      const f32x4 o0 = *(const f32x4*)(src + sl * 2048);
      const f32x4 o1 = *(const f32x4*)(src + sl * 2048 + 1024);
#pragma unroll
      for (int i = 0; i < 4; ++i) { mine[i] += o0[i]; mine[4 + i] += o1[i]; }
    }
  }
  float xo[8];
  float s1 = 0.f, s2 = 0.f;
#pragma unroll
  for (int i = 0; i < 8; ++i) {
    const float v = rbf(__uint_as_float(((unsigned)xr[i]) << 16) + rbf(mine[i]));
    xo[i] = v;
    s1 += v; s2 += v * v;
  }
  if (LAYER == 1) {
#pragma unroll
    for (int q = 0; q < 2; ++q)
      *(u32x2*)(p.out + (size_t)tok * D_ + dbase + 8 * q) =
          u32x2{pk_bf16(xo[4 * q], xo[4 * q + 1]), pk_bf16(xo[4 * q + 2], xo[4 * q + 3])};
  } else {
    unsigned short* xout = (unsigned short*)(ws + WS_XT2);
#pragma unroll
    for (int i = 0; i < 8; ++i)
      __hip_atomic_store(xout + (size_t)(dbase + (i & 3) + 8 * (i >> 2)) * L_ + tok,
                         (unsigned short)(__float_as_uint(xo[i]) >> 16), __ATOMIC_RELAXED, __HIP_MEMORY_SCOPE_AGENT);
    s1 += __shfl_xor(s1, 32, 64);
    s2 += __shfl_xor(s2, 32, 64);
    float* sred = (float*)(smem + 8 * 16384);
    sred[wave8 * 64 + lane] = hh ? s2 : s1;
    asm volatile("s_waitcnt lgkmcnt(0)" ::: "memory");
    __builtin_amdgcn_s_barrier();
    if (wave8 < 2) {
      const int w0 = wave8 * 4;
      const float t = sred[w0 * 64 + lane] + sred[(w0 + 1) * 64 + lane] + sred[(w0 + 2) * 64 + lane] + sred[(w0 + 3) * 64 + lane];
      (void)__hip_atomic_fetch_add((float*)(ws + WS_ST1) + 2 * (mt2 * 64 + wave8 * 32 + r32) + hh, t, __ATOMIC_RELAXED, __HIP_MEMORY_SCOPE_AGENT);
    }
    wait_vm_lgkm0<0>();
    __builtin_amdgcn_s_barrier();
    if (tid < CREP) (void)__hip_atomic_fetch_add(eflags + (tid * 16 + (blockIdx.x & 15)) * 32, 1u, __ATOMIC_RELAXED, __HIP_MEMORY_SCOPE_AGENT);
  }
  STAMP(KID, 15);
  }
}

extern "C" void kernel_launch(void* const* d_in, const int* in_sizes, int n_in,
                              void* d_out, int out_size, void* d_ws, size_t ws_size,
                              hipStream_t stream) {
  const unsigned short* x0 = (const unsigned short*)d_in[0];
  const unsigned short* logA = (const unsigned short*)d_in[1];
  const unsigned short* Aim = (const unsigned short*)d_in[2];
  const unsigned short* Cre = (const unsigned short*)d_in[3];
  const unsigned short* Cim = (const unsigned short*)d_in[4];
  const unsigned short* logdt = (const unsigned short*)d_in[5];
  const unsigned short* dskip = (const unsigned short*)d_in[6];
  const unsigned short* w1 = (const unsigned short*)d_in[8];
  const unsigned short* w2 = (const unsigned short*)d_in[9];
  char* ws = (char*)d_ws;
  (void)hipFuncSetAttribute((const void*)s4d_layer_kernel, hipFuncAttributeMaxDynamicSharedMemorySize, LAYER_LDS);
  prep_kernel<<<PREP_BLOCKS, 256, 0, stream>>>(x0, logA, Aim, Cre, Cim, logdt, w1, w2, ws);
  LayerArgs a0 = {dskip, (unsigned short*)d_out, ws};
  s4d_layer_kernel<<<256, 512, LAYER_LDS, stream>>>(a0);
}
```

```cpp
#include <hip/hip_runtime.h>
#include <hip/hip_bf16.h>
#include <stdint.h>

typedef __attribute__((ext_vector_type(8))) __bf16 bf16x8;
typedef __attribute__((ext_vector_type(2))) __bf16 bf16x2;
typedef __attribute__((ext_vector_type(4))) short s16x4;
typedef __attribute__((ext_vector_type(2))) short s16x2;
typedef __attribute__((ext_vector_type(8))) short s16x8;
typedef __attribute__((ext_vector_type(8))) _Float16 f16x8;
typedef __attribute__((ext_vector_type(2))) _Float16 f16x2;
typedef __attribute__((ext_vector_type(2))) float f32x2;
typedef __attribute__((ext_vector_type(4))) float f32x4;
typedef __attribute__((ext_vector_type(16))) float f32x16;
typedef __attribute__((ext_vector_type(4))) unsigned u32x4;
typedef __attribute__((ext_vector_type(2))) unsigned u32x2;
#define LAS __attribute__((address_space(3)))
#define DI __device__ __forceinline__

constexpr int L_ = 2048, D_ = 512, N_ = 128, F_ = 1792;

constexpr size_t MiB = 1024 * 1024;
constexpr size_t WS_G    = 0;
constexpr size_t WS_XT0  = 2 * MiB;
constexpr size_t WS_XT1  = 4 * MiB;
constexpr size_t WS_XT2  = 6 * MiB;
constexpr size_t WS_XT3  = 8 * MiB;
constexpr size_t WS_W1T  = 10 * MiB;
constexpr size_t WS_W2T  = 12 * MiB;
constexpr size_t WS_H    = 14 * MiB;
constexpr size_t WS_H2   = 24 * MiB;
constexpr size_t WS_KINV = 22 * MiB;
constexpr size_t WS_W1S  = WS_KINV + 4096;
constexpr size_t WS_Z0   = 23 * MiB;
constexpr size_t WS_ST1  = WS_Z0;
constexpr size_t WS_SW   = WS_ST1 + 16384;
#define FLS 32
#define CREP 8
#define CFS (3 * CREP)
constexpr size_t WS_FL   = WS_SW + 16384;
constexpr size_t WS_CF   = WS_FL + 2048 * FLS;
constexpr size_t WS_ZBYTES = 16384 + 16384 + 2048 * FLS + 2048 * CFS;
constexpr size_t WS_SW0  = WS_Z0 + 512 * 1024;

#define STAMP(kid, i)
#define STAMPT(kid, i, T)
DI float bf_lo(unsigned u) { return __uint_as_float(u << 16); }
DI float bf_hi(unsigned u) { return __uint_as_float(u & 0xffff0000u); }
DI unsigned pk_bf16(float a, float b) {
  f32x2 v = {a, b};
  return __builtin_bit_cast(unsigned, __builtin_convertvector(v, bf16x2));
}
DI unsigned pk_f16(float a, float b) {
  f32x2 v = {a, b};
  return __builtin_bit_cast(unsigned, __builtin_convertvector(v, f16x2));
}
DI float rbf(float a) { return bf_lo(pk_bf16(a, 0.f)); }
DI float ld_bf(const unsigned short* p) { return __uint_as_float(((unsigned)*p) << 16); }
DI unsigned perm_lo(unsigned hi, unsigned lo) { return __builtin_amdgcn_perm(hi, lo, 0x05040100u); }
DI unsigned perm_hi(unsigned hi, unsigned lo) { return __builtin_amdgcn_perm(hi, lo, 0x07060302u); }
template <int CTRL> DI float dpp_add(float v) {
  return v + __uint_as_float(__builtin_amdgcn_update_dpp(0u, __float_as_uint(v), CTRL, 0xf, 0xf, true));
}
DI float wave_sum(float v) {
  v = dpp_add<0xB1>(v);
  v = dpp_add<0x4E>(v);
  v = dpp_add<0x141>(v);
  v = dpp_add<0x140>(v);
  const unsigned u = __float_as_uint(v);
  return (__uint_as_float(__builtin_amdgcn_readlane(u, 0)) + __uint_as_float(__builtin_amdgcn_readlane(u, 16))) +
         (__uint_as_float(__builtin_amdgcn_readlane(u, 32)) + __uint_as_float(__builtin_amdgcn_readlane(u, 48)));
}
#define ST_FLAGS ""
DI void st16(void* p, u32x4 v) { asm volatile("global_store_dwordx4 %0, %1, off " ST_FLAGS "\n\ts_nop 1" ::"v"(p), "v"(v) : "memory"); }
DI void st8(void* p, u32x2 v) { asm volatile("global_store_dwordx2 %0, %1, off " ST_FLAGS "\n\ts_nop 1" ::"v"(p), "v"(v) : "memory"); }
DI void st2(void* p, unsigned v) { asm volatile("global_store_short %0, %1, off " ST_FLAGS "\n\ts_nop 1" ::"v"(p), "v"(v) : "memory"); }
template <int N> DI void wait_vm() { asm volatile("s_waitcnt vmcnt(%0)" ::"n"(N) : "memory"); }
template <int OFF> DI void gload16(u32x4& dst, const char* p) {
  asm volatile("global_load_dwordx4 %0, %1, off offset:%2" : "=v"(dst) : "v"(p), "n"(OFF));
}
template <int N> DI void wait_vm4(u32x4& a, u32x4& b, u32x4& c, u32x4& d) {
  asm volatile("s_waitcnt vmcnt(%4)" : "+v"(a), "+v"(b), "+v"(c), "+v"(d) : "n"(N) : "memory");
}
template <int OFF> DI void tr_read(u32x2& d, unsigned addr) {
  asm volatile("ds_read_b64_tr_b16 %0, %1 offset:%2" : "=v"(d) : "v"(addr), "n"(OFF));
}
template <int N> DI void wait_lgkm8(u32x2* b) {
  asm volatile("s_waitcnt lgkmcnt(%8)"
               : "+v"(b[0]), "+v"(b[1]), "+v"(b[2]), "+v"(b[3]), "+v"(b[4]), "+v"(b[5]), "+v"(b[6]), "+v"(b[7])
               : "n"(N) : "memory");
}
template <int N> DI void wait_vm_lgkm0() { asm volatile("s_waitcnt vmcnt(%0) lgkmcnt(0)" ::"n"(N) : "memory"); }
template <int I, int E, class F> DI void static_for(F&& f) {
  if constexpr (I < E) { f(std::integral_constant<int, I>{}); static_for<I + 1, E>(f); }
}

constexpr int TROW = 528;

__host__ __device__ constexpr int tab_w(int idx) {
  return idx < 16 ? idx : (idx < 40 ? ((8 + ((idx - 16) & 7)) << (1 + ((idx - 16) >> 3))) : 128);
}
__host__ __device__ constexpr int ilog2c(int w) { return w <= 1 ? 0 : 1 + ilog2c(w >> 1); }
__host__ __device__ constexpr int tab_idx(int w) {
  return w < 16 ? w : (w == 128 ? 40 : 16 + 8 * (ilog2c(w) - 4) + ((w >> (ilog2c(w) - 3)) - 8));
}
DI int tab_idx_rt(int w) {
  if (w < 16) return w;
  int lg = 31 - __clz(w);
  return 16 + 8 * (lg - 4) + ((w >> (lg - 3)) - 8);
}

DI void retile_load(int u, const unsigned short* __restrict__ w1, const unsigned short* __restrict__ w2, u32x2* r, size_t& dst) {
  if (u < 28672) {
    const int fg = u % 448, o = u / 448;
    const int f = 4 * fg;
#pragma unroll
    for (int j = 0; j < 8; ++j) r[j] = *(const u32x2*)(w1 + (size_t)(8 * o + j) * F_ + f);
    dst = WS_W1T + ((size_t)(((f >> 5) * 32 + (o >> 1)) * 64 + (o & 1) * 32 + (f & 31))) * 16;
  } else {
    const int v = u - 28672;
    const int dg = v & 127, o = v >> 7;
    const int dd = 4 * dg, ks = o >> 1, hh = o & 1;
#pragma unroll
    for (int j = 0; j < 8; ++j)
      r[j] = *(const u32x2*)(w2 + (size_t)(16 * ks + 8 * (j >> 2) + 4 * hh + (j & 3)) * D_ + dd);
    dst = WS_W2T + ((size_t)(((dd >> 5) * 112 + ks) * 64 + hh * 32 + (dd & 31))) * 16;
  }
}
DI void retile_store(const u32x2* r, char* ws, size_t dst) {
#pragma unroll
  for (int c = 0; c < 2; ++c) {
    u32x4 lo = {perm_lo(r[1][c], r[0][c]), perm_lo(r[3][c], r[2][c]), perm_lo(r[5][c], r[4][c]), perm_lo(r[7][c], r[6][c])};
    u32x4 hi = {perm_hi(r[1][c], r[0][c]), perm_hi(r[3][c], r[2][c]), perm_hi(r[5][c], r[4][c]), perm_hi(r[7][c], r[6][c])};
    st16(ws + dst + (2 * c) * 16, lo);
    st16(ws + dst + (2 * c + 1) * 16, hi);
  }
}

#define LAYER_CODE 25600
#define PREP_CODE 14336
template <int BYTES> DI unsigned warm_code(const char* pc, int t) {
  const int o = t * 128;
  return *(const __attribute__((address_space(1))) unsigned*)(uintptr_t)(pc + (o < BYTES ? o : 0));
}
constexpr int PB_KGEN = 512, PB_XT = 64, PB_ST = 64, PB_RT = 112, PB_WS = 16;
constexpr int PREP_BLOCKS = PB_KGEN + PB_XT + PB_ST + PB_RT + PB_WS;

__global__ __launch_bounds__(256) void prep_kernel(
    const unsigned short* __restrict__ x0, const unsigned short* __restrict__ logA,
    const unsigned short* __restrict__ Aim, const unsigned short* __restrict__ Cre,
    const unsigned short* __restrict__ Cim, const unsigned short* __restrict__ logdt,
    const unsigned short* __restrict__ w1, const unsigned short* __restrict__ w2, char* __restrict__ ws,
    const char* __restrict__ outp) {
  __shared__ __attribute__((aligned(16))) char smem[82 * TROW + 1536];
  float* red = (float*)smem;
  const int tid = threadIdx.x, lane = tid & 63;
  const int wave = __builtin_amdgcn_readfirstlane(tid >> 6);
  int b = blockIdx.x;
  STAMP(0, 0);
  unsigned cw1 = 0u;
  if (b >= PB_KGEN && b < PB_KGEN + PB_XT) cw1 = warm_code<PREP_CODE>((const char*)__builtin_amdgcn_s_getpc(), tid);
  if (b < PB_KGEN) {
    const int d = ((b & 7) * 32 + ((b >> 3) & 31)) * 2 + (b >> 8);
    const int isH = wave >> 1;
    const int n = (wave & 1) * 64 + lane;
    const float pdt = ld_bf(logdt + d), pla = ld_bf(logA + d * N_ + n), pai = ld_bf(Aim + d * N_ + n);
    const float p0r = ld_bf(Cre + d * N_ + lane), p0i = ld_bf(Cim + d * N_ + lane);
    const float p1r = ld_bf(Cre + d * N_ + 64 + lane), p1i = ld_bf(Cim + d * N_ + 64 + lane);
    const float dt = rbf(expf(pdt));
    const float ar = -rbf(expf(pla)) * dt;
    const float ai = pai * (isH ? -dt : dt);
    const float c0r = p0r * dt, c0i = p0i * dt, c1r = p1r * dt, c1i = p1i * dt;
    const float S = wave_sum(2.f * (fabsf(c0r) + fabsf(c0i) + fabsf(c1r) + fabsf(c1i)));
    int ex = 0;
    (void)frexpf(S, &ex);
    if (!(S > 0.f) || !(S < 3.0e38f)) ex = 15;
    ex = max(-100, min(ex, 120));
    const float scale = ldexpf(1.f, 15 - ex);
    const float cr = (wave & 1) ? c1r : c0r, ci = (wave & 1) ? c1i : c0i;
    const float base = isH ? 16.f : 1.f;
    const float er = ar * 1.44269504088896340736f * base;
    const float IH = 0.15915494309189533577f;
    const float IL = (float)(0.15915494309189533577 - (double)0.15915494309189533577f);
    float fh = ai * IH;
    float fl = fmaf(ai, IH, -fh) + ai * IL;
    fh *= base; fl *= base;
    STAMP(0, 1);
    unsigned tw = 0u;
    {
      const int pg = ((b >> 3) * 4 + wave) * 64 + lane;
      const char* tp = pg < 8192 ? ws + (size_t)pg * 4096 : (pg < 8704 ? outp + (size_t)(pg - 8192) * 4096 : ws);
      if (pg < 8704) tw = __builtin_nontemporal_load((const unsigned*)tp);
    }
    f32x2 Er2[8], Ei2[8];
#pragma unroll
    for (int k = 0; k < 8; ++k) {
      const float pw = (float)(1 << k);
      const float mag = __builtin_amdgcn_exp2f(er * pw);
      const float ph = __builtin_amdgcn_fractf(fh * pw) + fl * pw;
      const float re = mag * __builtin_amdgcn_cosf(ph), im = mag * __builtin_amdgcn_sinf(ph);
      Er2[k] = f32x2{re, re};
      Ei2[k] = f32x2{-im, im};
    }
    f32x2 tv[41];
    tv[0] = isH ? f32x2{2.f * scale * cr, -2.f * scale * ci} : f32x2{1.f, 0.f};
    char* trow = smem + isH * (41 * TROW) + n * 4;
    *(unsigned*)trow = __builtin_bit_cast(unsigned, __builtin_convertvector(tv[0], f16x2));
    static_for<1, 41>([&](auto I) {
      constexpr int idx = I;
      constexpr int w = tab_w(idx);
      constexpr int lg = ilog2c(w);
      constexpr int ridx = tab_idx(w - (1 << lg));
      const f32x2 sw = __builtin_shufflevector(tv[ridx], tv[ridx], 1, 0);
      tv[idx] = __builtin_elementwise_fma(Ei2[lg], sw, Er2[lg] * tv[ridx]);
      *(unsigned*)(trow + idx * TROW) = __builtin_bit_cast(unsigned, __builtin_convertvector(tv[idx], f16x2));
    });
    int cix[8];
    {
      const int l0 = 2040 - tid * 8;
      const int sh = max((31 - __clz(l0 | 1)) - 7, 0);
      const int one = sh > 0 ? 1 : 0;
#pragma unroll
      for (int e = 0; e < 8; ++e) {
        const int l = l0 + 7 - e;
        cix[e] = 128 * sh + ((l + ((1 << sh) >> 1) - one + ((l >> sh) & one)) >> sh);
      }
    }
    STAMP(0, 2);
    __syncthreads();
    _Float16* kc = (_Float16*)(smem + 82 * TROW);
    {
      const int s = wave, i16 = lane & 15, g = lane >> 4;
      const int hw = (s == 0) ? i16 : (i16 < 8 ? ((8 + i16) << s) : 128);
      const char* arow = smem + 41 * TROW + tab_idx_rt(hw) * TROW + g * 16;
      const char* brow = smem + tab_idx_rt(i16 << s) * TROW + g * 16;
      f16x8 af[8], bf[8];
#pragma unroll
      for (int ks = 0; ks < 8; ++ks) { af[ks] = *(const f16x8*)(arow + ks * 64); bf[ks] = *(const f16x8*)(brow + ks * 64); }
      f32x4 k0 = {0.f, 0.f, 0.f, 0.f}, k1 = {0.f, 0.f, 0.f, 0.f};
#pragma unroll
      for (int ks = 0; ks < 8; ks += 2) {
        k0 = __builtin_amdgcn_mfma_f32_16x16x32_f16(af[ks], bf[ks], k0, 0, 0, 0);
        k1 = __builtin_amdgcn_mfma_f32_16x16x32_f16(af[ks + 1], bf[ks + 1], k1, 0, 0, 0);
      }
#pragma unroll
      for (int r = 0; r < 4; ++r) {
        const int i = 4 * g + r;
        const int cidx = (s == 0) ? (16 * i + i16) : (256 + 128 * (s - 1) + 16 * i + i16);
        const bool ok = (s == 0) || (i < 8) || (s == 3 && i == 8 && i16 == 0);
        if (ok) kc[cidx] = (_Float16)(k0[r] + k1[r]);
      }
    }
    __syncthreads();
    STAMP(0, 3);
    unsigned short o[8];
#pragma unroll
    for (int e = 0; e < 8; ++e) o[e] = ((const unsigned short*)kc)[cix[e]];
    *(u32x4*)(ws + WS_G + (size_t)d * 4096 + tid * 16) =
        u32x4{(unsigned)o[0] | ((unsigned)o[1] << 16), (unsigned)o[2] | ((unsigned)o[3] << 16),
              (unsigned)o[4] | ((unsigned)o[5] << 16), (unsigned)o[6] | ((unsigned)o[7] << 16)};
    if (tid == 0) ((float*)(ws + WS_KINV))[d] = ldexpf(1.f, ex - 15);
    asm volatile("" ::"v"(tw));
    STAMP(0, 15);
    return;
  }
  b -= PB_KGEN;
  if (b < PB_XT) {
    unsigned long long lpc;
    asm volatile("s_getpc_b64 s[40:41]\n\ts_add_u32 s40, s40, s4d_layer_kernel@rel32@lo+4\n\t"
                 "s_addc_u32 s41, s41, s4d_layer_kernel@rel32@hi+12\n\ts_mov_b64 %0, s[40:41]"
                 : "=s"(lpc) :: "scc", "s40", "s41");
    const unsigned cw2 = warm_code<LAYER_CODE>((const char*)lpc, tid);
    const int tile = b * 4 + wave, tt = tile >> 3, ct = tile & 7;
    const int tg = lane & 7, cg = lane >> 3;
    const unsigned short* src = x0 + (size_t)(tt * 64 + tg * 8) * D_ + ct * 64 + cg * 8;
    u32x4 r[8];
#pragma unroll
    for (int j = 0; j < 8; ++j) r[j] = *(const u32x4*)(src + (size_t)j * D_);
    unsigned short* dst = (unsigned short*)(ws + WS_XT0) + (size_t)(ct * 64 + cg * 8) * L_ + tt * 64 + tg * 8;
#pragma unroll
    for (int c = 0; c < 4; ++c) {
      u32x4 lo = {perm_lo(r[1][c], r[0][c]), perm_lo(r[3][c], r[2][c]), perm_lo(r[5][c], r[4][c]), perm_lo(r[7][c], r[6][c])};
      u32x4 hi = {perm_hi(r[1][c], r[0][c]), perm_hi(r[3][c], r[2][c]), perm_hi(r[5][c], r[4][c]), perm_hi(r[7][c], r[6][c])};
      *(u32x4*)(dst + (size_t)(2 * c) * L_) = lo;
      *(u32x4*)(dst + (size_t)(2 * c + 1) * L_) = hi;
    }
    asm volatile("" ::"v"(cw1), "v"(cw2));
    STAMP(0, 15);
    return;
  }
  b -= PB_XT;
  if (b < PB_ST) {
    const int t0 = b * 32 + wave * 8;
    u32x4 r[8];
#pragma unroll
    for (int j = 0; j < 8; ++j) r[j] = *(const u32x4*)(x0 + (size_t)(t0 + j) * D_ + lane * 8);
    for (int i = b * 256 + tid; i < (int)(WS_ZBYTES / 16); i += PB_ST * 256) ((u32x4*)(ws + WS_Z0))[i] = u32x4{0u, 0u, 0u, 0u};
    asm volatile("" : "+v"(r[0]), "+v"(r[1]), "+v"(r[2]), "+v"(r[3]), "+v"(r[4]), "+v"(r[5]), "+v"(r[6]), "+v"(r[7]));
    unsigned word = 0u;
#pragma unroll
    for (int j = 0; j < 8; ++j) {
      float s = 0.f;
#pragma unroll
      for (int c = 0; c < 4; ++c) s += bf_lo(r[j][c]) + bf_hi(r[j][c]);
      const float mean = wave_sum(s) * (1.f / D_);
      float q = 0.f;
#pragma unroll
      for (int c = 0; c < 4; ++c) {
        const float a = bf_lo(r[j][c]) - mean, bb = bf_hi(r[j][c]) - mean;
        q += a * a + bb * bb;
      }
      const float var = wave_sum(q) * (1.f / D_);
      const unsigned wv = pk_bf16(mean, sqrtf(rbf(rbf(var) + 1e-5f)));
      if (lane == j) word = wv;
    }
    if (lane < 8) ((unsigned*)(ws + WS_SW0))[t0 + lane] = word;
    STAMP(0, 15);
    return;
  }
  b -= PB_ST;
  if (b < PB_RT) {
    u32x2 r0[8], r1[8];
    size_t d0_, d1_;
    retile_load(b * 512 + tid, w1, w2, r0, d0_);
    retile_load(b * 512 + 256 + tid, w1, w2, r1, d1_);
    retile_store(r0, ws, d0_);
    retile_store(r1, ws, d1_);
    STAMP(0, 15);
    return;
  }
  b -= PB_RT;
  {
    const int cg = tid % 14, rg = tid / 14;
    const int f0 = b * 112 + cg * 8;
    float s[8] = {0.f, 0.f, 0.f, 0.f, 0.f, 0.f, 0.f, 0.f};
    if (tid < 224) {
#pragma unroll
      for (int h = 0; h < 2; ++h) {
        u32x4 v[16];
#pragma unroll
        for (int j = 0; j < 16; ++j) v[j] = *(const u32x4*)(w1 + (size_t)(rg * 32 + h * 16 + j) * F_ + f0);
#pragma unroll
        for (int j = 0; j < 16; ++j)
#pragma unroll
          for (int c = 0; c < 4; ++c) { s[2 * c] += bf_lo(v[j][c]); s[2 * c + 1] += bf_hi(v[j][c]); }
      }
#pragma unroll
      for (int e = 0; e < 8; ++e) red[rg * 112 + cg * 8 + e] = s[e];
    }
    __syncthreads();
    if (tid < 112) {
      float t = 0.f;
#pragma unroll
      for (int r = 0; r < 16; ++r) t += red[r * 112 + tid];
      ((float*)(ws + WS_W1S))[b * 112 + tid] = t;
    }
    STAMP(0, 15);
  }
}

constexpr int CV_CS = 260 * 16;
constexpr int CV_KCH = 8 * CV_CS;
constexpr int CV_U0 = 2 * CV_KCH;
constexpr int CV_UCH = (256 + 2048) * 2;
constexpr int CV_XS = CV_U0 + 2 * CV_UCH;
constexpr int CV_RED = CV_XS + 2 * 8192;
constexpr int G1_R = 9;
constexpr int G2_R = 9;
constexpr int LAYER_LDS = 9 * 16384 + 1024;
static_assert(CV_RED + 16384 <= 9 * 16384, "conv LDS");
#define G1_W0 5
#define G2_W0 6
#define CV_PF1 16
#define CV_PF2 22
#define W1PRE 5
#define POLL_SLEEP 2
static_assert(G1_W0 <= G1_R - 2 && W1PRE <= G1_R, "ring safety");
__host__ __device__ constexpr int g2_iss(int i) {
  int v = G2_W0 + i;
  return v < 14 ? v : 14;
}
__host__ __device__ constexpr int g1_young(int c) {
  int n = 0;
  for (int j = c + 1; j < c + G1_W0 && j < 16; ++j) n += (j >= W1PRE) ? 4 : 2;
  return n;
}
template <int N> DI void wait_lgkm2(u32x2& a, u32x2& b) {
  asm volatile("s_waitcnt lgkmcnt(%2)" : "+v"(a), "+v"(b) : "n"(N) : "memory");
}
template <int N> DI void wait_lgkm3(u32x2& a, u32x2& b, u32x4& c) {
  asm volatile("s_waitcnt lgkmcnt(%3)" : "+v"(a), "+v"(b), "+v"(c) : "n"(N) : "memory");
}
template <int OFF> DI void lds_read16(u32x4& d, unsigned addr) {
  asm volatile("ds_read_b128 %0, %1 offset:%2" : "=v"(d) : "v"(addr), "n"(OFF));
}
DI void pin4(bf16x8* f) { asm volatile("" : "+v"(f[0]), "+v"(f[1]), "+v"(f[2]), "+v"(f[3])); }
DI void pin7(u32x4* f) { asm volatile("" : "+v"(f[0]), "+v"(f[1]), "+v"(f[2]), "+v"(f[3]), "+v"(f[4]), "+v"(f[5]), "+v"(f[6])); }
DI void st16_sc1(void* p, u32x4 v) { asm volatile("global_store_dwordx4 %0, %1, off sc1\n\ts_nop 1" ::"v"(p), "v"(v) : "memory"); }
DI void st8_sc1(void* p, u32x2 v) { asm volatile("global_store_dwordx2 %0, %1, off sc1\n\ts_nop 1" ::"v"(p), "v"(v) : "memory"); }
DI u32x4 ld16_sc1_wait(const void* p) {
  u32x4 v;
  asm volatile("global_load_dwordx4 %0, %1, off sc1\n\ts_waitcnt vmcnt(0)" : "=&v"(v) : "v"(p) : "memory");
  return v;
}

struct LayerArgs {
  const unsigned short* dskip;
  unsigned short* out;
  char* ws;
};

extern "C" __global__ __launch_bounds__(512, 2) void s4d_layer_kernel(LayerArgs p) {
  const unsigned cw = warm_code<LAYER_CODE>((const char*)__builtin_amdgcn_s_getpc(), threadIdx.x);
  extern __shared__ __attribute__((aligned(16))) char smem[];
  u32x4 wr[28];
#pragma unroll 1
  for (int LAYER = 0; LAYER < 2; ++LAYER) {
  int tid = threadIdx.x;
  asm volatile("" : "+v"(tid));
  const int lane = tid & 63;
  const int wave8 = __builtin_amdgcn_readfirstlane(tid >> 6);
  const int wave = wave8 & 3;
  const int bx = blockIdx.x & 7, bi = blockIdx.x >> 3;
  const int r32 = lane & 31, hh = lane >> 5;
  char* ws = p.ws;
  const char* w1t = ws + WS_W1T;
  const char* w2t = ws + WS_W2T;
  const int mt2 = bx * 4 + (bi & 3), nt2 = bi >> 2;
  const int ni = wave8 & 1, kq = wave8 >> 1;
  const int mi_f = kq >> 1, kh_f = kq & 1;
  const int tok = mt2 * 64 + mi_f * 32 + r32;
  const int dbase = nt2 * 64 + ni * 32 + 4 * hh + 16 * kh_f;
  unsigned short xr[8];
  const char* w2src = w2t + (size_t)(nt2 * 2 + ni) * 112 * 1024 + kq * 2048 + lane * 16;
  auto pf_w2 = [&](auto J) {
    constexpr int j = J;
    wr[j] = *(const u32x4*)(w2src + (j >> 1) * 8192 + (j & 1) * 1024);
  };
  const int mt = (bx >> 1) * 4 + (bi & 3), nt = (bx & 1) * 7 + (bi >> 2);
  const int ft = nt * 4 + wave;
  const int KID = 1 + 3 * LAYER;
  const unsigned short* xin = (const unsigned short*)(ws + (LAYER ? WS_XT2 : WS_XT0));
  unsigned short* xt = (unsigned short*)(ws + (LAYER ? WS_XT3 : WS_XT1));
  char* hbuf = ws + (LAYER ? WS_H2 : WS_H);
  unsigned* sw = (unsigned*)(ws + WS_SW) + LAYER * 2048;
  unsigned* flags = (unsigned*)(ws + WS_FL) + LAYER * 256 * FLS;
  unsigned* cflags = (unsigned*)(ws + WS_CF) + LAYER * (CREP * 16 * 32);
  unsigned* eflags = (unsigned*)(ws + WS_CF) + 2 * (CREP * 16 * 32);
  const unsigned short* xrsrc = xt + ((size_t)(tok >> 7) * D_ + dbase) * 128 + (tok & 127);
  auto pf_xr = [&](auto J) {
    constexpr int j = J;
    xr[j] = __hip_atomic_load(xrsrc + ((j & 3) + 8 * (j >> 2)) * 128, __ATOMIC_RELAXED, __HIP_MEMORY_SCOPE_AGENT);
  };
  STAMP(KID, 0);
  {
    const int d0 = 2 * (bx * 32 + bi);
    const int gz = tid & 255, gc = tid >> 8;
    const int t4 = tid * 4;
    const int ch = wave8 >> 2, half = wave8 & 1, qs = (wave8 >> 1) & 1;
    float xf[2][4], m[4], sd[4];
    const unsigned dsk2 = *(const unsigned*)(p.dskip + d0);
    const char* gp = ws + WS_G + (size_t)(d0 + gc) * 4096 + gz * 16;
    const u32x4 ga = *(const u32x4*)gp;
    const u32x4 gb = *(const u32x4*)(gp + 16);
    const float kscale = ((const float*)(ws + WS_KINV))[d0 + ch];
    u32x4 swv0 = {0u, 0u, 0u, 0u};
    u32x2 xv0 = {0u, 0u}, xv1 = {0u, 0u};
    if (LAYER == 0) {
      swv0 = *(const u32x4*)(ws + WS_SW0 + t4 * 4);
      xv0 = *(const u32x2*)(xin + (size_t)d0 * L_ + t4);
      xv1 = *(const u32x2*)(xin + (size_t)(d0 + 1) * L_ + t4);
    }
    unsigned zz = 0u;
    asm volatile("" : "+v"(zz));
    if (tid < 16) *(u32x4*)(smem + (tid >> 3) * CV_KCH + (tid & 7) * CV_CS + 256 * 16) = u32x4{zz, zz, zz, zz};
    if (tid >= 64 && tid < 128) {
      const int q = tid - 64;
      *(u32x4*)(smem + CV_U0 + (q >> 5) * CV_UCH + (q & 31) * 16) = u32x4{zz, zz, zz, zz};
    }
    if (tid == 128) *(unsigned*)(smem + 9 * 16384) = 0u;
    {
      const unsigned msk = (gz < 255) ? 0xffffffffu : 0u;
      unsigned w[8] = {ga[0], ga[1], ga[2], ga[3], gb[0] & msk, gb[1] & msk, gb[2] & msk, gb[3] & msk};
      unsigned o[7];
#pragma unroll
      for (int k = 0; k < 7; ++k) o[k] = __builtin_amdgcn_alignbit(w[k + 1], w[k], 16);
      char* dst = smem + gc * CV_KCH + gz * 16;
#pragma unroll
      for (int mm = 0; mm < 4; ++mm) {
        *(u32x4*)(dst + (2 * mm) * CV_CS) = u32x4{w[mm], w[mm + 1], w[mm + 2], w[mm + 3]};
        *(u32x4*)(dst + (2 * mm + 1) * CV_CS) = u32x4{o[mm], o[mm + 1], o[mm + 2], o[mm + 3]};
      }
    }
    f32x4 st0 = {0.f, 0.f, 0.f, 0.f}, st1 = {0.f, 0.f, 0.f, 0.f};
    if (LAYER != 0) {
      if (wave8 == 7) {
        const unsigned* cp = eflags + ((blockIdx.x % CREP) * 16 + (lane & 15)) * 32;
        for (int it = 0; it < (1 << 20); ++it) {
          const unsigned f = __hip_atomic_load(cp, __ATOMIC_RELAXED, __HIP_MEMORY_SCOPE_AGENT);
          if (__all(f >= 16u)) break;
          __builtin_amdgcn_s_sleep(POLL_SLEEP);
        }
      }
      __builtin_amdgcn_s_barrier();
      STAMP(KID, 12);
      const auto rs_st = __builtin_amdgcn_make_buffer_rsrc((void*)(ws + WS_ST1), 0, 16384, 0x00020000);
      const auto rs_x = __builtin_amdgcn_make_buffer_rsrc((void*)xin, 0, L_ * D_ * 2, 0x00020000);
      st0 = __builtin_bit_cast(f32x4, __builtin_amdgcn_raw_buffer_load_b128(rs_st, t4 * 8, 0, 16  ));
      st1 = __builtin_bit_cast(f32x4, __builtin_amdgcn_raw_buffer_load_b128(rs_st, t4 * 8 + 16, 0, 16));
      xv0 = __builtin_amdgcn_raw_buffer_load_b64(rs_x, (d0 * L_ + t4) * 2, 0, 16);
      xv1 = __builtin_amdgcn_raw_buffer_load_b64(rs_x, ((d0 + 1) * L_ + t4) * 2, 0, 16);
    }
#pragma unroll
    for (int r = 0; r < 4; ++r) {
      {
        const unsigned wv = swv0[r];
        const float a = (r < 2) ? st0[(r & 1) * 2] : st1[(r & 1) * 2];
        const float bq = (r < 2) ? st0[(r & 1) * 2 + 1] : st1[(r & 1) * 2 + 1];
        const float mean = a * (1.f / D_), var = fmaxf(bq * (1.f / D_) - mean * mean, 0.f);
        const float m1 = rbf(mean), sd1 = rbf(__builtin_amdgcn_sqrtf(rbf(rbf(var) + 1e-5f)));
        m[r] = LAYER ? m1 : bf_lo(wv);
        sd[r] = LAYER ? sd1 : bf_hi(wv);
      }
      const unsigned w0 = xv0[r >> 1], w1_ = xv1[r >> 1];
      xf[0][r] = (r & 1) ? bf_hi(w0) : bf_lo(w0);
      xf[1][r] = (r & 1) ? bf_hi(w1_) : bf_lo(w1_);
    }
    {
      float rs[4];
#pragma unroll
      for (int r = 0; r < 4; ++r) rs[r] = __builtin_amdgcn_rcpf(sd[r]);
#pragma unroll
      for (int c = 0; c < 2; ++c) {
        const float dsk = c ? bf_hi(dsk2) : bf_lo(dsk2);
        float uu[4];
        unsigned xs[4];
#pragma unroll
        for (int r = 0; r < 4; ++r) {
          const float x = xf[c][r];
          const float dd = rbf(x - m[r]);
          float q = dd * rs[r];
          q = fmaf(fmaf(-q, sd[r], dd), rs[r], q);
          uu[r] = rbf(q);
          xs[r] = pk_bf16(x, uu[r] * dsk);
        }
        *(u32x2*)(smem + CV_U0 + c * CV_UCH + (256 + t4) * 2) = u32x2{pk_f16(uu[0], uu[1]), pk_f16(uu[2], uu[3])};
        *(u32x4*)(smem + CV_XS + c * 8192 + t4 * 4) = u32x4{xs[0], xs[1], xs[2], xs[3]};
      }
    }
    asm volatile("" ::"v"(cw));
    __syncthreads();
    STAMP(KID, 1);
    const int j16 = lane & 15, g = lane >> 4;
    const int cc = (7 - j16) & 7;
    const int zl = (2031 - j16 - cc) / 8 + g;
    const char* abase = smem + ch * CV_KCH + cc * CV_CS + (zl - 32 * 7 - 4 * (4 * half + 3)) * 16;
    const char* bbase = smem + CV_U0 + ch * CV_UCH + 480 + 32 * j16 + 16 * g - 64 * (4 * half + 3);
    f32x4 acc[4];
#pragma unroll
    for (int q = 0; q < 4; ++q) acc[q] = f32x4{0.f, 0.f, 0.f, 0.f};
    auto body = [&](auto QS, auto PFF) {
      constexpr int S = QS;
      constexpr bool PF = PFF;
      constexpr int Q[4] = {S ? 6 : 7, S ? 5 : 4, S ? 2 : 3, S ? 1 : 0};
      constexpr int NF = S ? 7 : 8;
      f16x8 A[2][NF], B[2][NF];
      auto ld = [&](auto IT) {
        constexpr int it = IT;
#pragma unroll
        for (int k = 0; k < NF; ++k) {
          A[it & 1][k] = *(const f16x8*)(abase + (7 - k) * 512 + (3 - it) * 64);
          B[it & 1][NF - 1 - k] = *(const f16x8*)(bbase + (NF - 1 - k) * 512 + (3 - it) * 64);
        }
      };
      ld(std::integral_constant<int, 0>{});
      __builtin_amdgcn_sched_barrier(0);
      static_for<0, 4>([&](auto IT) {
        constexpr int it = IT;
        if constexpr (it + 1 < 4) ld(std::integral_constant<int, it + 1>{});
        if constexpr (PF) static_for<it * (CV_PF1 / 4), (it + 1) * (CV_PF1 / 4)>([&](auto J) { pf_w2(J); });
#pragma unroll
        for (int b1 = 0; b1 < NF; ++b1)
#pragma unroll
          for (int e = 0; e < 4; ++e)
            if (b1 <= Q[e]) acc[e] = __builtin_amdgcn_mfma_f32_16x16x32_f16(A[it & 1][b1], B[it & 1][Q[e] - b1], acc[e], 0, 0, 0);
#pragma unroll
        for (int k = 0; k < 18; ++k) {
          __builtin_amdgcn_sched_group_barrier(0x8, 1, 0);
          if (it + 1 < 4 && k >= 2 && k - 2 < 2 * NF) __builtin_amdgcn_sched_group_barrier(0x100, 1, 0);
          if (PF && CV_PF1 > 0 && (k % (16 / (CV_PF1 / 4))) == 1 && k / (16 / (CV_PF1 / 4)) < CV_PF1 / 4)
            __builtin_amdgcn_sched_group_barrier(0x20, 1, 0);
        }
        __builtin_amdgcn_sched_barrier(0);
      });
    };
    if (LAYER == 0) {
      if (qs) body(std::integral_constant<int, 1>{}, std::true_type{}); else body(std::integral_constant<int, 0>{}, std::true_type{});
    } else {
      if (qs) body(std::integral_constant<int, 1>{}, std::false_type{}); else body(std::integral_constant<int, 0>{}, std::false_type{});
    }
    f32x4 keep[2];
    {
      char* wrp = smem + CV_RED + wave8 * 2048 + lane * 16;
#pragma unroll
      for (int e = 0; e < 2; ++e) {
        f32x4 give;
#pragma unroll
        for (int r = 0; r < 4; ++r) {
          const float lo = acc[e][r], hi = acc[2 + e][r];
          give[r] = half ? lo : hi;
          keep[e][r] = half ? hi : lo;
        }
        *(f32x4*)(wrp + e * 1024) = give;
      }
    }
    asm volatile("s_waitcnt lgkmcnt(0)" ::: "memory");
    __builtin_amdgcn_s_barrier();
    STAMP(KID, 2);
    {
      const char* rd = smem + CV_RED + (wave8 ^ 1) * 2048 + lane * 16;
      const int toff = 16 * j16 + 4 * g;
#pragma unroll
      for (int e = 0; e < 2; ++e) {
        const int q = half ? (e ? (qs ? 1 : 0) : (qs ? 2 : 3)) : (e ? (qs ? 5 : 4) : (qs ? 6 : 7));
        const int t = 256 * q + toff;
        const f32x4 o = *(const f32x4*)(rd + e * 1024);
        const u32x4 xs = *(const u32x4*)(smem + CV_XS + ch * 8192 + t * 4);
        const f32x4 y = keep[e] + o;
        float xo[4];
#pragma unroll
        for (int r = 0; r < 4; ++r) {
          const unsigned w = xs[r];
          xo[r] = bf_lo(w) + rbf(rbf(y[r] * kscale) + bf_hi(w));
        }
        st8_sc1(xt + ((size_t)(t >> 7) * D_ + d0 + ch) * 128 + (t & 127), u32x2{pk_bf16(xo[0], xo[1]), pk_bf16(xo[2], xo[3])});
      }
    }
    wait_vm_lgkm0<0>();
    __builtin_amdgcn_s_barrier();
    if (tid < CREP) (void)__hip_atomic_fetch_add(cflags + (tid * 16 + (d0 >> 5)) * 32, 1u, __ATOMIC_RELAXED, __HIP_MEMORY_SCOPE_AGENT);
    STAMP(KID, 3);
  }
  const int grow = lane >> 4, gslot = lane & 15;
  const unsigned short* xsrc = xt + ((size_t)mt * D_ + wave * 8 + grow) * 128 + ((gslot ^ (grow << 2)) * 8);
  const char* wsrc = w1t + (size_t)ft * 32 * 1024 + lane * 16;
  auto issue_w = [&](int c) {
    char* slot = smem + (c % G1_R) * 16384;
#pragma unroll
    for (int i = 0; i < 2; ++i)
      __builtin_amdgcn_global_load_lds((const unsigned*)(wsrc + (size_t)(c * 2 + i) * 1024),
                                       (LAS unsigned*)(slot + 8192 + (wave * 2 + i) * 1024), 16, 0, 0);
  };
  auto issue_x = [&](int c) {
    char* slot = smem + (c % G1_R) * 16384;
#pragma unroll
    for (int i = 0; i < 2; ++i)
      __builtin_amdgcn_global_load_lds((const unsigned*)(xsrc + (size_t)(c * 32 + i * 4) * 128),
                                       (LAS unsigned*)(slot + (wave * 8 + i * 4) * 256), 16, 0, 16  );
  };
  if (bi < 28 && wave8 >= 4) {
    if (wave8 < 6)
      __builtin_amdgcn_global_load_lds((const unsigned*)(ws + WS_W1S) + nt * 128 + wave * 64 + lane,
                                       (LAS unsigned*)(smem + 9 * 16384 + 256 + wave * 256), 4, 0, 0);
#pragma unroll
    for (int c = 0; c < W1PRE; ++c) issue_w(c);
  }
  __builtin_amdgcn_sched_barrier(0);
  if (LAYER == 0) static_for<CV_PF1, 28>([&](auto J) { pf_w2(J); });
  __builtin_amdgcn_sched_barrier(0);
  if (wave8 == 7) {
    const unsigned* cp = cflags + ((blockIdx.x % CREP) * 16 + (lane & 15)) * 32;
    for (int it = 0; it < (1 << 20); ++it) {
      const unsigned f = __hip_atomic_load(cp, __ATOMIC_RELAXED, __HIP_MEMORY_SCOPE_AGENT);
      if (__all(f >= 16u)) break;
      __builtin_amdgcn_s_sleep(POLL_SLEEP);
    }
  }
  __builtin_amdgcn_s_barrier();
  STAMP(KID, 4);
  if (bi >= 28) {
    const int ht = bx * 4 + (bi - 28);
    const int cgp = wave8 * 2 + hh;
    const auto rsrc = __builtin_amdgcn_make_buffer_rsrc((void*)xt, 0, L_ * D_ * 2, 0x00020000);
    const int off0 = ((ht >> 1) * D_ * 128 + (ht & 1) * 64 + 2 * r32) * 2;
    const unsigned pv = __builtin_amdgcn_raw_buffer_load_b32(rsrc, off0, 0, 16  );
    unsigned v[32];
#pragma unroll
    for (int j = 0; j < 32; ++j) v[j] = __builtin_amdgcn_raw_buffer_load_b32(rsrc, off0 + (cgp * 32 + j) * 256, 0, 16  );
    static_for<0, 8>([&](auto J) { pf_xr(J); });
    const f32x2 piv = {bf_lo(pv), bf_hi(pv)};
    f32x2 s1 = {0.f, 0.f}, s2 = {0.f, 0.f};
#pragma unroll
    for (int j = 0; j < 32; ++j) {
      const f32x2 dd = f32x2{bf_lo(v[j]), bf_hi(v[j])} - piv;
      s1 += dd;
      s2 = __builtin_elementwise_fma(dd, dd, s2);
    }
    f32x4* red = (f32x4*)smem;
    red[cgp * 32 + r32] = f32x4{s1[0], s2[0], s1[1], s2[1]};
    asm volatile("s_waitcnt lgkmcnt(0)" ::: "memory");
    __builtin_amdgcn_s_barrier();
    if (tid < 64) {
      const f32x2* r2 = (const f32x2*)smem;
      float a = 0.f, q = 0.f;
#pragma unroll
      for (int w = 0; w < 16; ++w) { const f32x2 t = r2[w * 64 + tid]; a += t[0]; q += t[1]; }
      const unsigned pw = __shfl(pv, tid >> 1, 64);
      const float dm = a * (1.f / D_);
      const float var = fmaxf(q * (1.f / D_) - dm * dm, 0.f);
      const float mm = ((tid & 1) ? bf_hi(pw) : bf_lo(pw)) + dm;
      const float sdv = __builtin_amdgcn_sqrtf(rbf(rbf(var) + 1e-5f));
      __hip_atomic_store(sw + ht * 64 + tid, pk_bf16(mm, sdv), __ATOMIC_RELAXED, __HIP_MEMORY_SCOPE_AGENT);
    }
    STAMP(KID, 12);
  } else if (wave8 >= 4) {
#pragma unroll
    for (int c = 0; c < G1_W0; ++c) { issue_x(c); if (c >= W1PRE) issue_w(c); }
    static_for<0, 16>([&](auto C) {
      constexpr int c = C;
      constexpr int extra = (c + G1_W0 > 16) ? 8 : 0;
      wait_vm<g1_young(c) + extra>();
      __builtin_amdgcn_s_barrier();
      if constexpr (c + G1_W0 < 16) { issue_x(c + G1_W0); if (c + G1_W0 >= W1PRE) issue_w(c + G1_W0); }
      if constexpr (c + G1_W0 == 16) {
        __builtin_amdgcn_sched_barrier(0);
        static_for<0, 8>([&](auto J) { pf_xr(J); });
        __builtin_amdgcn_sched_barrier(0);
      }
    });
  } else {
    const int blk = (lane >> 4) & 1, q4 = (lane & 15) >> 2, p4 = lane & 3;
    const unsigned sbase = (unsigned)(uintptr_t)(LAS char*)smem;
    unsigned* lcnt = (unsigned*)(smem + 9 * 16384);
    unsigned tb[3][4], ta[3];
#pragma unroll
    for (int ct = 0; ct < 4; ++ct) {
      tb[0][ct] = sbase + (unsigned)((8 * hh + q4) * 256 + (((4 * ct + 2 * blk + (p4 >> 1)) ^ (q4 << 2)) * 16) + 8 * (p4 & 1));
      tb[1][ct] = tb[0][ct] + 65536u; tb[2][ct] = tb[0][ct] + 131072u;
    }
    ta[0] = sbase + 8192 + wave * 2048 + lane * 16; ta[1] = ta[0] + 65536u; ta[2] = ta[0] + 131072u;
    f32x16 acc[4];
#pragma unroll
    for (int ct = 0; ct < 4; ++ct)
#pragma unroll
      for (int i = 0; i < 16; ++i) acc[ct][i] = 0.f;
    constexpr int LA = 6;
    u32x2 bb[8][2];
    u32x4 af[3];
    unsigned swv[4];
    f32x4 ws4[4];
    auto issue_pair = [&](auto NN) {
      constexpr int n = NN, ks = n >> 2, ct = n & 3;
      constexpr int so = ((ks >> 1) % G1_R) * 16384, seg = so >> 16, o = (so & 65535) + (ks & 1) * 4096;
      if constexpr (ct == 0) lds_read16<(so & 65535) + (ks & 1) * 1024>(af[ks % 3], ta[seg]);
      tr_read<o>(bb[n & 7][0], tb[seg][ct]);
      tr_read<o + 1024>(bb[n & 7][1], tb[seg][ct]);
    };
    __builtin_amdgcn_s_barrier();
    STAMP(KID, 5);
    static_for<0, LA>([&](auto NN) { issue_pair(NN); });
    static_for<0, 128>([&](auto NN) {
      constexpr int n = NN, ks = n >> 2, ct = n & 3;
      constexpr int last = (n + LA - 1 < 127) ? (n + LA - 1) : 127;
      constexpr int younger = 2 * (last - n) + ((last >> 2) - (n >> 2));
      if constexpr (ct == 0) wait_lgkm3<younger>(bb[n & 7][0], bb[n & 7][1], af[ks % 3]);
      else wait_lgkm2<younger>(bb[n & 7][0], bb[n & 7][1]);
      const u32x4 bu = {bb[n & 7][0][0], bb[n & 7][0][1], bb[n & 7][1][0], bb[n & 7][1][1]};
      acc[ct] = __builtin_amdgcn_mfma_f32_32x32x16_bf16(__builtin_bit_cast(bf16x8, af[ks % 3]), __builtin_bit_cast(bf16x8, bu), acc[ct], 0, 0, 0);
      __builtin_amdgcn_sched_barrier(0);
      if constexpr (n + LA < 128) {
        if constexpr ((n + LA) % 8 == 0) __builtin_amdgcn_s_barrier();
        issue_pair(std::integral_constant<int, n + LA>{});
      }
      if constexpr (n >= 104 && n < 108)
        swv[n - 104] = __hip_atomic_load(sw + mt * 128 + (n - 104) * 32 + r32, __ATOMIC_RELAXED, __HIP_MEMORY_SCOPE_AGENT);
      __builtin_amdgcn_sched_barrier(0);
    });
    STAMP(KID, 6);
    for (int it = 0; it < (1 << 20); ++it) {
      if (__all(swv[0] != 0u && swv[1] != 0u && swv[2] != 0u && swv[3] != 0u)) break;
      __builtin_amdgcn_s_sleep(1);
#pragma unroll
      for (int ct = 0; ct < 4; ++ct)
        swv[ct] = __hip_atomic_load(sw + mt * 128 + ct * 32 + r32, __ATOMIC_RELAXED, __HIP_MEMORY_SCOPE_AGENT);
    }
    asm volatile("" : "+v"(swv[0]), "+v"(swv[1]), "+v"(swv[2]), "+v"(swv[3]));
#pragma unroll
    for (int q = 0; q < 4; ++q) ws4[q] = *(const f32x4*)(smem + 9 * 16384 + 256 + (wave * 32 + 8 * q + 4 * hh) * 4);
#pragma unroll
    for (int ct = 0; ct < 4; ++ct) {
      const float rs = __builtin_amdgcn_rcpf(bf_hi(swv[ct]));
      const float cm = -bf_lo(swv[ct]) * rs;
      char* dst = hbuf + ((size_t)((mt * 4 + ct) * 112 + ft * 2) * 64 + lane) * 16;
      const f32x2 c2 = {cm, cm}, r2 = {rs, rs};
#pragma unroll
      for (int s = 0; s < 2; ++s) {
        u32x4 o;
#pragma unroll
        for (int j = 0; j < 4; ++j) {
          const int reg = 8 * s + 2 * j;
          const f32x2 wsum = {ws4[reg >> 2][reg & 3], ws4[reg >> 2][(reg & 3) + 1]};
          const f32x2 a2 = {acc[ct][reg], acc[ct][reg + 1]};
          const f32x2 v2 = __builtin_elementwise_fma(a2, r2, c2 * wsum);
          const s16x2 pv = __builtin_bit_cast(s16x2, __builtin_convertvector(v2, bf16x2));
          const s16x2 z = {0, 0};
          o[j] = __builtin_bit_cast(unsigned, __builtin_elementwise_max(pv, z));
        }
        st16_sc1(dst + s * 1024, o);
      }
    }
    STAMP(KID, 7);
    wait_vm<0>();
    if (lane == 0) {
      const unsigned old = __hip_atomic_fetch_add(lcnt, 1u, __ATOMIC_RELAXED, __HIP_MEMORY_SCOPE_WORKGROUP);
      if (old == 3u) __hip_atomic_store(flags + (mt * 16 + nt) * FLS, 1u, __ATOMIC_RELAXED, __HIP_MEMORY_SCOPE_AGENT);
    }
    STAMP(KID, 8);
    static_for<0, 8>([&](auto J) { pf_xr(J); });
  }
  pin7(wr); pin7(wr + 7); pin7(wr + 14); pin7(wr + 21);
  {
    unsigned x0_ = xr[0], x1_ = xr[1], x2_ = xr[2], x3_ = xr[3], x4_ = xr[4], x5_ = xr[5], x6_ = xr[6], x7_ = xr[7];
    asm volatile("" : "+v"(x0_), "+v"(x1_), "+v"(x2_), "+v"(x3_), "+v"(x4_), "+v"(x5_), "+v"(x6_), "+v"(x7_));
    xr[0] = x0_; xr[1] = x1_; xr[2] = x2_; xr[3] = x3_; xr[4] = x4_; xr[5] = x5_; xr[6] = x6_; xr[7] = x7_;
  }
  __builtin_amdgcn_s_barrier();
  if (wave8 == 7) {
    const unsigned* fp = flags + ((mt2 >> 1) * 16 + (lane < 14 ? lane : 0)) * FLS;
    for (int it = 0; it < (1 << 20); ++it) {
      const unsigned f = __hip_atomic_load(fp, __ATOMIC_RELAXED, __HIP_MEMORY_SCOPE_AGENT);
      if (__all(f != 0u)) break;
      __builtin_amdgcn_s_sleep(POLL_SLEEP);
    }
  }
  asm volatile("s_waitcnt lgkmcnt(0)" ::: "memory");
  __builtin_amdgcn_s_barrier();
  STAMP(KID, 9);
  const char* hsrc = hbuf + (size_t)(mt2 * 2) * 112 * 1024 + wave8 * 1024 + lane * 16;
  char* dstb = smem + wave8 * 2048;
  auto issue2 = [&](int c) {
#pragma unroll
    for (int mi = 0; mi < 2; ++mi)
      __builtin_amdgcn_global_load_lds((const unsigned*)(hsrc + (size_t)mi * 112 * 1024 + c * 8192),
                                       (LAS unsigned*)(dstb + (c % G2_R) * 16384 + mi * 1024), 16, 0, 16  );
  };
  f32x16 acc[2];
#pragma unroll
  for (int i = 0; i < 16; ++i) { acc[0][i] = 0.f; acc[1][i] = 0.f; }
  const char* rdh = smem + kq * 4096 + lane * 16;
  bf16x8 hf[2][4];
#pragma unroll
  for (int c = 0; c < g2_iss(0); ++c) issue2(c);
  static_for<0, 15>([&](auto C) {
    constexpr int c = C;
    auto mma = [&](auto JJ) {
      constexpr int j = JJ;
      if constexpr (c >= 1) {
        constexpr int pc = c - 1;
#pragma unroll
        for (int mi = 0; mi < 2; ++mi)
          acc[mi] = __builtin_amdgcn_mfma_f32_32x32x16_bf16(__builtin_bit_cast(bf16x8, wr[2 * pc + j]), hf[pc & 1][j * 2 + mi], acc[mi], 0, 0, 0);
      }
    };
    mma(std::integral_constant<int, 0>{});
    __builtin_amdgcn_sched_barrier(0);
    if constexpr (c < 14) {
      wait_vm_lgkm0<2 * (g2_iss(c) - (c + 1))>();
      __builtin_amdgcn_s_barrier();
      if constexpr (c == 0) STAMP(KID, 10);
#pragma unroll
      for (int e = 0; e < 4; ++e) hf[c & 1][e] = *(const bf16x8*)(rdh + (c % G2_R) * 16384 + e * 1024);
    }
    __builtin_amdgcn_sched_barrier(0);
    mma(std::integral_constant<int, 1>{});
    __builtin_amdgcn_sched_barrier(0);
    if constexpr (c < 14) {
#pragma unroll
      for (int cn = g2_iss(c); cn < g2_iss(c + 1); ++cn) issue2(cn);
    }
    __builtin_amdgcn_sched_barrier(0);
  });
  STAMP(KID, 11);
  char* red = smem + 5 * 16384;
  {
    char* dst = red + (ni * 4 + kq * 8) * 0 + (ni * 16 + kq) * 2048 + lane * 16;
#pragma unroll
    for (int pc = 0; pc < 4; ++pc) {
      f32x4 g0, g1;
#pragma unroll
      for (int i = 0; i < 4; ++i) { g0[i] = acc[pc >> 1][8 * (pc & 1) + i]; g1[i] = acc[pc >> 1][8 * (pc & 1) + 4 + i]; }
      *(f32x4*)(dst + pc * 8192) = g0;
      *(f32x4*)(dst + pc * 8192 + 1024) = g1;
    }
  }
  asm volatile("s_waitcnt lgkmcnt(0)" ::: "memory");
  __builtin_amdgcn_s_barrier();
  float mine[8];
  {
    const char* src = red + (ni * 16 + kq * 4) * 2048 + lane * 16;
    f32x4 o[8];
#pragma unroll
    for (int sl = 0; sl < 8; ++sl) o[sl] = *(const f32x4*)(src + sl * 1024);
    asm volatile("" : "+v"(o[0]), "+v"(o[1]), "+v"(o[2]), "+v"(o[3]), "+v"(o[4]), "+v"(o[5]), "+v"(o[6]), "+v"(o[7]));
    const f32x4 t0 = (o[0] + o[2]) + (o[4] + o[6]), t1 = (o[1] + o[3]) + (o[5] + o[7]);
#pragma unroll
    for (int i = 0; i < 4; ++i) { mine[i] = t0[i]; mine[4 + i] = t1[i]; }
  }
  float xo[8];
  float s1 = 0.f, s2 = 0.f;
#pragma unroll
  for (int i = 0; i < 8; ++i) {
    const float v = rbf(__uint_as_float(((unsigned)xr[i]) << 16) + rbf(mine[i]));
    xo[i] = v;
    s1 += v; s2 += v * v;
  }
  if (LAYER == 1) {
#pragma unroll
    for (int q = 0; q < 2; ++q)
      *(u32x2*)(p.out + (size_t)tok * D_ + dbase + 8 * q) =
          u32x2{pk_bf16(xo[4 * q], xo[4 * q + 1]), pk_bf16(xo[4 * q + 2], xo[4 * q + 3])};
  } else {
    unsigned short* xout = (unsigned short*)(ws + WS_XT2);
#pragma unroll
    for (int i = 0; i < 8; ++i)
      __hip_atomic_store(xout + (size_t)(dbase + (i & 3) + 8 * (i >> 2)) * L_ + tok,
                         (unsigned short)(__float_as_uint(xo[i]) >> 16), __ATOMIC_RELAXED, __HIP_MEMORY_SCOPE_AGENT);
    s1 += __shfl_xor(s1, 32, 64);
    s2 += __shfl_xor(s2, 32, 64);
    float* sred = (float*)smem;
    sred[wave8 * 64 + lane] = hh ? s2 : s1;
    asm volatile("s_waitcnt lgkmcnt(0)" ::: "memory");
    __builtin_amdgcn_s_barrier();
    if (wave8 < 2) {
      const int w0 = wave8 * 4;
      const float t = sred[w0 * 64 + lane] + sred[(w0 + 1) * 64 + lane] + sred[(w0 + 2) * 64 + lane] + sred[(w0 + 3) * 64 + lane];
      (void)__hip_atomic_fetch_add((float*)(ws + WS_ST1) + 2 * (mt2 * 64 + wave8 * 32 + r32) + hh, t, __ATOMIC_RELAXED, __HIP_MEMORY_SCOPE_AGENT);
    }
    wait_vm_lgkm0<0>();
    __builtin_amdgcn_s_barrier();
    if (tid < CREP) (void)__hip_atomic_fetch_add(eflags + (tid * 16 + (blockIdx.x & 15)) * 32, 1u, __ATOMIC_RELAXED, __HIP_MEMORY_SCOPE_AGENT);
  }
  STAMP(KID, 15);
  }
}

extern "C" void kernel_launch(void* const* d_in, const int* in_sizes, int n_in,
                              void* d_out, int out_size, void* d_ws, size_t ws_size,
                              hipStream_t stream) {
  const unsigned short* x0 = (const unsigned short*)d_in[0];
  const unsigned short* logA = (const unsigned short*)d_in[1];
  const unsigned short* Aim = (const unsigned short*)d_in[2];
  const unsigned short* Cre = (const unsigned short*)d_in[3];
  const unsigned short* Cim = (const unsigned short*)d_in[4];
  const unsigned short* logdt = (const unsigned short*)d_in[5];
  const unsigned short* dskip = (const unsigned short*)d_in[6];
  const unsigned short* w1 = (const unsigned short*)d_in[8];
  const unsigned short* w2 = (const unsigned short*)d_in[9];
  char* ws = (char*)d_ws;
  (void)hipFuncSetAttribute((const void*)s4d_layer_kernel, hipFuncAttributeMaxDynamicSharedMemorySize, LAYER_LDS);
  prep_kernel<<<PREP_BLOCKS, 256, 0, stream>>>(x0, logA, Aim, Cre, Cim, logdt, w1, w2, ws, (const char*)d_out);
  LayerArgs a0 = {dskip, (unsigned short*)d_out, ws};
  s4d_layer_kernel<<<256, 512, LAYER_LDS, stream>>>(a0);
}
```

```cpp
#include <hip/hip_runtime.h>
#include <hip/hip_bf16.h>
#include <stdint.h>

typedef __attribute__((ext_vector_type(8))) __bf16 bf16x8;
typedef __attribute__((ext_vector_type(2))) __bf16 bf16x2;
typedef __attribute__((ext_vector_type(4))) short s16x4;
typedef __attribute__((ext_vector_type(2))) short s16x2;
typedef __attribute__((ext_vector_type(8))) short s16x8;
typedef __attribute__((ext_vector_type(8))) _Float16 f16x8;
typedef __attribute__((ext_vector_type(2))) _Float16 f16x2;
typedef __attribute__((ext_vector_type(2))) float f32x2;
typedef __attribute__((ext_vector_type(4))) float f32x4;
typedef __attribute__((ext_vector_type(16))) float f32x16;
typedef __attribute__((ext_vector_type(4))) unsigned u32x4;
typedef __attribute__((ext_vector_type(2))) unsigned u32x2;
#define LAS __attribute__((address_space(3)))
#define DI __device__ __forceinline__

constexpr int L_ = 2048, D_ = 512, N_ = 128, F_ = 1792;

constexpr size_t MiB = 1024 * 1024;
constexpr size_t WS_G    = 0;
constexpr size_t WS_XT0  = 2 * MiB;
constexpr size_t WS_XT1  = 4 * MiB;
constexpr size_t WS_XT2  = 6 * MiB;
constexpr size_t WS_XT3  = 8 * MiB;
constexpr size_t WS_W1T  = 10 * MiB;
constexpr size_t WS_W2T  = 12 * MiB;
constexpr size_t WS_H    = 14 * MiB;
constexpr size_t WS_H2   = 24 * MiB;
constexpr size_t WS_KINV = 22 * MiB;
constexpr size_t WS_W1S  = WS_KINV + 4096;
constexpr size_t WS_Z0   = 23 * MiB;
constexpr size_t WS_ST1  = WS_Z0;
constexpr size_t WS_SW   = WS_ST1 + 16384;
#define FLS 32
#define CREP 8
#define CFS (3 * CREP)
constexpr size_t WS_FL   = WS_SW + 16384;
constexpr size_t WS_CF   = WS_FL + 2048 * FLS;
constexpr size_t WS_ZBYTES = 16384 + 16384 + 2048 * FLS + 2048 * CFS;
constexpr size_t WS_SW0  = WS_Z0 + 512 * 1024;

#define STAMP(kid, i)
#define STAMPT(kid, i, T)
DI float bf_lo(unsigned u) { return __uint_as_float(u << 16); }
DI float bf_hi(unsigned u) { return __uint_as_float(u & 0xffff0000u); }
DI unsigned pk_bf16(float a, float b) {
  f32x2 v = {a, b};
  return __builtin_bit_cast(unsigned, __builtin_convertvector(v, bf16x2));
}
DI unsigned pk_f16(float a, float b) {
  f32x2 v = {a, b};
  return __builtin_bit_cast(unsigned, __builtin_convertvector(v, f16x2));
}
DI float rbf(float a) { return bf_lo(pk_bf16(a, 0.f)); }
DI float ld_bf(const unsigned short* p) { return __uint_as_float(((unsigned)*p) << 16); }
DI unsigned perm_lo(unsigned hi, unsigned lo) { return __builtin_amdgcn_perm(hi, lo, 0x05040100u); }
DI unsigned perm_hi(unsigned hi, unsigned lo) { return __builtin_amdgcn_perm(hi, lo, 0x07060302u); }
template <int CTRL> DI float dpp_add(float v) {
  return v + __uint_as_float(__builtin_amdgcn_update_dpp(0u, __float_as_uint(v), CTRL, 0xf, 0xf, true));
}
DI float wave_sum(float v) {
  v = dpp_add<0xB1>(v);
  v = dpp_add<0x4E>(v);
  v = dpp_add<0x141>(v);
  v = dpp_add<0x140>(v);
  const unsigned u = __float_as_uint(v);
  return (__uint_as_float(__builtin_amdgcn_readlane(u, 0)) + __uint_as_float(__builtin_amdgcn_readlane(u, 16))) +
         (__uint_as_float(__builtin_amdgcn_readlane(u, 32)) + __uint_as_float(__builtin_amdgcn_readlane(u, 48)));
}
#define ST_FLAGS ""
DI void st16(void* p, u32x4 v) { asm volatile("global_store_dwordx4 %0, %1, off " ST_FLAGS "\n\ts_nop 1" ::"v"(p), "v"(v) : "memory"); }
DI void st8(void* p, u32x2 v) { asm volatile("global_store_dwordx2 %0, %1, off " ST_FLAGS "\n\ts_nop 1" ::"v"(p), "v"(v) : "memory"); }
DI void st2(void* p, unsigned v) { asm volatile("global_store_short %0, %1, off " ST_FLAGS "\n\ts_nop 1" ::"v"(p), "v"(v) : "memory"); }
template <int N> DI void wait_vm() { asm volatile("s_waitcnt vmcnt(%0)" ::"n"(N) : "memory"); }
template <int OFF> DI void gload16(u32x4& dst, const char* p) {
  asm volatile("global_load_dwordx4 %0, %1, off offset:%2" : "=v"(dst) : "v"(p), "n"(OFF));
}
template <int N> DI void wait_vm4(u32x4& a, u32x4& b, u32x4& c, u32x4& d) {
  asm volatile("s_waitcnt vmcnt(%4)" : "+v"(a), "+v"(b), "+v"(c), "+v"(d) : "n"(N) : "memory");
}
template <int OFF> DI void tr_read(u32x2& d, unsigned addr) {
  asm volatile("ds_read_b64_tr_b16 %0, %1 offset:%2" : "=v"(d) : "v"(addr), "n"(OFF));
}
template <int N> DI void wait_lgkm8(u32x2* b) {
  asm volatile("s_waitcnt lgkmcnt(%8)"
               : "+v"(b[0]), "+v"(b[1]), "+v"(b[2]), "+v"(b[3]), "+v"(b[4]), "+v"(b[5]), "+v"(b[6]), "+v"(b[7])
               : "n"(N) : "memory");
}
template <int N> DI void wait_vm_lgkm0() { asm volatile("s_waitcnt vmcnt(%0) lgkmcnt(0)" ::"n"(N) : "memory"); }
template <int I, int E, class F> DI void static_for(F&& f) {
  if constexpr (I < E) { f(std::integral_constant<int, I>{}); static_for<I + 1, E>(f); }
}

constexpr int TROW = 528;

__host__ __device__ constexpr int tab_w(int idx) {
  return idx < 16 ? idx : (idx < 40 ? ((8 + ((idx - 16) & 7)) << (1 + ((idx - 16) >> 3))) : 128);
}
__host__ __device__ constexpr int ilog2c(int w) { return w <= 1 ? 0 : 1 + ilog2c(w >> 1); }
__host__ __device__ constexpr int tab_idx(int w) {
  return w < 16 ? w : (w == 128 ? 40 : 16 + 8 * (ilog2c(w) - 4) + ((w >> (ilog2c(w) - 3)) - 8));
}
DI int tab_idx_rt(int w) {
  if (w < 16) return w;
  int lg = 31 - __clz(w);
  return 16 + 8 * (lg - 4) + ((w >> (lg - 3)) - 8);
}

DI void retile_load(int u, const unsigned short* __restrict__ w1, const unsigned short* __restrict__ w2, u32x2* r, size_t& dst) {
  if (u < 28672) {
    const int fg = u % 448, o = u / 448;
    const int f = 4 * fg;
#pragma unroll
    for (int j = 0; j < 8; ++j) r[j] = *(const u32x2*)(w1 + (size_t)(8 * o + j) * F_ + f);
    dst = WS_W1T + ((size_t)(((f >> 5) * 32 + (o >> 1)) * 64 + (o & 1) * 32 + (f & 31))) * 16;
  } else {
    const int v = u - 28672;
    const int dg = v & 127, o = v >> 7;
    const int dd = 4 * dg, ks = o >> 1, hh = o & 1;
#pragma unroll
    for (int j = 0; j < 8; ++j)
      r[j] = *(const u32x2*)(w2 + (size_t)(16 * ks + 8 * (j >> 2) + 4 * hh + (j & 3)) * D_ + dd);
    dst = WS_W2T + ((size_t)(((dd >> 5) * 112 + ks) * 64 + hh * 32 + (dd & 31))) * 16;
  }
}
DI void retile_store(const u32x2* r, char* ws, size_t dst) {
#pragma unroll
  for (int c = 0; c < 2; ++c) {
    u32x4 lo = {perm_lo(r[1][c], r[0][c]), perm_lo(r[3][c], r[2][c]), perm_lo(r[5][c], r[4][c]), perm_lo(r[7][c], r[6][c])};
    u32x4 hi = {perm_hi(r[1][c], r[0][c]), perm_hi(r[3][c], r[2][c]), perm_hi(r[5][c], r[4][c]), perm_hi(r[7][c], r[6][c])};
    st16(ws + dst + (2 * c) * 16, lo);
    st16(ws + dst + (2 * c + 1) * 16, hi);
  }
}

#define LAYER_CODE 25600
#define PREP_CODE 14336
template <int BYTES> DI unsigned warm_code(const char* pc, int t) {
  const int o = t * 128;
  return *(const __attribute__((address_space(1))) unsigned*)(uintptr_t)(pc + (o < BYTES ? o : 0));
}
constexpr int PB_KGEN = 512, PB_XT = 64, PB_ST = 64, PB_RT = 112, PB_WS = 16;
constexpr int PREP_BLOCKS = PB_KGEN + PB_XT + PB_ST + PB_RT + PB_WS;

__global__ __launch_bounds__(256) void prep_kernel(
    const unsigned short* __restrict__ x0, const unsigned short* __restrict__ logA,
    const unsigned short* __restrict__ Aim, const unsigned short* __restrict__ Cre,
    const unsigned short* __restrict__ Cim, const unsigned short* __restrict__ logdt,
    const unsigned short* __restrict__ w1, const unsigned short* __restrict__ w2, char* __restrict__ ws) {
  __shared__ __attribute__((aligned(16))) char smem[82 * TROW + 1536];
  float* red = (float*)smem;
  const int tid = threadIdx.x, lane = tid & 63;
  const int wave = __builtin_amdgcn_readfirstlane(tid >> 6);
  int b = blockIdx.x;
  STAMP(0, 0);
  unsigned cw1 = 0u;
  if (b >= PB_KGEN && b < PB_KGEN + PB_XT) cw1 = warm_code<PREP_CODE>((const char*)__builtin_amdgcn_s_getpc(), tid);
  if (b < PB_KGEN) {
    const int d = ((b & 7) * 32 + ((b >> 3) & 31)) * 2 + (b >> 8);
    const int isH = wave >> 1;
    const int n = (wave & 1) * 64 + lane;
    const float pdt = ld_bf(logdt + d), pla = ld_bf(logA + d * N_ + n), pai = ld_bf(Aim + d * N_ + n);
    const float p0r = ld_bf(Cre + d * N_ + lane), p0i = ld_bf(Cim + d * N_ + lane);
    const float p1r = ld_bf(Cre + d * N_ + 64 + lane), p1i = ld_bf(Cim + d * N_ + 64 + lane);
    const float dt = rbf(expf(pdt));
    const float ar = -rbf(expf(pla)) * dt;
    const float ai = pai * (isH ? -dt : dt);
    const float c0r = p0r * dt, c0i = p0i * dt, c1r = p1r * dt, c1i = p1i * dt;
    const float S = wave_sum(2.f * (fabsf(c0r) + fabsf(c0i) + fabsf(c1r) + fabsf(c1i)));
    int ex = 0;
    (void)frexpf(S, &ex);
    if (!(S > 0.f) || !(S < 3.0e38f)) ex = 15;
    ex = max(-100, min(ex, 120));
    const float scale = ldexpf(1.f, 15 - ex);
    const float cr = (wave & 1) ? c1r : c0r, ci = (wave & 1) ? c1i : c0i;
    const float base = isH ? 16.f : 1.f;
    const float er = ar * 1.44269504088896340736f * base;
    const float IH = 0.15915494309189533577f;
    const float IL = (float)(0.15915494309189533577 - (double)0.15915494309189533577f);
    float fh = ai * IH;
    float fl = fmaf(ai, IH, -fh) + ai * IL;
    fh *= base; fl *= base;
    STAMP(0, 1);
    f32x2 Er2[8], Ei2[8];
#pragma unroll
    for (int k = 0; k < 8; ++k) {
      const float pw = (float)(1 << k);
      const float mag = __builtin_amdgcn_exp2f(er * pw);
      const float ph = __builtin_amdgcn_fractf(fh * pw) + fl * pw;
      const float re = mag * __builtin_amdgcn_cosf(ph), im = mag * __builtin_amdgcn_sinf(ph);
      Er2[k] = f32x2{re, re};
      Ei2[k] = f32x2{-im, im};
    }
    f32x2 tv[41];
    tv[0] = isH ? f32x2{2.f * scale * cr, -2.f * scale * ci} : f32x2{1.f, 0.f};
    char* trow = smem + isH * (41 * TROW) + n * 4;
    *(unsigned*)trow = __builtin_bit_cast(unsigned, __builtin_convertvector(tv[0], f16x2));
    static_for<1, 41>([&](auto I) {
      constexpr int idx = I;
      constexpr int w = tab_w(idx);
      constexpr int lg = ilog2c(w);
      constexpr int ridx = tab_idx(w - (1 << lg));
      const f32x2 sw = __builtin_shufflevector(tv[ridx], tv[ridx], 1, 0);
      tv[idx] = __builtin_elementwise_fma(Ei2[lg], sw, Er2[lg] * tv[ridx]);
      *(unsigned*)(trow + idx * TROW) = __builtin_bit_cast(unsigned, __builtin_convertvector(tv[idx], f16x2));
    });
    int cix[8];
    {
      const int l0 = 2040 - tid * 8;
      const int sh = max((31 - __clz(l0 | 1)) - 7, 0);
      const int one = sh > 0 ? 1 : 0;
#pragma unroll
      for (int e = 0; e < 8; ++e) {
        const int l = l0 + 7 - e;
        cix[e] = 128 * sh + ((l + ((1 << sh) >> 1) - one + ((l >> sh) & one)) >> sh);
      }
    }
    STAMP(0, 2);
    __syncthreads();
    _Float16* kc = (_Float16*)(smem + 82 * TROW);
    {
      const int s = wave, i16 = lane & 15, g = lane >> 4;
      const int hw = (s == 0) ? i16 : (i16 < 8 ? ((8 + i16) << s) : 128);
      const char* arow = smem + 41 * TROW + tab_idx_rt(hw) * TROW + g * 16;
      const char* brow = smem + tab_idx_rt(i16 << s) * TROW + g * 16;
      f16x8 af[8], bf[8];
#pragma unroll
      for (int ks = 0; ks < 8; ++ks) { af[ks] = *(const f16x8*)(arow + ks * 64); bf[ks] = *(const f16x8*)(brow + ks * 64); }
      f32x4 k0 = {0.f, 0.f, 0.f, 0.f}, k1 = {0.f, 0.f, 0.f, 0.f};
#pragma unroll
      for (int ks = 0; ks < 8; ks += 2) {
        k0 = __builtin_amdgcn_mfma_f32_16x16x32_f16(af[ks], bf[ks], k0, 0, 0, 0);
        k1 = __builtin_amdgcn_mfma_f32_16x16x32_f16(af[ks + 1], bf[ks + 1], k1, 0, 0, 0);
      }
#pragma unroll
      for (int r = 0; r < 4; ++r) {
        const int i = 4 * g + r;
        const int cidx = (s == 0) ? (16 * i + i16) : (256 + 128 * (s - 1) + 16 * i + i16);
        const bool ok = (s == 0) || (i < 8) || (s == 3 && i == 8 && i16 == 0);
        if (ok) kc[cidx] = (_Float16)(k0[r] + k1[r]);
      }
    }
    __syncthreads();
    STAMP(0, 3);
    unsigned short o[8];
#pragma unroll
    for (int e = 0; e < 8; ++e) o[e] = ((const unsigned short*)kc)[cix[e]];
    *(u32x4*)(ws + WS_G + (size_t)d * 4096 + tid * 16) =
        u32x4{(unsigned)o[0] | ((unsigned)o[1] << 16), (unsigned)o[2] | ((unsigned)o[3] << 16),
              (unsigned)o[4] | ((unsigned)o[5] << 16), (unsigned)o[6] | ((unsigned)o[7] << 16)};
    if (tid == 0) ((float*)(ws + WS_KINV))[d] = ldexpf(1.f, ex - 15);
    STAMP(0, 15);
    return;
  }
  b -= PB_KGEN;
  if (b < PB_XT) {
    unsigned long long lpc;
    asm volatile("s_getpc_b64 s[40:41]\n\ts_add_u32 s40, s40, s4d_layer_kernel@rel32@lo+4\n\t"
                 "s_addc_u32 s41, s41, s4d_layer_kernel@rel32@hi+12\n\ts_mov_b64 %0, s[40:41]"
                 : "=s"(lpc) :: "scc", "s40", "s41");
    const unsigned cw2 = warm_code<LAYER_CODE>((const char*)lpc, tid);
    const int tile = b * 4 + wave, tt = tile >> 3, ct = tile & 7;
    const int tg = lane & 7, cg = lane >> 3;
    const unsigned short* src = x0 + (size_t)(tt * 64 + tg * 8) * D_ + ct * 64 + cg * 8;
    u32x4 r[8];
#pragma unroll
    for (int j = 0; j < 8; ++j) r[j] = *(const u32x4*)(src + (size_t)j * D_);
    unsigned short* dst = (unsigned short*)(ws + WS_XT0) + (size_t)(ct * 64 + cg * 8) * L_ + tt * 64 + tg * 8;
#pragma unroll
    for (int c = 0; c < 4; ++c) {
      u32x4 lo = {perm_lo(r[1][c], r[0][c]), perm_lo(r[3][c], r[2][c]), perm_lo(r[5][c], r[4][c]), perm_lo(r[7][c], r[6][c])};
      u32x4 hi = {perm_hi(r[1][c], r[0][c]), perm_hi(r[3][c], r[2][c]), perm_hi(r[5][c], r[4][c]), perm_hi(r[7][c], r[6][c])};
      *(u32x4*)(dst + (size_t)(2 * c) * L_) = lo;
      *(u32x4*)(dst + (size_t)(2 * c + 1) * L_) = hi;
    }
    asm volatile("" ::"v"(cw1), "v"(cw2));
    STAMP(0, 15);
    return;
  }
  b -= PB_XT;
  if (b < PB_ST) {
    const int t0 = b * 32 + wave * 8;
    u32x4 r[8];
#pragma unroll
    for (int j = 0; j < 8; ++j) r[j] = *(const u32x4*)(x0 + (size_t)(t0 + j) * D_ + lane * 8);
    for (int i = b * 256 + tid; i < (int)(WS_ZBYTES / 16); i += PB_ST * 256) ((u32x4*)(ws + WS_Z0))[i] = u32x4{0u, 0u, 0u, 0u};
    asm volatile("" : "+v"(r[0]), "+v"(r[1]), "+v"(r[2]), "+v"(r[3]), "+v"(r[4]), "+v"(r[5]), "+v"(r[6]), "+v"(r[7]));
    unsigned word = 0u;
#pragma unroll
    for (int j = 0; j < 8; ++j) {
      float s = 0.f;
#pragma unroll
      for (int c = 0; c < 4; ++c) s += bf_lo(r[j][c]) + bf_hi(r[j][c]);
      const float mean = wave_sum(s) * (1.f / D_);
      float q = 0.f;
#pragma unroll
      for (int c = 0; c < 4; ++c) {
        const float a = bf_lo(r[j][c]) - mean, bb = bf_hi(r[j][c]) - mean;
        q += a * a + bb * bb;
      }
      const float var = wave_sum(q) * (1.f / D_);
      const unsigned wv = pk_bf16(mean, sqrtf(rbf(rbf(var) + 1e-5f)));
      if (lane == j) word = wv;
    }
    if (lane < 8) ((unsigned*)(ws + WS_SW0))[t0 + lane] = word;
    STAMP(0, 15);
    return;
  }
  b -= PB_ST;
  if (b < PB_RT) {
    u32x2 r0[8], r1[8];
    size_t d0_, d1_;
    retile_load(b * 512 + tid, w1, w2, r0, d0_);
    retile_load(b * 512 + 256 + tid, w1, w2, r1, d1_);
    retile_store(r0, ws, d0_);
    retile_store(r1, ws, d1_);
    STAMP(0, 15);
    return;
  }
  b -= PB_RT;
  {
    const int cg = tid % 14, rg = tid / 14;
    const int f0 = b * 112 + cg * 8;
    float s[8] = {0.f, 0.f, 0.f, 0.f, 0.f, 0.f, 0.f, 0.f};
    if (tid < 224) {
#pragma unroll
      for (int h = 0; h < 2; ++h) {
        u32x4 v[16];
#pragma unroll
        for (int j = 0; j < 16; ++j) v[j] = *(const u32x4*)(w1 + (size_t)(rg * 32 + h * 16 + j) * F_ + f0);
#pragma unroll
        for (int j = 0; j < 16; ++j)
#pragma unroll
          for (int c = 0; c < 4; ++c) { s[2 * c] += bf_lo(v[j][c]); s[2 * c + 1] += bf_hi(v[j][c]); }
      }
#pragma unroll
      for (int e = 0; e < 8; ++e) red[rg * 112 + cg * 8 + e] = s[e];
    }
    __syncthreads();
    if (tid < 112) {
      float t = 0.f;
#pragma unroll
      for (int r = 0; r < 16; ++r) t += red[r * 112 + tid];
      ((float*)(ws + WS_W1S))[b * 112 + tid] = t;
    }
    STAMP(0, 15);
  }
}

constexpr int CV_CS = 260 * 16;
constexpr int CV_KCH = 8 * CV_CS;
constexpr int CV_U0 = 2 * CV_KCH;
constexpr int CV_UCH = (256 + 2048) * 2;
constexpr int CV_XS = CV_U0 + 2 * CV_UCH;
constexpr int CV_RED = CV_XS + 2 * 8192;
constexpr int G1_R = 9;
constexpr int G2_R = 9;
constexpr int LAYER_LDS = 9 * 16384 + 1024;
static_assert(CV_RED + 16384 <= 9 * 16384, "conv LDS");
#define G1_W0 5
#define G2_W0 6
#define CV_PF1 16
#define CV_PF2 22
#define W1PRE 5
#define POLL_SLEEP 2
static_assert(G1_W0 <= G1_R - 2 && W1PRE <= G1_R, "ring safety");
__host__ __device__ constexpr int g2_iss(int i) {
  int v = G2_W0 + i;
  return v < 14 ? v : 14;
}
__host__ __device__ constexpr int g1_young(int c) {
  int n = 0;
  for (int j = c + 1; j < c + G1_W0 && j < 16; ++j) n += (j >= W1PRE) ? 4 : 2;
  return n;
}
template <int N> DI void wait_lgkm2(u32x2& a, u32x2& b) {
  asm volatile("s_waitcnt lgkmcnt(%2)" : "+v"(a), "+v"(b) : "n"(N) : "memory");
}
template <int N> DI void wait_lgkm3(u32x2& a, u32x2& b, u32x4& c) {
  asm volatile("s_waitcnt lgkmcnt(%3)" : "+v"(a), "+v"(b), "+v"(c) : "n"(N) : "memory");
}
template <int OFF> DI void lds_read16(u32x4& d, unsigned addr) {
  asm volatile("ds_read_b128 %0, %1 offset:%2" : "=v"(d) : "v"(addr), "n"(OFF));
}
DI void pin4(bf16x8* f) { asm volatile("" : "+v"(f[0]), "+v"(f[1]), "+v"(f[2]), "+v"(f[3])); }
DI void pin7(u32x4* f) { asm volatile("" : "+v"(f[0]), "+v"(f[1]), "+v"(f[2]), "+v"(f[3]), "+v"(f[4]), "+v"(f[5]), "+v"(f[6])); }
DI void st16_sc1(void* p, u32x4 v) { asm volatile("global_store_dwordx4 %0, %1, off sc1\n\ts_nop 1" ::"v"(p), "v"(v) : "memory"); }
DI void st8_sc1(void* p, u32x2 v) { asm volatile("global_store_dwordx2 %0, %1, off sc1\n\ts_nop 1" ::"v"(p), "v"(v) : "memory"); }
DI u32x4 ld16_sc1_wait(const void* p) {
  u32x4 v;
  asm volatile("global_load_dwordx4 %0, %1, off sc1\n\ts_waitcnt vmcnt(0)" : "=&v"(v) : "v"(p) : "memory");
  return v;
}

struct LayerArgs {
  const unsigned short* dskip;
  unsigned short* out;
  char* ws;
};

extern "C" __global__ __launch_bounds__(512, 2) void s4d_layer_kernel(LayerArgs p) {
  const unsigned cw = warm_code<LAYER_CODE>((const char*)__builtin_amdgcn_s_getpc(), threadIdx.x);
  extern __shared__ __attribute__((aligned(16))) char smem[];
  u32x4 wr[28];
#pragma unroll 1
  for (int LAYER = 0; LAYER < 2; ++LAYER) {
  int tid = threadIdx.x;
  asm volatile("" : "+v"(tid));
  const int lane = tid & 63;
  const int wave8 = __builtin_amdgcn_readfirstlane(tid >> 6);
  const int wave = wave8 & 3;
  const int bx = blockIdx.x & 7, bi = blockIdx.x >> 3;
  const int r32 = lane & 31, hh = lane >> 5;
  char* ws = p.ws;
  const char* w1t = ws + WS_W1T;
  const char* w2t = ws + WS_W2T;
  const int mt2 = bx * 4 + (bi & 3), nt2 = bi >> 2;
  const int ni = wave8 & 1, kq = wave8 >> 1;
  const int mi_f = kq >> 1, kh_f = kq & 1;
  const int tok = mt2 * 64 + mi_f * 32 + r32;
  const int dbase = nt2 * 64 + ni * 32 + 4 * hh + 16 * kh_f;
  unsigned short xr[8];
  const char* w2src = w2t + (size_t)(nt2 * 2 + ni) * 112 * 1024 + kq * 2048 + lane * 16;
  auto pf_w2 = [&](auto J) {
    constexpr int j = J;
    wr[j] = *(const u32x4*)(w2src + (j >> 1) * 8192 + (j & 1) * 1024);
  };
  const int mt = (bx >> 1) * 4 + (bi & 3), nt = (bx & 1) * 7 + (bi >> 2);
  const int ft = nt * 4 + wave;
  const int KID = 1 + 3 * LAYER;
  const unsigned short* xin = (const unsigned short*)(ws + (LAYER ? WS_XT2 : WS_XT0));
  unsigned short* xt = (unsigned short*)(ws + (LAYER ? WS_XT3 : WS_XT1));
  char* hbuf = ws + (LAYER ? WS_H2 : WS_H);
  unsigned* sw = (unsigned*)(ws + WS_SW) + LAYER * 2048;
  unsigned* flags = (unsigned*)(ws + WS_FL) + LAYER * 256 * FLS;
  unsigned* cflags = (unsigned*)(ws + WS_CF) + LAYER * (CREP * 16 * 32);
  unsigned* eflags = (unsigned*)(ws + WS_CF) + 2 * (CREP * 16 * 32);
  const unsigned short* xrsrc = xt + ((size_t)(tok >> 7) * D_ + dbase) * 128 + (tok & 127);
  auto pf_xr = [&](auto J) {
    constexpr int j = J;
    xr[j] = __hip_atomic_load(xrsrc + ((j & 3) + 8 * (j >> 2)) * 128, __ATOMIC_RELAXED, __HIP_MEMORY_SCOPE_AGENT);
  };
  STAMP(KID, 0);
  {
    const int d0 = 2 * (bx * 32 + bi);
    const int gz = tid & 255, gc = tid >> 8;
    const int t4 = tid * 4;
    const int ch = wave8 >> 2, half = wave8 & 1, qs = (wave8 >> 1) & 1;
    float xf[2][4], m[4], sd[4];
    const unsigned dsk2 = *(const unsigned*)(p.dskip + d0);
    const char* gp = ws + WS_G + (size_t)(d0 + gc) * 4096 + gz * 16;
    const u32x4 ga = *(const u32x4*)gp;
    const u32x4 gb = *(const u32x4*)(gp + 16);
    const float kscale = ((const float*)(ws + WS_KINV))[d0 + ch];
    u32x4 swv0 = {0u, 0u, 0u, 0u};
    u32x2 xv0 = {0u, 0u}, xv1 = {0u, 0u};
    if (LAYER == 0) {
      swv0 = *(const u32x4*)(ws + WS_SW0 + t4 * 4);
      xv0 = *(const u32x2*)(xin + (size_t)d0 * L_ + t4);
      xv1 = *(const u32x2*)(xin + (size_t)(d0 + 1) * L_ + t4);
    }
    unsigned zz = 0u;
    asm volatile("" : "+v"(zz));
    if (tid < 16) *(u32x4*)(smem + (tid >> 3) * CV_KCH + (tid & 7) * CV_CS + 256 * 16) = u32x4{zz, zz, zz, zz};
    if (tid >= 64 && tid < 128) {
      const int q = tid - 64;
      *(u32x4*)(smem + CV_U0 + (q >> 5) * CV_UCH + (q & 31) * 16) = u32x4{zz, zz, zz, zz};
    }
    if (tid == 128) *(unsigned*)(smem + 9 * 16384) = 0u;
    {
      const unsigned msk = (gz < 255) ? 0xffffffffu : 0u;
      unsigned w[8] = {ga[0], ga[1], ga[2], ga[3], gb[0] & msk, gb[1] & msk, gb[2] & msk, gb[3] & msk};
      unsigned o[7];
#pragma unroll
      for (int k = 0; k < 7; ++k) o[k] = __builtin_amdgcn_alignbit(w[k + 1], w[k], 16);
      char* dst = smem + gc * CV_KCH + gz * 16;
#pragma unroll
      for (int mm = 0; mm < 4; ++mm) {
        *(u32x4*)(dst + (2 * mm) * CV_CS) = u32x4{w[mm], w[mm + 1], w[mm + 2], w[mm + 3]};
        *(u32x4*)(dst + (2 * mm + 1) * CV_CS) = u32x4{o[mm], o[mm + 1], o[mm + 2], o[mm + 3]};
      }
    }
    f32x4 st0 = {0.f, 0.f, 0.f, 0.f}, st1 = {0.f, 0.f, 0.f, 0.f};
    if (LAYER != 0) {
      if (wave8 == 7) {
        const unsigned* cp = eflags + ((blockIdx.x % CREP) * 16 + (lane & 15)) * 32;
        for (int it = 0; it < (1 << 20); ++it) {
          const unsigned f = __hip_atomic_load(cp, __ATOMIC_RELAXED, __HIP_MEMORY_SCOPE_AGENT);
          if (__all(f >= 16u)) break;
          __builtin_amdgcn_s_sleep(POLL_SLEEP);
        }
      }
      __builtin_amdgcn_s_barrier();
      STAMP(KID, 12);
      const auto rs_st = __builtin_amdgcn_make_buffer_rsrc((void*)(ws + WS_ST1), 0, 16384, 0x00020000);
      const auto rs_x = __builtin_amdgcn_make_buffer_rsrc((void*)xin, 0, L_ * D_ * 2, 0x00020000);
      st0 = __builtin_bit_cast(f32x4, __builtin_amdgcn_raw_buffer_load_b128(rs_st, t4 * 8, 0, 16  ));
      st1 = __builtin_bit_cast(f32x4, __builtin_amdgcn_raw_buffer_load_b128(rs_st, t4 * 8 + 16, 0, 16));
      xv0 = __builtin_amdgcn_raw_buffer_load_b64(rs_x, (d0 * L_ + t4) * 2, 0, 16);
      xv1 = __builtin_amdgcn_raw_buffer_load_b64(rs_x, ((d0 + 1) * L_ + t4) * 2, 0, 16);
    }
#pragma unroll
    for (int r = 0; r < 4; ++r) {
      {
        const unsigned wv = swv0[r];
        const float a = (r < 2) ? st0[(r & 1) * 2] : st1[(r & 1) * 2];
        const float bq = (r < 2) ? st0[(r & 1) * 2 + 1] : st1[(r & 1) * 2 + 1];
        const float mean = a * (1.f / D_), var = fmaxf(bq * (1.f / D_) - mean * mean, 0.f);
        const float m1 = rbf(mean), sd1 = rbf(__builtin_amdgcn_sqrtf(rbf(rbf(var) + 1e-5f)));
        m[r] = LAYER ? m1 : bf_lo(wv);
        sd[r] = LAYER ? sd1 : bf_hi(wv);
      }
      const unsigned w0 = xv0[r >> 1], w1_ = xv1[r >> 1];
      xf[0][r] = (r & 1) ? bf_hi(w0) : bf_lo(w0);
      xf[1][r] = (r & 1) ? bf_hi(w1_) : bf_lo(w1_);
    }
    {
      float rs[4];
#pragma unroll
      for (int r = 0; r < 4; ++r) rs[r] = __builtin_amdgcn_rcpf(sd[r]);
#pragma unroll
      for (int c = 0; c < 2; ++c) {
        const float dsk = c ? bf_hi(dsk2) : bf_lo(dsk2);
        float uu[4];
        unsigned xs[4];
#pragma unroll
        for (int r = 0; r < 4; ++r) {
          const float x = xf[c][r];
          const float dd = rbf(x - m[r]);
          float q = dd * rs[r];
          q = fmaf(fmaf(-q, sd[r], dd), rs[r], q);
          uu[r] = rbf(q);
          xs[r] = pk_bf16(x, uu[r] * dsk);
        }
        *(u32x2*)(smem + CV_U0 + c * CV_UCH + (256 + t4) * 2) = u32x2{pk_f16(uu[0], uu[1]), pk_f16(uu[2], uu[3])};
        *(u32x4*)(smem + CV_XS + c * 8192 + t4 * 4) = u32x4{xs[0], xs[1], xs[2], xs[3]};
      }
    }
    asm volatile("" ::"v"(cw));
    __syncthreads();
    STAMP(KID, 1);
    const int j16 = lane & 15, g = lane >> 4;
    const int cc = (7 - j16) & 7;
    const int zl = (2031 - j16 - cc) / 8 + g;
    const char* abase = smem + ch * CV_KCH + cc * CV_CS + (zl - 32 * 7 - 4 * (4 * half + 3)) * 16;
    const char* bbase = smem + CV_U0 + ch * CV_UCH + 480 + 32 * j16 + 16 * g - 64 * (4 * half + 3);
    f32x4 acc[4];
#pragma unroll
    for (int q = 0; q < 4; ++q) acc[q] = f32x4{0.f, 0.f, 0.f, 0.f};
    auto body = [&](auto QS, auto PFF) {
      constexpr int S = QS;
      constexpr bool PF = PFF;
      constexpr int Q[4] = {S ? 6 : 7, S ? 5 : 4, S ? 2 : 3, S ? 1 : 0};
      constexpr int NF = S ? 7 : 8;
      f16x8 A[2][NF], B[2][NF];
      auto ld = [&](auto IT) {
        constexpr int it = IT;
#pragma unroll
        for (int k = 0; k < NF; ++k) {
          A[it & 1][k] = *(const f16x8*)(abase + (7 - k) * 512 + (3 - it) * 64);
          B[it & 1][NF - 1 - k] = *(const f16x8*)(bbase + (NF - 1 - k) * 512 + (3 - it) * 64);
        }
      };
      ld(std::integral_constant<int, 0>{});
      __builtin_amdgcn_sched_barrier(0);
      static_for<0, 4>([&](auto IT) {
        constexpr int it = IT;
        if constexpr (it + 1 < 4) ld(std::integral_constant<int, it + 1>{});
        if constexpr (PF) static_for<it * (CV_PF1 / 4), (it + 1) * (CV_PF1 / 4)>([&](auto J) { pf_w2(J); });
#pragma unroll
        for (int b1 = 0; b1 < NF; ++b1)
#pragma unroll
          for (int e = 0; e < 4; ++e)
            if (b1 <= Q[e]) acc[e] = __builtin_amdgcn_mfma_f32_16x16x32_f16(A[it & 1][b1], B[it & 1][Q[e] - b1], acc[e], 0, 0, 0);
#pragma unroll
        for (int k = 0; k < 18; ++k) {
          __builtin_amdgcn_sched_group_barrier(0x8, 1, 0);
          if (it + 1 < 4 && k >= 2 && k - 2 < 2 * NF) __builtin_amdgcn_sched_group_barrier(0x100, 1, 0);
          if (PF && CV_PF1 > 0 && (k % (16 / (CV_PF1 / 4))) == 1 && k / (16 / (CV_PF1 / 4)) < CV_PF1 / 4)
            __builtin_amdgcn_sched_group_barrier(0x20, 1, 0);
        }
        __builtin_amdgcn_sched_barrier(0);
      });
    };
    if (LAYER == 0) {
      if (qs) body(std::integral_constant<int, 1>{}, std::true_type{}); else body(std::integral_constant<int, 0>{}, std::true_type{});
    } else {
      if (qs) body(std::integral_constant<int, 1>{}, std::false_type{}); else body(std::integral_constant<int, 0>{}, std::false_type{});
    }
    f32x4 keep[2];
    {
      char* wrp = smem + CV_RED + wave8 * 2048 + lane * 16;
#pragma unroll
      for (int e = 0; e < 2; ++e) {
        f32x4 give;
#pragma unroll
        for (int r = 0; r < 4; ++r) {
          const float lo = acc[e][r], hi = acc[2 + e][r];
          give[r] = half ? lo : hi;
          keep[e][r] = half ? hi : lo;
        }
        *(f32x4*)(wrp + e * 1024) = give;
      }
    }
    asm volatile("s_waitcnt lgkmcnt(0)" ::: "memory");
    __builtin_amdgcn_s_barrier();
    STAMP(KID, 2);
    {
      const char* rd = smem + CV_RED + (wave8 ^ 1) * 2048 + lane * 16;
      const int toff = 16 * j16 + 4 * g;
#pragma unroll
      for (int e = 0; e < 2; ++e) {
        const int q = half ? (e ? (qs ? 1 : 0) : (qs ? 2 : 3)) : (e ? (qs ? 5 : 4) : (qs ? 6 : 7));
        const int t = 256 * q + toff;
        const f32x4 o = *(const f32x4*)(rd + e * 1024);
        const u32x4 xs = *(const u32x4*)(smem + CV_XS + ch * 8192 + t * 4);
        const f32x4 y = keep[e] + o;
        float xo[4];
#pragma unroll
        for (int r = 0; r < 4; ++r) {
          const unsigned w = xs[r];
          xo[r] = bf_lo(w) + rbf(rbf(y[r] * kscale) + bf_hi(w));
        }
        st8_sc1(xt + ((size_t)(t >> 7) * D_ + d0 + ch) * 128 + (t & 127), u32x2{pk_bf16(xo[0], xo[1]), pk_bf16(xo[2], xo[3])});
      }
    }
    wait_vm_lgkm0<0>();
    __builtin_amdgcn_s_barrier();
    if (tid < CREP) (void)__hip_atomic_fetch_add(cflags + (tid * 16 + (d0 >> 5)) * 32, 1u, __ATOMIC_RELAXED, __HIP_MEMORY_SCOPE_AGENT);
    STAMP(KID, 3);
  }
  const int grow = lane >> 4, gslot = lane & 15;
  const unsigned short* xsrc = xt + ((size_t)mt * D_ + wave * 8 + grow) * 128 + ((gslot ^ (grow << 2)) * 8);
  const char* wsrc = w1t + (size_t)ft * 32 * 1024 + lane * 16;
  auto issue_w = [&](int c) {
    char* slot = smem + (c % G1_R) * 16384;
#pragma unroll
    for (int i = 0; i < 2; ++i)
      __builtin_amdgcn_global_load_lds((const unsigned*)(wsrc + (size_t)(c * 2 + i) * 1024),
                                       (LAS unsigned*)(slot + 8192 + (wave * 2 + i) * 1024), 16, 0, 0);
  };
  auto issue_x = [&](int c) {
    char* slot = smem + (c % G1_R) * 16384;
#pragma unroll
    for (int i = 0; i < 2; ++i)
      __builtin_amdgcn_global_load_lds((const unsigned*)(xsrc + (size_t)(c * 32 + i * 4) * 128),
                                       (LAS unsigned*)(slot + (wave * 8 + i * 4) * 256), 16, 0, 16  );
  };
  if (bi < 28 && wave8 >= 4) {
    if (wave8 < 6)
      __builtin_amdgcn_global_load_lds((const unsigned*)(ws + WS_W1S) + nt * 128 + wave * 64 + lane,
                                       (LAS unsigned*)(smem + 9 * 16384 + 256 + wave * 256), 4, 0, 0);
#pragma unroll
    for (int c = 0; c < W1PRE; ++c) issue_w(c);
  }
  __builtin_amdgcn_sched_barrier(0);
  if (LAYER == 0) static_for<CV_PF1, 28>([&](auto J) { pf_w2(J); });
  __builtin_amdgcn_sched_barrier(0);
  if (wave8 == 7) {
    const unsigned* cp = cflags + ((blockIdx.x % CREP) * 16 + (lane & 15)) * 32;
    for (int it = 0; it < (1 << 20); ++it) {
      const unsigned f = __hip_atomic_load(cp, __ATOMIC_RELAXED, __HIP_MEMORY_SCOPE_AGENT);
      if (__all(f >= 16u)) break;
      __builtin_amdgcn_s_sleep(POLL_SLEEP);
    }
  }
  __builtin_amdgcn_s_barrier();
  STAMP(KID, 4);
  if (bi >= 28) {
    const int ht = bx * 4 + (bi - 28);
    const int cgp = wave8 * 2 + hh;
    const auto rsrc = __builtin_amdgcn_make_buffer_rsrc((void*)xt, 0, L_ * D_ * 2, 0x00020000);
    const int off0 = ((ht >> 1) * D_ * 128 + (ht & 1) * 64 + 2 * r32) * 2;
    const unsigned pv = __builtin_amdgcn_raw_buffer_load_b32(rsrc, off0, 0, 16  );
    unsigned v[32];
#pragma unroll
    for (int j = 0; j < 32; ++j) v[j] = __builtin_amdgcn_raw_buffer_load_b32(rsrc, off0 + (cgp * 32 + j) * 256, 0, 16  );
    static_for<0, 8>([&](auto J) { pf_xr(J); });
    const f32x2 piv = {bf_lo(pv), bf_hi(pv)};
    f32x2 s1 = {0.f, 0.f}, s2 = {0.f, 0.f};
#pragma unroll
    for (int j = 0; j < 32; ++j) {
      const f32x2 dd = f32x2{bf_lo(v[j]), bf_hi(v[j])} - piv;
      s1 += dd;
      s2 = __builtin_elementwise_fma(dd, dd, s2);
    }
    f32x4* red = (f32x4*)smem;
    red[cgp * 32 + r32] = f32x4{s1[0], s2[0], s1[1], s2[1]};
    asm volatile("s_waitcnt lgkmcnt(0)" ::: "memory");
    __builtin_amdgcn_s_barrier();
    if (tid < 64) {
      const f32x2* r2 = (const f32x2*)smem;
      float a = 0.f, q = 0.f;
#pragma unroll
      for (int w = 0; w < 16; ++w) { const f32x2 t = r2[w * 64 + tid]; a += t[0]; q += t[1]; }
      const unsigned pw = __shfl(pv, tid >> 1, 64);
      const float dm = a * (1.f / D_);
      const float var = fmaxf(q * (1.f / D_) - dm * dm, 0.f);
      const float mm = ((tid & 1) ? bf_hi(pw) : bf_lo(pw)) + dm;
      const float sdv = __builtin_amdgcn_sqrtf(rbf(rbf(var) + 1e-5f));
      __hip_atomic_store(sw + ht * 64 + tid, pk_bf16(mm, sdv), __ATOMIC_RELAXED, __HIP_MEMORY_SCOPE_AGENT);
    }
    STAMP(KID, 12);
  } else if (wave8 >= 4) {
#pragma unroll
    for (int c = 0; c < G1_W0; ++c) { issue_x(c); if (c >= W1PRE) issue_w(c); }
    static_for<0, 16>([&](auto C) {
      constexpr int c = C;
      constexpr int extra = (c + G1_W0 > 16) ? 8 : 0;
      wait_vm<g1_young(c) + extra>();
      __builtin_amdgcn_s_barrier();
      if constexpr (c + G1_W0 < 16) { issue_x(c + G1_W0); if (c + G1_W0 >= W1PRE) issue_w(c + G1_W0); }
      if constexpr (c + G1_W0 == 16) {
        __builtin_amdgcn_sched_barrier(0);
        static_for<0, 8>([&](auto J) { pf_xr(J); });
        __builtin_amdgcn_sched_barrier(0);
      }
    });
  } else {
    const int blk = (lane >> 4) & 1, q4 = (lane & 15) >> 2, p4 = lane & 3;
    const unsigned sbase = (unsigned)(uintptr_t)(LAS char*)smem;
    unsigned* lcnt = (unsigned*)(smem + 9 * 16384);
    unsigned tb[3][4], ta[3];
#pragma unroll
    for (int ct = 0; ct < 4; ++ct) {
      tb[0][ct] = sbase + (unsigned)((8 * hh + q4) * 256 + (((4 * ct + 2 * blk + (p4 >> 1)) ^ (q4 << 2)) * 16) + 8 * (p4 & 1));
      tb[1][ct] = tb[0][ct] + 65536u; tb[2][ct] = tb[0][ct] + 131072u;
    }
    ta[0] = sbase + 8192 + wave * 2048 + lane * 16; ta[1] = ta[0] + 65536u; ta[2] = ta[0] + 131072u;
    f32x16 acc[4];
#pragma unroll
    for (int ct = 0; ct < 4; ++ct)
#pragma unroll
      for (int i = 0; i < 16; ++i) acc[ct][i] = 0.f;
    constexpr int LA = 6;
    u32x2 bb[8][2];
    u32x4 af[3];
    unsigned swv[4];
    f32x4 ws4[4];
    auto issue_pair = [&](auto NN) {
      constexpr int n = NN, ks = n >> 2, ct = n & 3;
      constexpr int so = ((ks >> 1) % G1_R) * 16384, seg = so >> 16, o = (so & 65535) + (ks & 1) * 4096;
      if constexpr (ct == 0) lds_read16<(so & 65535) + (ks & 1) * 1024>(af[ks % 3], ta[seg]);
      tr_read<o>(bb[n & 7][0], tb[seg][ct]);
      tr_read<o + 1024>(bb[n & 7][1], tb[seg][ct]);
    };
    __builtin_amdgcn_s_barrier();
    STAMP(KID, 5);
    static_for<0, LA>([&](auto NN) { issue_pair(NN); });
    static_for<0, 128>([&](auto NN) {
      constexpr int n = NN, ks = n >> 2, ct = n & 3;
      constexpr int last = (n + LA - 1 < 127) ? (n + LA - 1) : 127;
      constexpr int younger = 2 * (last - n) + ((last >> 2) - (n >> 2));
      if constexpr (ct == 0) wait_lgkm3<younger>(bb[n & 7][0], bb[n & 7][1], af[ks % 3]);
      else wait_lgkm2<younger>(bb[n & 7][0], bb[n & 7][1]);
      const u32x4 bu = {bb[n & 7][0][0], bb[n & 7][0][1], bb[n & 7][1][0], bb[n & 7][1][1]};
      acc[ct] = __builtin_amdgcn_mfma_f32_32x32x16_bf16(__builtin_bit_cast(bf16x8, af[ks % 3]), __builtin_bit_cast(bf16x8, bu), acc[ct], 0, 0, 0);
      __builtin_amdgcn_sched_barrier(0);
      if constexpr (n + LA < 128) {
        if constexpr ((n + LA) % 8 == 0) __builtin_amdgcn_s_barrier();
        issue_pair(std::integral_constant<int, n + LA>{});
      }
      if constexpr (n >= 104 && n < 108)
        swv[n - 104] = __hip_atomic_load(sw + mt * 128 + (n - 104) * 32 + r32, __ATOMIC_RELAXED, __HIP_MEMORY_SCOPE_AGENT);
      __builtin_amdgcn_sched_barrier(0);
    });
    STAMP(KID, 6);
    for (int it = 0; it < (1 << 20); ++it) {
      if (__all(swv[0] != 0u && swv[1] != 0u && swv[2] != 0u && swv[3] != 0u)) break;
      __builtin_amdgcn_s_sleep(1);
#pragma unroll
      for (int ct = 0; ct < 4; ++ct)
        swv[ct] = __hip_atomic_load(sw + mt * 128 + ct * 32 + r32, __ATOMIC_RELAXED, __HIP_MEMORY_SCOPE_AGENT);
    }
    asm volatile("" : "+v"(swv[0]), "+v"(swv[1]), "+v"(swv[2]), "+v"(swv[3]));
#pragma unroll
    for (int q = 0; q < 4; ++q) ws4[q] = *(const f32x4*)(smem + 9 * 16384 + 256 + (wave * 32 + 8 * q + 4 * hh) * 4);
#pragma unroll
    for (int ct = 0; ct < 4; ++ct) {
      const float rs = __builtin_amdgcn_rcpf(bf_hi(swv[ct]));
      const float cm = -bf_lo(swv[ct]) * rs;
      char* dst = hbuf + ((size_t)((mt * 4 + ct) * 112 + ft * 2) * 64 + lane) * 16;
      const f32x2 c2 = {cm, cm}, r2 = {rs, rs};
#pragma unroll
      for (int s = 0; s < 2; ++s) {
        u32x4 o;
#pragma unroll
        for (int j = 0; j < 4; ++j) {
          const int reg = 8 * s + 2 * j;
          const f32x2 wsum = {ws4[reg >> 2][reg & 3], ws4[reg >> 2][(reg & 3) + 1]};
          const f32x2 a2 = {acc[ct][reg], acc[ct][reg + 1]};
          const f32x2 v2 = __builtin_elementwise_fma(a2, r2, c2 * wsum);
          const s16x2 pv = __builtin_bit_cast(s16x2, __builtin_convertvector(v2, bf16x2));
          const s16x2 z = {0, 0};
          o[j] = __builtin_bit_cast(unsigned, __builtin_elementwise_max(pv, z));
        }
        st16_sc1(dst + s * 1024, o);
      }
    }
    STAMP(KID, 7);
    wait_vm<0>();
    if (lane == 0) {
      const unsigned old = __hip_atomic_fetch_add(lcnt, 1u, __ATOMIC_RELAXED, __HIP_MEMORY_SCOPE_WORKGROUP);
      if (old == 3u) __hip_atomic_store(flags + (mt * 16 + nt) * FLS, 1u, __ATOMIC_RELAXED, __HIP_MEMORY_SCOPE_AGENT);
    }
    STAMP(KID, 8);
    static_for<0, 8>([&](auto J) { pf_xr(J); });
  }
  pin7(wr); pin7(wr + 7); pin7(wr + 14); pin7(wr + 21);
  {
    unsigned x0_ = xr[0], x1_ = xr[1], x2_ = xr[2], x3_ = xr[3], x4_ = xr[4], x5_ = xr[5], x6_ = xr[6], x7_ = xr[7];
    asm volatile("" : "+v"(x0_), "+v"(x1_), "+v"(x2_), "+v"(x3_), "+v"(x4_), "+v"(x5_), "+v"(x6_), "+v"(x7_));
    xr[0] = x0_; xr[1] = x1_; xr[2] = x2_; xr[3] = x3_; xr[4] = x4_; xr[5] = x5_; xr[6] = x6_; xr[7] = x7_;
  }
  __builtin_amdgcn_s_barrier();
  if (wave8 == 7) {
    const unsigned* fp = flags + ((mt2 >> 1) * 16 + (lane < 14 ? lane : 0)) * FLS;
    for (int it = 0; it < (1 << 20); ++it) {
      const unsigned f = __hip_atomic_load(fp, __ATOMIC_RELAXED, __HIP_MEMORY_SCOPE_AGENT);
      if (__all(f != 0u)) break;
      __builtin_amdgcn_s_sleep(POLL_SLEEP);
    }
  }
  asm volatile("s_waitcnt lgkmcnt(0)" ::: "memory");
  __builtin_amdgcn_s_barrier();
  STAMP(KID, 9);
  const char* hsrc = hbuf + (size_t)(mt2 * 2) * 112 * 1024 + wave8 * 1024 + lane * 16;
  char* dstb = smem + wave8 * 2048;
  auto issue2 = [&](int c) {
#pragma unroll
    for (int mi = 0; mi < 2; ++mi)
      __builtin_amdgcn_global_load_lds((const unsigned*)(hsrc + (size_t)mi * 112 * 1024 + c * 8192),
                                       (LAS unsigned*)(dstb + (c % G2_R) * 16384 + mi * 1024), 16, 0, 16  );
  };
  f32x16 acc[2];
#pragma unroll
  for (int i = 0; i < 16; ++i) { acc[0][i] = 0.f; acc[1][i] = 0.f; }
  const char* rdh = smem + kq * 4096 + lane * 16;
  bf16x8 hf[2][4];
#pragma unroll
  for (int c = 0; c < g2_iss(0); ++c) issue2(c);
  static_for<0, 15>([&](auto C) {
    constexpr int c = C;
    auto mma = [&](auto JJ) {
      constexpr int j = JJ;
      if constexpr (c >= 1) {
        constexpr int pc = c - 1;
#pragma unroll
        for (int mi = 0; mi < 2; ++mi)
          acc[mi] = __builtin_amdgcn_mfma_f32_32x32x16_bf16(__builtin_bit_cast(bf16x8, wr[2 * pc + j]), hf[pc & 1][j * 2 + mi], acc[mi], 0, 0, 0);
      }
    };
    mma(std::integral_constant<int, 0>{});
    __builtin_amdgcn_sched_barrier(0);
    if constexpr (c < 14) {
      wait_vm_lgkm0<2 * (g2_iss(c) - (c + 1))>();
      __builtin_amdgcn_s_barrier();
      if constexpr (c == 0) STAMP(KID, 10);
#pragma unroll
      for (int e = 0; e < 4; ++e) hf[c & 1][e] = *(const bf16x8*)(rdh + (c % G2_R) * 16384 + e * 1024);
    }
    __builtin_amdgcn_sched_barrier(0);
    mma(std::integral_constant<int, 1>{});
    __builtin_amdgcn_sched_barrier(0);
    if constexpr (c < 14) {
#pragma unroll
      for (int cn = g2_iss(c); cn < g2_iss(c + 1); ++cn) issue2(cn);
    }
    __builtin_amdgcn_sched_barrier(0);
  });
  STAMP(KID, 11);
  char* red = smem + 5 * 16384;
  {
    char* dst = red + (ni * 4 + kq * 8) * 0 + (ni * 16 + kq) * 2048 + lane * 16;
#pragma unroll
    for (int pc = 0; pc < 4; ++pc) {
      f32x4 g0, g1;
#pragma unroll
      for (int i = 0; i < 4; ++i) { g0[i] = acc[pc >> 1][8 * (pc & 1) + i]; g1[i] = acc[pc >> 1][8 * (pc & 1) + 4 + i]; }
      *(f32x4*)(dst + pc * 8192) = g0;
      *(f32x4*)(dst + pc * 8192 + 1024) = g1;
    }
  }
  asm volatile("s_waitcnt lgkmcnt(0)" ::: "memory");
  __builtin_amdgcn_s_barrier();
  float mine[8];
  {
    const char* src = red + (ni * 16 + kq * 4) * 2048 + lane * 16;
    f32x4 o[8];
#pragma unroll
    for (int sl = 0; sl < 8; ++sl) o[sl] = *(const f32x4*)(src + sl * 1024);
    asm volatile("" : "+v"(o[0]), "+v"(o[1]), "+v"(o[2]), "+v"(o[3]), "+v"(o[4]), "+v"(o[5]), "+v"(o[6]), "+v"(o[7]));
    const f32x4 t0 = (o[0] + o[2]) + (o[4] + o[6]), t1 = (o[1] + o[3]) + (o[5] + o[7]);
#pragma unroll
    for (int i = 0; i < 4; ++i) { mine[i] = t0[i]; mine[4 + i] = t1[i]; }
  }
  float xo[8];
  float s1 = 0.f, s2 = 0.f;
#pragma unroll
  for (int i = 0; i < 8; ++i) {
    const float v = rbf(__uint_as_float(((unsigned)xr[i]) << 16) + rbf(mine[i]));
    xo[i] = v;
    s1 += v; s2 += v * v;
  }
  if (LAYER == 1) {
#pragma unroll
    for (int q = 0; q < 2; ++q)
      *(u32x2*)(p.out + (size_t)tok * D_ + dbase + 8 * q) =
          u32x2{pk_bf16(xo[4 * q], xo[4 * q + 1]), pk_bf16(xo[4 * q + 2], xo[4 * q + 3])};
  } else {
    unsigned short* xout = (unsigned short*)(ws + WS_XT2);
#pragma unroll
    for (int i = 0; i < 8; ++i)
      __hip_atomic_store(xout + (size_t)(dbase + (i & 3) + 8 * (i >> 2)) * L_ + tok,
                         (unsigned short)(__float_as_uint(xo[i]) >> 16), __ATOMIC_RELAXED, __HIP_MEMORY_SCOPE_AGENT);
    s1 += __shfl_xor(s1, 32, 64);
    s2 += __shfl_xor(s2, 32, 64);
    float* sred = (float*)smem;
    sred[wave8 * 64 + lane] = hh ? s2 : s1;
    asm volatile("s_waitcnt lgkmcnt(0)" ::: "memory");
    __builtin_amdgcn_s_barrier();
    if (wave8 < 2) {
      const int w0 = wave8 * 4;
      const float t = sred[w0 * 64 + lane] + sred[(w0 + 1) * 64 + lane] + sred[(w0 + 2) * 64 + lane] + sred[(w0 + 3) * 64 + lane];
      (void)__hip_atomic_fetch_add((float*)(ws + WS_ST1) + 2 * (mt2 * 64 + wave8 * 32 + r32) + hh, t, __ATOMIC_RELAXED, __HIP_MEMORY_SCOPE_AGENT);
    }
    wait_vm_lgkm0<0>();
    __builtin_amdgcn_s_barrier();
    if (tid < CREP) (void)__hip_atomic_fetch_add(eflags + (tid * 16 + (blockIdx.x & 15)) * 32, 1u, __ATOMIC_RELAXED, __HIP_MEMORY_SCOPE_AGENT);
  }
  STAMP(KID, 15);
  }
}

extern "C" void kernel_launch(void* const* d_in, const int* in_sizes, int n_in,
                              void* d_out, int out_size, void* d_ws, size_t ws_size,
                              hipStream_t stream) {
  const unsigned short* x0 = (const unsigned short*)d_in[0];
  const unsigned short* logA = (const unsigned short*)d_in[1];
  const unsigned short* Aim = (const unsigned short*)d_in[2];
  const unsigned short* Cre = (const unsigned short*)d_in[3];
  const unsigned short* Cim = (const unsigned short*)d_in[4];
  const unsigned short* logdt = (const unsigned short*)d_in[5];
  const unsigned short* dskip = (const unsigned short*)d_in[6];
  const unsigned short* w1 = (const unsigned short*)d_in[8];
  const unsigned short* w2 = (const unsigned short*)d_in[9];
  char* ws = (char*)d_ws;
  (void)hipFuncSetAttribute((const void*)s4d_layer_kernel, hipFuncAttributeMaxDynamicSharedMemorySize, LAYER_LDS);
  prep_kernel<<<PREP_BLOCKS, 256, 0, stream>>>(x0, logA, Aim, Cre, Cim, logdt, w1, w2, ws);
  LayerArgs a0 = {dskip, (unsigned short*)d_out, ws};
  s4d_layer_kernel<<<256, 512, LAYER_LDS, stream>>>(a0);
}
```

```cpp
#include <hip/hip_runtime.h>
#include <hip/hip_bf16.h>
#include <stdint.h>

typedef __attribute__((ext_vector_type(8))) __bf16 bf16x8;
typedef __attribute__((ext_vector_type(2))) __bf16 bf16x2;
typedef __attribute__((ext_vector_type(4))) short s16x4;
typedef __attribute__((ext_vector_type(2))) short s16x2;
typedef __attribute__((ext_vector_type(8))) short s16x8;
typedef __attribute__((ext_vector_type(8))) _Float16 f16x8;
typedef __attribute__((ext_vector_type(2))) _Float16 f16x2;
typedef __attribute__((ext_vector_type(2))) float f32x2;
typedef __attribute__((ext_vector_type(4))) float f32x4;
typedef __attribute__((ext_vector_type(16))) float f32x16;
typedef __attribute__((ext_vector_type(4))) unsigned u32x4;
typedef __attribute__((ext_vector_type(2))) unsigned u32x2;
#define LAS __attribute__((address_space(3)))
#define DI __device__ __forceinline__

constexpr int L_ = 2048, D_ = 512, N_ = 128, F_ = 1792;

constexpr size_t MiB = 1024 * 1024;
constexpr size_t WS_G    = 0;
constexpr size_t WS_XT0  = 2 * MiB;
constexpr size_t WS_XT1  = 4 * MiB;
constexpr size_t WS_XT2  = 6 * MiB;
constexpr size_t WS_XT3  = 8 * MiB;
constexpr size_t WS_W1T  = 10 * MiB;
constexpr size_t WS_W2T  = 12 * MiB;
constexpr size_t WS_H    = 14 * MiB;
constexpr size_t WS_H2   = 24 * MiB;
constexpr size_t WS_KINV = 22 * MiB;
constexpr size_t WS_W1S  = WS_KINV + 4096;
constexpr size_t WS_Z0   = 23 * MiB;
constexpr size_t WS_ST1  = WS_Z0;
constexpr size_t WS_SW   = WS_ST1 + 16384;
#define FLS 32
#define CREP 8
#define CFS (3 * CREP)
constexpr size_t WS_FL   = WS_SW + 16384;
constexpr size_t WS_CF   = WS_FL + 2048 * FLS;
constexpr size_t WS_ZBYTES = 16384 + 16384 + 2048 * FLS + 2048 * CFS;
constexpr size_t WS_SW0  = WS_Z0 + 512 * 1024;

#define STAMP(kid, i)
#define STAMPT(kid, i, T)
DI float bf_lo(unsigned u) { return __uint_as_float(u << 16); }
DI float bf_hi(unsigned u) { return __uint_as_float(u & 0xffff0000u); }
DI unsigned pk_bf16(float a, float b) {
  f32x2 v = {a, b};
  return __builtin_bit_cast(unsigned, __builtin_convertvector(v, bf16x2));
}
DI unsigned pk_f16(float a, float b) {
  f32x2 v = {a, b};
  return __builtin_bit_cast(unsigned, __builtin_convertvector(v, f16x2));
}
DI float rbf(float a) { return bf_lo(pk_bf16(a, 0.f)); }
DI float ld_bf(const unsigned short* p) { return __uint_as_float(((unsigned)*p) << 16); }
DI unsigned perm_lo(unsigned hi, unsigned lo) { return __builtin_amdgcn_perm(hi, lo, 0x05040100u); }
DI unsigned perm_hi(unsigned hi, unsigned lo) { return __builtin_amdgcn_perm(hi, lo, 0x07060302u); }
template <int CTRL> DI float dpp_add(float v) {
  return v + __uint_as_float(__builtin_amdgcn_update_dpp(0u, __float_as_uint(v), CTRL, 0xf, 0xf, true));
}
DI float wave_sum(float v) {
  v = dpp_add<0xB1>(v);
  v = dpp_add<0x4E>(v);
  v = dpp_add<0x141>(v);
  v = dpp_add<0x140>(v);
  const unsigned u = __float_as_uint(v);
  return (__uint_as_float(__builtin_amdgcn_readlane(u, 0)) + __uint_as_float(__builtin_amdgcn_readlane(u, 16))) +
         (__uint_as_float(__builtin_amdgcn_readlane(u, 32)) + __uint_as_float(__builtin_amdgcn_readlane(u, 48)));
}
#define ST_FLAGS ""
DI void st16(void* p, u32x4 v) { asm volatile("global_store_dwordx4 %0, %1, off " ST_FLAGS "\n\ts_nop 1" ::"v"(p), "v"(v) : "memory"); }
DI void st8(void* p, u32x2 v) { asm volatile("global_store_dwordx2 %0, %1, off " ST_FLAGS "\n\ts_nop 1" ::"v"(p), "v"(v) : "memory"); }
DI void st2(void* p, unsigned v) { asm volatile("global_store_short %0, %1, off " ST_FLAGS "\n\ts_nop 1" ::"v"(p), "v"(v) : "memory"); }
template <int N> DI void wait_vm() { asm volatile("s_waitcnt vmcnt(%0)" ::"n"(N) : "memory"); }
template <int OFF> DI void gload16(u32x4& dst, const char* p) {
  asm volatile("global_load_dwordx4 %0, %1, off offset:%2" : "=v"(dst) : "v"(p), "n"(OFF));
}
template <int N> DI void wait_vm4(u32x4& a, u32x4& b, u32x4& c, u32x4& d) {
  asm volatile("s_waitcnt vmcnt(%4)" : "+v"(a), "+v"(b), "+v"(c), "+v"(d) : "n"(N) : "memory");
}
template <int OFF> DI void tr_read(u32x2& d, unsigned addr) {
  asm volatile("ds_read_b64_tr_b16 %0, %1 offset:%2" : "=v"(d) : "v"(addr), "n"(OFF));
}
template <int N> DI void wait_lgkm8(u32x2* b) {
  asm volatile("s_waitcnt lgkmcnt(%8)"
               : "+v"(b[0]), "+v"(b[1]), "+v"(b[2]), "+v"(b[3]), "+v"(b[4]), "+v"(b[5]), "+v"(b[6]), "+v"(b[7])
               : "n"(N) : "memory");
}
template <int N> DI void wait_vm_lgkm0() { asm volatile("s_waitcnt vmcnt(%0) lgkmcnt(0)" ::"n"(N) : "memory"); }
template <int I, int E, class F> DI void static_for(F&& f) {
  if constexpr (I < E) { f(std::integral_constant<int, I>{}); static_for<I + 1, E>(f); }
}

constexpr int TROW = 528;

__host__ __device__ constexpr int tab_w(int idx) {
  return idx < 16 ? idx : (idx < 40 ? ((8 + ((idx - 16) & 7)) << (1 + ((idx - 16) >> 3))) : 128);
}
__host__ __device__ constexpr int ilog2c(int w) { return w <= 1 ? 0 : 1 + ilog2c(w >> 1); }
__host__ __device__ constexpr int tab_idx(int w) {
  return w < 16 ? w : (w == 128 ? 40 : 16 + 8 * (ilog2c(w) - 4) + ((w >> (ilog2c(w) - 3)) - 8));
}
DI int tab_idx_rt(int w) {
  if (w < 16) return w;
  int lg = 31 - __clz(w);
  return 16 + 8 * (lg - 4) + ((w >> (lg - 3)) - 8);
}

DI void retile_load(int u, const unsigned short* __restrict__ w1, const unsigned short* __restrict__ w2, u32x2* r, size_t& dst) {
  if (u < 28672) {
    const int fg = u % 448, o = u / 448;
    const int f = 4 * fg;
#pragma unroll
    for (int j = 0; j < 8; ++j) r[j] = *(const u32x2*)(w1 + (size_t)(8 * o + j) * F_ + f);
    dst = WS_W1T + ((size_t)(((f >> 5) * 32 + (o >> 1)) * 64 + (o & 1) * 32 + (f & 31))) * 16;
  } else {
    const int v = u - 28672;
    const int dg = v & 127, o = v >> 7;
    const int dd = 4 * dg, ks = o >> 1, hh = o & 1;
#pragma unroll
    for (int j = 0; j < 8; ++j)
      r[j] = *(const u32x2*)(w2 + (size_t)(16 * ks + 8 * (j >> 2) + 4 * hh + (j & 3)) * D_ + dd);
    dst = WS_W2T + ((size_t)(((dd >> 5) * 112 + ks) * 64 + hh * 32 + (dd & 31))) * 16;
  }
}
DI void retile_store(const u32x2* r, char* ws, size_t dst) {
#pragma unroll
  for (int c = 0; c < 2; ++c) {
    u32x4 lo = {perm_lo(r[1][c], r[0][c]), perm_lo(r[3][c], r[2][c]), perm_lo(r[5][c], r[4][c]), perm_lo(r[7][c], r[6][c])};
    u32x4 hi = {perm_hi(r[1][c], r[0][c]), perm_hi(r[3][c], r[2][c]), perm_hi(r[5][c], r[4][c]), perm_hi(r[7][c], r[6][c])};
    st16(ws + dst + (2 * c) * 16, lo);
    st16(ws + dst + (2 * c + 1) * 16, hi);
  }
}

#define LAYER_CODE 25600
#define PREP_CODE 14336
template <int BYTES> DI unsigned warm_code(const char* pc, int t) {
  const int o = t * 128;
  return *(const __attribute__((address_space(1))) unsigned*)(uintptr_t)(pc + (o < BYTES ? o : 0));
}
constexpr int PB_KGEN = 512, PB_XT = 64, PB_ST = 64, PB_RT = 112, PB_WS = 16;
constexpr int PREP_BLOCKS = PB_KGEN + PB_XT + PB_ST + PB_RT + PB_WS;

__global__ __launch_bounds__(256) void prep_kernel(
    const unsigned short* __restrict__ x0, const unsigned short* __restrict__ logA,
    const unsigned short* __restrict__ Aim, const unsigned short* __restrict__ Cre,
    const unsigned short* __restrict__ Cim, const unsigned short* __restrict__ logdt,
    const unsigned short* __restrict__ w1, const unsigned short* __restrict__ w2, char* __restrict__ ws,
    const char* __restrict__ outp) {
  __shared__ __attribute__((aligned(16))) char smem[82 * TROW + 1536];
  float* red = (float*)smem;
  const int tid = threadIdx.x, lane = tid & 63;
  const int wave = __builtin_amdgcn_readfirstlane(tid >> 6);
  int b = blockIdx.x;
  STAMP(0, 0);
  unsigned cw1 = 0u;
  if (b >= PB_KGEN && b < PB_KGEN + PB_XT) cw1 = warm_code<PREP_CODE>((const char*)__builtin_amdgcn_s_getpc(), tid);
  if (b < PB_KGEN) {
    const int d = ((b & 7) * 32 + ((b >> 3) & 31)) * 2 + (b >> 8);
    const int isH = wave >> 1;
    const int n = (wave & 1) * 64 + lane;
    const float pdt = ld_bf(logdt + d), pla = ld_bf(logA + d * N_ + n), pai = ld_bf(Aim + d * N_ + n);
    const float p0r = ld_bf(Cre + d * N_ + lane), p0i = ld_bf(Cim + d * N_ + lane);
    const float p1r = ld_bf(Cre + d * N_ + 64 + lane), p1i = ld_bf(Cim + d * N_ + 64 + lane);
    const float dt = rbf(expf(pdt));
    const float ar = -rbf(expf(pla)) * dt;
    const float ai = pai * (isH ? -dt : dt);
    const float c0r = p0r * dt, c0i = p0i * dt, c1r = p1r * dt, c1i = p1i * dt;
    const float S = wave_sum(2.f * (fabsf(c0r) + fabsf(c0i) + fabsf(c1r) + fabsf(c1i)));
    int ex = 0;
    (void)frexpf(S, &ex);
    if (!(S > 0.f) || !(S < 3.0e38f)) ex = 15;
    ex = max(-100, min(ex, 120));
    const float scale = ldexpf(1.f, 15 - ex);
    const float cr = (wave & 1) ? c1r : c0r, ci = (wave & 1) ? c1i : c0i;
    const float base = isH ? 16.f : 1.f;
    const float er = ar * 1.44269504088896340736f * base;
    const float IH = 0.15915494309189533577f;
    const float IL = (float)(0.15915494309189533577 - (double)0.15915494309189533577f);
    float fh = ai * IH;
    float fl = fmaf(ai, IH, -fh) + ai * IL;
    fh *= base; fl *= base;
    STAMP(0, 1);
    unsigned tw = 0u;
    {
      const int pg = ((b >> 3) * 4 + wave) * 64 + lane;
      const char* tp = (pg < 512 ? ws + (size_t)pg * 65536 : outp + (size_t)((pg - 512) & 31) * 65536) + (pg & 15) * 256;
      if (pg < 544) tw = __builtin_nontemporal_load((const unsigned*)tp);
    }
    f32x2 Er2[8], Ei2[8];
#pragma unroll
    for (int k = 0; k < 8; ++k) {
      const float pw = (float)(1 << k);
      const float mag = __builtin_amdgcn_exp2f(er * pw);
      const float ph = __builtin_amdgcn_fractf(fh * pw) + fl * pw;
      const float re = mag * __builtin_amdgcn_cosf(ph), im = mag * __builtin_amdgcn_sinf(ph);
      Er2[k] = f32x2{re, re};
      Ei2[k] = f32x2{-im, im};
    }
    f32x2 tv[41];
    tv[0] = isH ? f32x2{2.f * scale * cr, -2.f * scale * ci} : f32x2{1.f, 0.f};
    char* trow = smem + isH * (41 * TROW) + n * 4;
    *(unsigned*)trow = __builtin_bit_cast(unsigned, __builtin_convertvector(tv[0], f16x2));
    static_for<1, 41>([&](auto I) {
      constexpr int idx = I;
      constexpr int w = tab_w(idx);
      constexpr int lg = ilog2c(w);
      constexpr int ridx = tab_idx(w - (1 << lg));
      const f32x2 sw = __builtin_shufflevector(tv[ridx], tv[ridx], 1, 0);
      tv[idx] = __builtin_elementwise_fma(Ei2[lg], sw, Er2[lg] * tv[ridx]);
      *(unsigned*)(trow + idx * TROW) = __builtin_bit_cast(unsigned, __builtin_convertvector(tv[idx], f16x2));
    });
    int cix[8];
    {
      const int l0 = 2040 - tid * 8;
      const int sh = max((31 - __clz(l0 | 1)) - 7, 0);
      const int one = sh > 0 ? 1 : 0;
#pragma unroll
      for (int e = 0; e < 8; ++e) {
        const int l = l0 + 7 - e;
        cix[e] = 128 * sh + ((l + ((1 << sh) >> 1) - one + ((l >> sh) & one)) >> sh);
      }
    }
    STAMP(0, 2);
    __syncthreads();
    _Float16* kc = (_Float16*)(smem + 82 * TROW);
    {
      const int s = wave, i16 = lane & 15, g = lane >> 4;
      const int hw = (s == 0) ? i16 : (i16 < 8 ? ((8 + i16) << s) : 128);
      const char* arow = smem + 41 * TROW + tab_idx_rt(hw) * TROW + g * 16;
      const char* brow = smem + tab_idx_rt(i16 << s) * TROW + g * 16;
      f16x8 af[8], bf[8];
#pragma unroll
      for (int ks = 0; ks < 8; ++ks) { af[ks] = *(const f16x8*)(arow + ks * 64); bf[ks] = *(const f16x8*)(brow + ks * 64); }
      f32x4 k0 = {0.f, 0.f, 0.f, 0.f}, k1 = {0.f, 0.f, 0.f, 0.f};
#pragma unroll
      for (int ks = 0; ks < 8; ks += 2) {
        k0 = __builtin_amdgcn_mfma_f32_16x16x32_f16(af[ks], bf[ks], k0, 0, 0, 0);
        k1 = __builtin_amdgcn_mfma_f32_16x16x32_f16(af[ks + 1], bf[ks + 1], k1, 0, 0, 0);
      }
#pragma unroll
      for (int r = 0; r < 4; ++r) {
        const int i = 4 * g + r;
        const int cidx = (s == 0) ? (16 * i + i16) : (256 + 128 * (s - 1) + 16 * i + i16);
        const bool ok = (s == 0) || (i < 8) || (s == 3 && i == 8 && i16 == 0);
        if (ok) kc[cidx] = (_Float16)(k0[r] + k1[r]);
      }
    }
    __syncthreads();
    STAMP(0, 3);
    unsigned short o[8];
#pragma unroll
    for (int e = 0; e < 8; ++e) o[e] = ((const unsigned short*)kc)[cix[e]];
    *(u32x4*)(ws + WS_G + (size_t)d * 4096 + tid * 16) =
        u32x4{(unsigned)o[0] | ((unsigned)o[1] << 16), (unsigned)o[2] | ((unsigned)o[3] << 16),
              (unsigned)o[4] | ((unsigned)o[5] << 16), (unsigned)o[6] | ((unsigned)o[7] << 16)};
    if (tid == 0) ((float*)(ws + WS_KINV))[d] = ldexpf(1.f, ex - 15);
    asm volatile("" ::"v"(tw));
    STAMP(0, 15);
    return;
  }
  b -= PB_KGEN;
  if (b < PB_XT) {
    unsigned long long lpc;
    asm volatile("s_getpc_b64 s[40:41]\n\ts_add_u32 s40, s40, s4d_layer_kernel@rel32@lo+4\n\t"
                 "s_addc_u32 s41, s41, s4d_layer_kernel@rel32@hi+12\n\ts_mov_b64 %0, s[40:41]"
                 : "=s"(lpc) :: "scc", "s40", "s41");
    const unsigned cw2 = warm_code<LAYER_CODE>((const char*)lpc, tid);
    const int tile = b * 4 + wave, tt = tile >> 3, ct = tile & 7;
    const int tg = lane & 7, cg = lane >> 3;
    const unsigned short* src = x0 + (size_t)(tt * 64 + tg * 8) * D_ + ct * 64 + cg * 8;
    u32x4 r[8];
#pragma unroll
    for (int j = 0; j < 8; ++j) r[j] = *(const u32x4*)(src + (size_t)j * D_);
    unsigned short* dst = (unsigned short*)(ws + WS_XT0) + (size_t)(ct * 64 + cg * 8) * L_ + tt * 64 + tg * 8;
#pragma unroll
    for (int c = 0; c < 4; ++c) {
      u32x4 lo = {perm_lo(r[1][c], r[0][c]), perm_lo(r[3][c], r[2][c]), perm_lo(r[5][c], r[4][c]), perm_lo(r[7][c], r[6][c])};
      u32x4 hi = {perm_hi(r[1][c], r[0][c]), perm_hi(r[3][c], r[2][c]), perm_hi(r[5][c], r[4][c]), perm_hi(r[7][c], r[6][c])};
      *(u32x4*)(dst + (size_t)(2 * c) * L_) = lo;
      *(u32x4*)(dst + (size_t)(2 * c + 1) * L_) = hi;
    }
    asm volatile("" ::"v"(cw1), "v"(cw2));
    STAMP(0, 15);
    return;
  }
  b -= PB_XT;
  if (b < PB_ST) {
    const int t0 = b * 32 + wave * 8;
    u32x4 r[8];
#pragma unroll
    for (int j = 0; j < 8; ++j) r[j] = *(const u32x4*)(x0 + (size_t)(t0 + j) * D_ + lane * 8);
    for (int i = b * 256 + tid; i < (int)(WS_ZBYTES / 16); i += PB_ST * 256) ((u32x4*)(ws + WS_Z0))[i] = u32x4{0u, 0u, 0u, 0u};
    asm volatile("" : "+v"(r[0]), "+v"(r[1]), "+v"(r[2]), "+v"(r[3]), "+v"(r[4]), "+v"(r[5]), "+v"(r[6]), "+v"(r[7]));
    unsigned word = 0u;
#pragma unroll
    for (int j = 0; j < 8; ++j) {
      float s = 0.f;
#pragma unroll
      for (int c = 0; c < 4; ++c) s += bf_lo(r[j][c]) + bf_hi(r[j][c]);
      const float mean = wave_sum(s) * (1.f / D_);
      float q = 0.f;
#pragma unroll
      for (int c = 0; c < 4; ++c) {
        const float a = bf_lo(r[j][c]) - mean, bb = bf_hi(r[j][c]) - mean;
        q += a * a + bb * bb;
      }
      const float var = wave_sum(q) * (1.f / D_);
      const unsigned wv = pk_bf16(mean, sqrtf(rbf(rbf(var) + 1e-5f)));
      if (lane == j) word = wv;
    }
    if (lane < 8) ((unsigned*)(ws + WS_SW0))[t0 + lane] = word;
    STAMP(0, 15);
    return;
  }
  b -= PB_ST;
  if (b < PB_RT) {
    u32x2 r0[8], r1[8];
    size_t d0_, d1_;
    retile_load(b * 512 + tid, w1, w2, r0, d0_);
    retile_load(b * 512 + 256 + tid, w1, w2, r1, d1_);
    retile_store(r0, ws, d0_);
    retile_store(r1, ws, d1_);
    STAMP(0, 15);
    return;
  }
  b -= PB_RT;
  {
    const int cg = tid % 14, rg = tid / 14;
    const int f0 = b * 112 + cg * 8;
    float s[8] = {0.f, 0.f, 0.f, 0.f, 0.f, 0.f, 0.f, 0.f};
    if (tid < 224) {
#pragma unroll
      for (int h = 0; h < 2; ++h) {
        u32x4 v[16];
#pragma unroll
        for (int j = 0; j < 16; ++j) v[j] = *(const u32x4*)(w1 + (size_t)(rg * 32 + h * 16 + j) * F_ + f0);
#pragma unroll
        for (int j = 0; j < 16; ++j)
#pragma unroll
          for (int c = 0; c < 4; ++c) { s[2 * c] += bf_lo(v[j][c]); s[2 * c + 1] += bf_hi(v[j][c]); }
      }
#pragma unroll
      for (int e = 0; e < 8; ++e) red[rg * 112 + cg * 8 + e] = s[e];
    }
    __syncthreads();
    if (tid < 112) {
      float t = 0.f;
#pragma unroll
      for (int r = 0; r < 16; ++r) t += red[r * 112 + tid];
      ((float*)(ws + WS_W1S))[b * 112 + tid] = t;
    }
    STAMP(0, 15);
  }
}

constexpr int CV_CS = 260 * 16;
constexpr int CV_KCH = 8 * CV_CS;
constexpr int CV_U0 = 2 * CV_KCH;
constexpr int CV_UCH = (256 + 2048) * 2;
constexpr int CV_XS = CV_U0 + 2 * CV_UCH;
constexpr int CV_RED = CV_XS + 2 * 8192;
constexpr int G1_R = 9;
constexpr int G2_R = 9;
constexpr int LAYER_LDS = 9 * 16384 + 1024;
static_assert(CV_RED + 16384 <= 9 * 16384, "conv LDS");
#define G1_W0 5
#define G2_W0 6
#define CV_PF1 16
#define CV_PF2 22
#define W1PRE 5
#define POLL_SLEEP 2
static_assert(G1_W0 <= G1_R - 2 && W1PRE <= G1_R, "ring safety");
__host__ __device__ constexpr int g2_iss(int i) {
  int v = G2_W0 + i;
  return v < 14 ? v : 14;
}
__host__ __device__ constexpr int g1_young(int c) {
  int n = 0;
  for (int j = c + 1; j < c + G1_W0 && j < 16; ++j) n += (j >= W1PRE) ? 4 : 2;
  return n;
}
template <int N> DI void wait_lgkm2(u32x2& a, u32x2& b) {
  asm volatile("s_waitcnt lgkmcnt(%2)" : "+v"(a), "+v"(b) : "n"(N) : "memory");
}
template <int N> DI void wait_lgkm3(u32x2& a, u32x2& b, u32x4& c) {
  asm volatile("s_waitcnt lgkmcnt(%3)" : "+v"(a), "+v"(b), "+v"(c) : "n"(N) : "memory");
}
template <int OFF> DI void lds_read16(u32x4& d, unsigned addr) {
  asm volatile("ds_read_b128 %0, %1 offset:%2" : "=v"(d) : "v"(addr), "n"(OFF));
}
DI void pin4(bf16x8* f) { asm volatile("" : "+v"(f[0]), "+v"(f[1]), "+v"(f[2]), "+v"(f[3])); }
DI void pin7(u32x4* f) { asm volatile("" : "+v"(f[0]), "+v"(f[1]), "+v"(f[2]), "+v"(f[3]), "+v"(f[4]), "+v"(f[5]), "+v"(f[6])); }
DI void st16_sc1(void* p, u32x4 v) { asm volatile("global_store_dwordx4 %0, %1, off sc1\n\ts_nop 1" ::"v"(p), "v"(v) : "memory"); }
DI void st8_sc1(void* p, u32x2 v) { asm volatile("global_store_dwordx2 %0, %1, off sc1\n\ts_nop 1" ::"v"(p), "v"(v) : "memory"); }
DI u32x4 ld16_sc1_wait(const void* p) {
  u32x4 v;
  asm volatile("global_load_dwordx4 %0, %1, off sc1\n\ts_waitcnt vmcnt(0)" : "=&v"(v) : "v"(p) : "memory");
  return v;
}

struct LayerArgs {
  const unsigned short* dskip;
  unsigned short* out;
  char* ws;
};

extern "C" __global__ __launch_bounds__(512, 2) void s4d_layer_kernel(LayerArgs p) {
  const unsigned cw = warm_code<LAYER_CODE>((const char*)__builtin_amdgcn_s_getpc(), threadIdx.x);
  extern __shared__ __attribute__((aligned(16))) char smem[];
  u32x4 wr[28];
#pragma unroll 1
  for (int LAYER = 0; LAYER < 2; ++LAYER) {
  int tid = threadIdx.x;
  asm volatile("" : "+v"(tid));
  const int lane = tid & 63;
  const int wave8 = __builtin_amdgcn_readfirstlane(tid >> 6);
  const int wave = wave8 & 3;
  const int bx = blockIdx.x & 7, bi = blockIdx.x >> 3;
  const int r32 = lane & 31, hh = lane >> 5;
  char* ws = p.ws;
  const char* w1t = ws + WS_W1T;
  const char* w2t = ws + WS_W2T;
  const int mt2 = bx * 4 + (bi & 3), nt2 = bi >> 2;
  const int ni = wave8 & 1, kq = wave8 >> 1;
  const int mi_f = kq >> 1, kh_f = kq & 1;
  const int tok = mt2 * 64 + mi_f * 32 + r32;
  const int dbase = nt2 * 64 + ni * 32 + 4 * hh + 16 * kh_f;
  unsigned short xr[8];
  const char* w2src = w2t + (size_t)(nt2 * 2 + ni) * 112 * 1024 + kq * 2048 + lane * 16;
  auto pf_w2 = [&](auto J) {
    constexpr int j = J;
    wr[j] = *(const u32x4*)(w2src + (j >> 1) * 8192 + (j & 1) * 1024);
  };
  const int mt = (bx >> 1) * 4 + (bi & 3), nt = (bx & 1) * 7 + (bi >> 2);
  const int ft = nt * 4 + wave;
  const int KID = 1 + 3 * LAYER;
  const unsigned short* xin = (const unsigned short*)(ws + (LAYER ? WS_XT2 : WS_XT0));
  unsigned short* xt = (unsigned short*)(ws + (LAYER ? WS_XT3 : WS_XT1));
  char* hbuf = ws + (LAYER ? WS_H2 : WS_H);
  unsigned* sw = (unsigned*)(ws + WS_SW) + LAYER * 2048;
  unsigned* flags = (unsigned*)(ws + WS_FL) + LAYER * 256 * FLS;
  unsigned* cflags = (unsigned*)(ws + WS_CF) + LAYER * (CREP * 16 * 32);
  unsigned* eflags = (unsigned*)(ws + WS_CF) + 2 * (CREP * 16 * 32);
  const unsigned short* xrsrc = xt + ((size_t)(tok >> 7) * D_ + dbase) * 128 + (tok & 127);
  auto pf_xr = [&](auto J) {
    constexpr int j = J;
    xr[j] = __hip_atomic_load(xrsrc + ((j & 3) + 8 * (j >> 2)) * 128, __ATOMIC_RELAXED, __HIP_MEMORY_SCOPE_AGENT);
  };
  STAMP(KID, 0);
  {
    const int d0 = 2 * (bx * 32 + bi);
    const int gz = tid & 255, gc = tid >> 8;
    const int t4 = tid * 4;
    const int ch = wave8 >> 2, half = wave8 & 1, qs = (wave8 >> 1) & 1;
    float xf[2][4], m[4], sd[4];
    const unsigned dsk2 = *(const unsigned*)(p.dskip + d0);
    const char* gp = ws + WS_G + (size_t)(d0 + gc) * 4096 + gz * 16;
    const u32x4 ga = *(const u32x4*)gp;
    const u32x4 gb = *(const u32x4*)(gp + 16);
    const float kscale = ((const float*)(ws + WS_KINV))[d0 + ch];
    u32x4 swv0 = {0u, 0u, 0u, 0u};
    u32x2 xv0 = {0u, 0u}, xv1 = {0u, 0u};
    if (LAYER == 0) {
      swv0 = *(const u32x4*)(ws + WS_SW0 + t4 * 4);
      xv0 = *(const u32x2*)(xin + (size_t)d0 * L_ + t4);
      xv1 = *(const u32x2*)(xin + (size_t)(d0 + 1) * L_ + t4);
    }
    unsigned zz = 0u;
    asm volatile("" : "+v"(zz));
    if (tid < 16) *(u32x4*)(smem + (tid >> 3) * CV_KCH + (tid & 7) * CV_CS + 256 * 16) = u32x4{zz, zz, zz, zz};
    if (tid >= 64 && tid < 128) {
      const int q = tid - 64;
      *(u32x4*)(smem + CV_U0 + (q >> 5) * CV_UCH + (q & 31) * 16) = u32x4{zz, zz, zz, zz};
    }
    if (tid == 128) *(unsigned*)(smem + 9 * 16384) = 0u;
    {
      const unsigned msk = (gz < 255) ? 0xffffffffu : 0u;
      unsigned w[8] = {ga[0], ga[1], ga[2], ga[3], gb[0] & msk, gb[1] & msk, gb[2] & msk, gb[3] & msk};
      unsigned o[7];
#pragma unroll
      for (int k = 0; k < 7; ++k) o[k] = __builtin_amdgcn_alignbit(w[k + 1], w[k], 16);
      char* dst = smem + gc * CV_KCH + gz * 16;
#pragma unroll
      for (int mm = 0; mm < 4; ++mm) {
        *(u32x4*)(dst + (2 * mm) * CV_CS) = u32x4{w[mm], w[mm + 1], w[mm + 2], w[mm + 3]};
        *(u32x4*)(dst + (2 * mm + 1) * CV_CS) = u32x4{o[mm], o[mm + 1], o[mm + 2], o[mm + 3]};
      }
    }
    f32x4 st0 = {0.f, 0.f, 0.f, 0.f}, st1 = {0.f, 0.f, 0.f, 0.f};
    if (LAYER != 0) {
      if (wave8 == 7) {
        const unsigned* cp = eflags + ((blockIdx.x % CREP) * 16 + (lane & 15)) * 32;
        for (int it = 0; it < (1 << 20); ++it) {
          const unsigned f = __hip_atomic_load(cp, __ATOMIC_RELAXED, __HIP_MEMORY_SCOPE_AGENT);
          if (__all(f >= 16u)) break;
          __builtin_amdgcn_s_sleep(POLL_SLEEP);
        }
      }
      __builtin_amdgcn_s_barrier();
      STAMP(KID, 12);
      const auto rs_st = __builtin_amdgcn_make_buffer_rsrc((void*)(ws + WS_ST1), 0, 16384, 0x00020000);
      const auto rs_x = __builtin_amdgcn_make_buffer_rsrc((void*)xin, 0, L_ * D_ * 2, 0x00020000);
      st0 = __builtin_bit_cast(f32x4, __builtin_amdgcn_raw_buffer_load_b128(rs_st, t4 * 8, 0, 16  ));
      st1 = __builtin_bit_cast(f32x4, __builtin_amdgcn_raw_buffer_load_b128(rs_st, t4 * 8 + 16, 0, 16));
      xv0 = __builtin_amdgcn_raw_buffer_load_b64(rs_x, (d0 * L_ + t4) * 2, 0, 16);
      xv1 = __builtin_amdgcn_raw_buffer_load_b64(rs_x, ((d0 + 1) * L_ + t4) * 2, 0, 16);
    }
#pragma unroll
    for (int r = 0; r < 4; ++r) {
      {
        const unsigned wv = swv0[r];
        const float a = (r < 2) ? st0[(r & 1) * 2] : st1[(r & 1) * 2];
        const float bq = (r < 2) ? st0[(r & 1) * 2 + 1] : st1[(r & 1) * 2 + 1];
        const float mean = a * (1.f / D_), var = fmaxf(bq * (1.f / D_) - mean * mean, 0.f);
        const float m1 = rbf(mean), sd1 = rbf(__builtin_amdgcn_sqrtf(rbf(rbf(var) + 1e-5f)));
        m[r] = LAYER ? m1 : bf_lo(wv);
        sd[r] = LAYER ? sd1 : bf_hi(wv);
      }
      const unsigned w0 = xv0[r >> 1], w1_ = xv1[r >> 1];
      xf[0][r] = (r & 1) ? bf_hi(w0) : bf_lo(w0);
      xf[1][r] = (r & 1) ? bf_hi(w1_) : bf_lo(w1_);
    }
    {
      float rs[4];
#pragma unroll
      for (int r = 0; r < 4; ++r) rs[r] = __builtin_amdgcn_rcpf(sd[r]);
#pragma unroll
      for (int c = 0; c < 2; ++c) {
        const float dsk = c ? bf_hi(dsk2) : bf_lo(dsk2);
        float uu[4];
        unsigned xs[4];
#pragma unroll
        for (int r = 0; r < 4; ++r) {
          const float x = xf[c][r];
          const float dd = rbf(x - m[r]);
          float q = dd * rs[r];
          q = fmaf(fmaf(-q, sd[r], dd), rs[r], q);
          uu[r] = rbf(q);
          xs[r] = pk_bf16(x, uu[r] * dsk);
        }
        *(u32x2*)(smem + CV_U0 + c * CV_UCH + (256 + t4) * 2) = u32x2{pk_f16(uu[0], uu[1]), pk_f16(uu[2], uu[3])};
        *(u32x4*)(smem + CV_XS + c * 8192 + t4 * 4) = u32x4{xs[0], xs[1], xs[2], xs[3]};
      }
    }
    asm volatile("" ::"v"(cw));
    __syncthreads();
    STAMP(KID, 1);
    const int j16 = lane & 15, g = lane >> 4;
    const int cc = (7 - j16) & 7;
    const int zl = (2031 - j16 - cc) / 8 + g;
    const char* abase = smem + ch * CV_KCH + cc * CV_CS + (zl - 32 * 7 - 4 * (4 * half + 3)) * 16;
    const char* bbase = smem + CV_U0 + ch * CV_UCH + 480 + 32 * j16 + 16 * g - 64 * (4 * half + 3);
    f32x4 acc[4];
#pragma unroll
    for (int q = 0; q < 4; ++q) acc[q] = f32x4{0.f, 0.f, 0.f, 0.f};
    auto body = [&](auto QS, auto PFF) {
      constexpr int S = QS;
      constexpr bool PF = PFF;
      constexpr int Q[4] = {S ? 6 : 7, S ? 5 : 4, S ? 2 : 3, S ? 1 : 0};
      constexpr int NF = S ? 7 : 8;
      f16x8 A[2][NF], B[2][NF];
      auto ld = [&](auto IT) {
        constexpr int it = IT;
#pragma unroll
        for (int k = 0; k < NF; ++k) {
          A[it & 1][k] = *(const f16x8*)(abase + (7 - k) * 512 + (3 - it) * 64);
          B[it & 1][NF - 1 - k] = *(const f16x8*)(bbase + (NF - 1 - k) * 512 + (3 - it) * 64);
        }
      };
      ld(std::integral_constant<int, 0>{});
      __builtin_amdgcn_sched_barrier(0);
      static_for<0, 4>([&](auto IT) {
        constexpr int it = IT;
        if constexpr (it + 1 < 4) ld(std::integral_constant<int, it + 1>{});
        if constexpr (PF) static_for<it * (CV_PF1 / 4), (it + 1) * (CV_PF1 / 4)>([&](auto J) { pf_w2(J); });
#pragma unroll
        for (int b1 = 0; b1 < NF; ++b1)
#pragma unroll
          for (int e = 0; e < 4; ++e)
            if (b1 <= Q[e]) acc[e] = __builtin_amdgcn_mfma_f32_16x16x32_f16(A[it & 1][b1], B[it & 1][Q[e] - b1], acc[e], 0, 0, 0);
#pragma unroll
        for (int k = 0; k < 18; ++k) {
          __builtin_amdgcn_sched_group_barrier(0x8, 1, 0);
          if (it + 1 < 4 && k >= 2 && k - 2 < 2 * NF) __builtin_amdgcn_sched_group_barrier(0x100, 1, 0);
          if (PF && CV_PF1 > 0 && (k % (16 / (CV_PF1 / 4))) == 1 && k / (16 / (CV_PF1 / 4)) < CV_PF1 / 4)
            __builtin_amdgcn_sched_group_barrier(0x20, 1, 0);
        }
        __builtin_amdgcn_sched_barrier(0);
      });
    };
    if (LAYER == 0) {
      if (qs) body(std::integral_constant<int, 1>{}, std::true_type{}); else body(std::integral_constant<int, 0>{}, std::true_type{});
    } else {
      if (qs) body(std::integral_constant<int, 1>{}, std::false_type{}); else body(std::integral_constant<int, 0>{}, std::false_type{});
    }
    f32x4 keep[2];
    {
      char* wrp = smem + CV_RED + wave8 * 2048 + lane * 16;
#pragma unroll
      for (int e = 0; e < 2; ++e) {
        f32x4 give;
#pragma unroll
        for (int r = 0; r < 4; ++r) {
          const float lo = acc[e][r], hi = acc[2 + e][r];
          give[r] = half ? lo : hi;
          keep[e][r] = half ? hi : lo;
        }
        *(f32x4*)(wrp + e * 1024) = give;
      }
    }
    asm volatile("s_waitcnt lgkmcnt(0)" ::: "memory");
    __builtin_amdgcn_s_barrier();
    STAMP(KID, 2);
    {
      const char* rd = smem + CV_RED + (wave8 ^ 1) * 2048 + lane * 16;
      const int toff = 16 * j16 + 4 * g;
#pragma unroll
      for (int e = 0; e < 2; ++e) {
        const int q = half ? (e ? (qs ? 1 : 0) : (qs ? 2 : 3)) : (e ? (qs ? 5 : 4) : (qs ? 6 : 7));
        const int t = 256 * q + toff;
        const f32x4 o = *(const f32x4*)(rd + e * 1024);
        const u32x4 xs = *(const u32x4*)(smem + CV_XS + ch * 8192 + t * 4);
        const f32x4 y = keep[e] + o;
        float xo[4];
#pragma unroll
        for (int r = 0; r < 4; ++r) {
          const unsigned w = xs[r];
          xo[r] = bf_lo(w) + rbf(rbf(y[r] * kscale) + bf_hi(w));
        }
        st8_sc1(xt + ((size_t)(t >> 7) * D_ + d0 + ch) * 128 + (t & 127), u32x2{pk_bf16(xo[0], xo[1]), pk_bf16(xo[2], xo[3])});
      }
    }
    wait_vm_lgkm0<0>();
    __builtin_amdgcn_s_barrier();
    if (tid < CREP) (void)__hip_atomic_fetch_add(cflags + (tid * 16 + (d0 >> 5)) * 32, 1u, __ATOMIC_RELAXED, __HIP_MEMORY_SCOPE_AGENT);
    STAMP(KID, 3);
  }
  const int grow = lane >> 4, gslot = lane & 15;
  const unsigned short* xsrc = xt + ((size_t)mt * D_ + wave * 8 + grow) * 128 + ((gslot ^ (grow << 2)) * 8);
  const char* wsrc = w1t + (size_t)ft * 32 * 1024 + lane * 16;
  auto issue_w = [&](int c) {
    char* slot = smem + (c % G1_R) * 16384;
#pragma unroll
    for (int i = 0; i < 2; ++i)
      __builtin_amdgcn_global_load_lds((const unsigned*)(wsrc + (size_t)(c * 2 + i) * 1024),
                                       (LAS unsigned*)(slot + 8192 + (wave * 2 + i) * 1024), 16, 0, 0);
  };
  auto issue_x = [&](int c) {
    char* slot = smem + (c % G1_R) * 16384;
#pragma unroll
    for (int i = 0; i < 2; ++i)
      __builtin_amdgcn_global_load_lds((const unsigned*)(xsrc + (size_t)(c * 32 + i * 4) * 128),
                                       (LAS unsigned*)(slot + (wave * 8 + i * 4) * 256), 16, 0, 16  );
  };
  if (bi < 28 && wave8 >= 4) {
    if (wave8 < 6)
      __builtin_amdgcn_global_load_lds((const unsigned*)(ws + WS_W1S) + nt * 128 + wave * 64 + lane,
                                       (LAS unsigned*)(smem + 9 * 16384 + 256 + wave * 256), 4, 0, 0);
#pragma unroll
    for (int c = 0; c < W1PRE; ++c) issue_w(c);
  }
  __builtin_amdgcn_sched_barrier(0);
  if (LAYER == 0) static_for<CV_PF1, 28>([&](auto J) { pf_w2(J); });
  __builtin_amdgcn_sched_barrier(0);
  if (wave8 == 7) {
    const unsigned* cp = cflags + ((blockIdx.x % CREP) * 16 + (lane & 15)) * 32;
    for (int it = 0; it < (1 << 20); ++it) {
      const unsigned f = __hip_atomic_load(cp, __ATOMIC_RELAXED, __HIP_MEMORY_SCOPE_AGENT);
      if (__all(f >= 16u)) break;
      __builtin_amdgcn_s_sleep(POLL_SLEEP);
    }
  }
  __builtin_amdgcn_s_barrier();
  STAMP(KID, 4);
  if (bi >= 28) {
    const int ht = bx * 4 + (bi - 28);
    const int cgp = wave8 * 2 + hh;
    const auto rsrc = __builtin_amdgcn_make_buffer_rsrc((void*)xt, 0, L_ * D_ * 2, 0x00020000);
    const int off0 = ((ht >> 1) * D_ * 128 + (ht & 1) * 64 + 2 * r32) * 2;
    const unsigned pv = __builtin_amdgcn_raw_buffer_load_b32(rsrc, off0, 0, 16  );
    unsigned v[32];
#pragma unroll
    for (int j = 0; j < 32; ++j) v[j] = __builtin_amdgcn_raw_buffer_load_b32(rsrc, off0 + (cgp * 32 + j) * 256, 0, 16  );
    static_for<0, 8>([&](auto J) { pf_xr(J); });
    const f32x2 piv = {bf_lo(pv), bf_hi(pv)};
    f32x2 s1 = {0.f, 0.f}, s2 = {0.f, 0.f};
#pragma unroll
    for (int j = 0; j < 32; ++j) {
      const f32x2 dd = f32x2{bf_lo(v[j]), bf_hi(v[j])} - piv;
      s1 += dd;
      s2 = __builtin_elementwise_fma(dd, dd, s2);
    }
    f32x4* red = (f32x4*)smem;
    red[cgp * 32 + r32] = f32x4{s1[0], s2[0], s1[1], s2[1]};
    asm volatile("s_waitcnt lgkmcnt(0)" ::: "memory");
    __builtin_amdgcn_s_barrier();
    if (tid < 64) {
      const f32x2* r2 = (const f32x2*)smem;
      float a = 0.f, q = 0.f;
#pragma unroll
      for (int w = 0; w < 16; ++w) { const f32x2 t = r2[w * 64 + tid]; a += t[0]; q += t[1]; }
      const unsigned pw = __shfl(pv, tid >> 1, 64);
      const float dm = a * (1.f / D_);
      const float var = fmaxf(q * (1.f / D_) - dm * dm, 0.f);
      const float mm = ((tid & 1) ? bf_hi(pw) : bf_lo(pw)) + dm;
      const float sdv = __builtin_amdgcn_sqrtf(rbf(rbf(var) + 1e-5f));
      __hip_atomic_store(sw + ht * 64 + tid, pk_bf16(mm, sdv), __ATOMIC_RELAXED, __HIP_MEMORY_SCOPE_AGENT);
    }
    STAMP(KID, 12);
  } else if (wave8 >= 4) {
#pragma unroll
    for (int c = 0; c < G1_W0; ++c) { issue_x(c); if (c >= W1PRE) issue_w(c); }
    static_for<0, 16>([&](auto C) {
      constexpr int c = C;
      constexpr int extra = (c + G1_W0 > 16) ? 8 : 0;
      wait_vm<g1_young(c) + extra>();
      __builtin_amdgcn_s_barrier();
      if constexpr (c + G1_W0 < 16) { issue_x(c + G1_W0); if (c + G1_W0 >= W1PRE) issue_w(c + G1_W0); }
      if constexpr (c + G1_W0 == 16) {
        __builtin_amdgcn_sched_barrier(0);
        static_for<0, 8>([&](auto J) { pf_xr(J); });
        __builtin_amdgcn_sched_barrier(0);
      }
    });
  } else {
    const int blk = (lane >> 4) & 1, q4 = (lane & 15) >> 2, p4 = lane & 3;
    const unsigned sbase = (unsigned)(uintptr_t)(LAS char*)smem;
    unsigned* lcnt = (unsigned*)(smem + 9 * 16384);
    unsigned tb[3][4], ta[3];
#pragma unroll
    for (int ct = 0; ct < 4; ++ct) {
      tb[0][ct] = sbase + (unsigned)((8 * hh + q4) * 256 + (((4 * ct + 2 * blk + (p4 >> 1)) ^ (q4 << 2)) * 16) + 8 * (p4 & 1));
      tb[1][ct] = tb[0][ct] + 65536u; tb[2][ct] = tb[0][ct] + 131072u;
    }
    ta[0] = sbase + 8192 + wave * 2048 + lane * 16; ta[1] = ta[0] + 65536u; ta[2] = ta[0] + 131072u;
    f32x16 acc[4];
#pragma unroll
    for (int ct = 0; ct < 4; ++ct)
#pragma unroll
      for (int i = 0; i < 16; ++i) acc[ct][i] = 0.f;
    constexpr int LA = 6;
    u32x2 bb[8][2];
    u32x4 af[3];
    unsigned swv[4];
    f32x4 ws4[4];
    auto issue_pair = [&](auto NN) {
      constexpr int n = NN, ks = n >> 2, ct = n & 3;
      constexpr int so = ((ks >> 1) % G1_R) * 16384, seg = so >> 16, o = (so & 65535) + (ks & 1) * 4096;
      if constexpr (ct == 0) lds_read16<(so & 65535) + (ks & 1) * 1024>(af[ks % 3], ta[seg]);
      tr_read<o>(bb[n & 7][0], tb[seg][ct]);
      tr_read<o + 1024>(bb[n & 7][1], tb[seg][ct]);
    };
    __builtin_amdgcn_s_barrier();
    STAMP(KID, 5);
    static_for<0, LA>([&](auto NN) { issue_pair(NN); });
    static_for<0, 128>([&](auto NN) {
      constexpr int n = NN, ks = n >> 2, ct = n & 3;
      constexpr int last = (n + LA - 1 < 127) ? (n + LA - 1) : 127;
      constexpr int younger = 2 * (last - n) + ((last >> 2) - (n >> 2));
      if constexpr (ct == 0) wait_lgkm3<younger>(bb[n & 7][0], bb[n & 7][1], af[ks % 3]);
      else wait_lgkm2<younger>(bb[n & 7][0], bb[n & 7][1]);
      const u32x4 bu = {bb[n & 7][0][0], bb[n & 7][0][1], bb[n & 7][1][0], bb[n & 7][1][1]};
      acc[ct] = __builtin_amdgcn_mfma_f32_32x32x16_bf16(__builtin_bit_cast(bf16x8, af[ks % 3]), __builtin_bit_cast(bf16x8, bu), acc[ct], 0, 0, 0);
      __builtin_amdgcn_sched_barrier(0);
      if constexpr (n + LA < 128) {
        if constexpr ((n + LA) % 8 == 0) __builtin_amdgcn_s_barrier();
        issue_pair(std::integral_constant<int, n + LA>{});
      }
      if constexpr (n >= 104 && n < 108)
        swv[n - 104] = __hip_atomic_load(sw + mt * 128 + (n - 104) * 32 + r32, __ATOMIC_RELAXED, __HIP_MEMORY_SCOPE_AGENT);
      __builtin_amdgcn_sched_barrier(0);
    });
    STAMP(KID, 6);
    for (int it = 0; it < (1 << 20); ++it) {
      if (__all(swv[0] != 0u && swv[1] != 0u && swv[2] != 0u && swv[3] != 0u)) break;
      __builtin_amdgcn_s_sleep(1);
#pragma unroll
      for (int ct = 0; ct < 4; ++ct)
        swv[ct] = __hip_atomic_load(sw + mt * 128 + ct * 32 + r32, __ATOMIC_RELAXED, __HIP_MEMORY_SCOPE_AGENT);
    }
    asm volatile("" : "+v"(swv[0]), "+v"(swv[1]), "+v"(swv[2]), "+v"(swv[3]));
#pragma unroll
    for (int q = 0; q < 4; ++q) ws4[q] = *(const f32x4*)(smem + 9 * 16384 + 256 + (wave * 32 + 8 * q + 4 * hh) * 4);
#pragma unroll
    for (int ct = 0; ct < 4; ++ct) {
      const float rs = __builtin_amdgcn_rcpf(bf_hi(swv[ct]));
      const float cm = -bf_lo(swv[ct]) * rs;
      char* dst = hbuf + ((size_t)((mt * 4 + ct) * 112 + ft * 2) * 64 + lane) * 16;
      const f32x2 c2 = {cm, cm}, r2 = {rs, rs};
#pragma unroll
      for (int s = 0; s < 2; ++s) {
        u32x4 o;
#pragma unroll
        for (int j = 0; j < 4; ++j) {
          const int reg = 8 * s + 2 * j;
          const f32x2 wsum = {ws4[reg >> 2][reg & 3], ws4[reg >> 2][(reg & 3) + 1]};
          const f32x2 a2 = {acc[ct][reg], acc[ct][reg + 1]};
          const f32x2 v2 = __builtin_elementwise_fma(a2, r2, c2 * wsum);
          const s16x2 pv = __builtin_bit_cast(s16x2, __builtin_convertvector(v2, bf16x2));
          const s16x2 z = {0, 0};
          o[j] = __builtin_bit_cast(unsigned, __builtin_elementwise_max(pv, z));
        }
        st16_sc1(dst + s * 1024, o);
      }
    }
    STAMP(KID, 7);
    wait_vm<0>();
    if (lane == 0) {
      const unsigned old = __hip_atomic_fetch_add(lcnt, 1u, __ATOMIC_RELAXED, __HIP_MEMORY_SCOPE_WORKGROUP);
      if (old == 3u) __hip_atomic_store(flags + (mt * 16 + nt) * FLS, 1u, __ATOMIC_RELAXED, __HIP_MEMORY_SCOPE_AGENT);
    }
    STAMP(KID, 8);
    static_for<0, 8>([&](auto J) { pf_xr(J); });
  }
  pin7(wr); pin7(wr + 7); pin7(wr + 14); pin7(wr + 21);
  {
    unsigned x0_ = xr[0], x1_ = xr[1], x2_ = xr[2], x3_ = xr[3], x4_ = xr[4], x5_ = xr[5], x6_ = xr[6], x7_ = xr[7];
    asm volatile("" : "+v"(x0_), "+v"(x1_), "+v"(x2_), "+v"(x3_), "+v"(x4_), "+v"(x5_), "+v"(x6_), "+v"(x7_));
    xr[0] = x0_; xr[1] = x1_; xr[2] = x2_; xr[3] = x3_; xr[4] = x4_; xr[5] = x5_; xr[6] = x6_; xr[7] = x7_;
  }
  __builtin_amdgcn_s_barrier();
  if (wave8 == 7) {
    const unsigned* fp = flags + ((mt2 >> 1) * 16 + (lane < 14 ? lane : 0)) * FLS;
    for (int it = 0; it < (1 << 20); ++it) {
      const unsigned f = __hip_atomic_load(fp, __ATOMIC_RELAXED, __HIP_MEMORY_SCOPE_AGENT);
      if (__all(f != 0u)) break;
      __builtin_amdgcn_s_sleep(POLL_SLEEP);
    }
  }
  asm volatile("s_waitcnt lgkmcnt(0)" ::: "memory");
  __builtin_amdgcn_s_barrier();
  STAMP(KID, 9);
  const char* hsrc = hbuf + (size_t)(mt2 * 2) * 112 * 1024 + wave8 * 1024 + lane * 16;
  char* dstb = smem + wave8 * 2048;
  auto issue2 = [&](int c) {
#pragma unroll
    for (int mi = 0; mi < 2; ++mi)
      __builtin_amdgcn_global_load_lds((const unsigned*)(hsrc + (size_t)mi * 112 * 1024 + c * 8192),
                                       (LAS unsigned*)(dstb + (c % G2_R) * 16384 + mi * 1024), 16, 0, 16  );
  };
  f32x16 acc[2];
#pragma unroll
  for (int i = 0; i < 16; ++i) { acc[0][i] = 0.f; acc[1][i] = 0.f; }
  const char* rdh = smem + kq * 4096 + lane * 16;
  bf16x8 hf[2][4];
#pragma unroll
  for (int c = 0; c < g2_iss(0); ++c) issue2(c);
  static_for<0, 15>([&](auto C) {
    constexpr int c = C;
    auto mma = [&](auto JJ) {
      constexpr int j = JJ;
      if constexpr (c >= 1) {
        constexpr int pc = c - 1;
#pragma unroll
        for (int mi = 0; mi < 2; ++mi)
          acc[mi] = __builtin_amdgcn_mfma_f32_32x32x16_bf16(__builtin_bit_cast(bf16x8, wr[2 * pc + j]), hf[pc & 1][j * 2 + mi], acc[mi], 0, 0, 0);
      }
    };
    mma(std::integral_constant<int, 0>{});
    __builtin_amdgcn_sched_barrier(0);
    if constexpr (c < 14) {
      wait_vm_lgkm0<2 * (g2_iss(c) - (c + 1))>();
      __builtin_amdgcn_s_barrier();
      if constexpr (c == 0) STAMP(KID, 10);
#pragma unroll
      for (int e = 0; e < 4; ++e) hf[c & 1][e] = *(const bf16x8*)(rdh + (c % G2_R) * 16384 + e * 1024);
    }
    __builtin_amdgcn_sched_barrier(0);
    mma(std::integral_constant<int, 1>{});
    __builtin_amdgcn_sched_barrier(0);
    if constexpr (c < 14) {
#pragma unroll
      for (int cn = g2_iss(c); cn < g2_iss(c + 1); ++cn) issue2(cn);
    }
    __builtin_amdgcn_sched_barrier(0);
  });
  STAMP(KID, 11);
  char* red = smem + 5 * 16384;
  {
    char* dst = red + (ni * 4 + kq * 8) * 0 + (ni * 16 + kq) * 2048 + lane * 16;
#pragma unroll
    for (int pc = 0; pc < 4; ++pc) {
      f32x4 g0, g1;
#pragma unroll
      for (int i = 0; i < 4; ++i) { g0[i] = acc[pc >> 1][8 * (pc & 1) + i]; g1[i] = acc[pc >> 1][8 * (pc & 1) + 4 + i]; }
      *(f32x4*)(dst + pc * 8192) = g0;
      *(f32x4*)(dst + pc * 8192 + 1024) = g1;
    }
  }
  asm volatile("s_waitcnt lgkmcnt(0)" ::: "memory");
  __builtin_amdgcn_s_barrier();
  float mine[8];
  {
    const char* src = red + (ni * 16 + kq * 4) * 2048 + lane * 16;
    f32x4 o[8];
#pragma unroll
    for (int sl = 0; sl < 8; ++sl) o[sl] = *(const f32x4*)(src + sl * 1024);
    asm volatile("" : "+v"(o[0]), "+v"(o[1]), "+v"(o[2]), "+v"(o[3]), "+v"(o[4]), "+v"(o[5]), "+v"(o[6]), "+v"(o[7]));
    const f32x4 t0 = (o[0] + o[2]) + (o[4] + o[6]), t1 = (o[1] + o[3]) + (o[5] + o[7]);
#pragma unroll
    for (int i = 0; i < 4; ++i) { mine[i] = t0[i]; mine[4 + i] = t1[i]; }
  }
  float xo[8];
  float s1 = 0.f, s2 = 0.f;
#pragma unroll
  for (int i = 0; i < 8; ++i) {
    const float v = rbf(__uint_as_float(((unsigned)xr[i]) << 16) + rbf(mine[i]));
    xo[i] = v;
    s1 += v; s2 += v * v;
  }
  if (LAYER == 1) {
#pragma unroll
    for (int q = 0; q < 2; ++q)
      *(u32x2*)(p.out + (size_t)tok * D_ + dbase + 8 * q) =
          u32x2{pk_bf16(xo[4 * q], xo[4 * q + 1]), pk_bf16(xo[4 * q + 2], xo[4 * q + 3])};
  } else {
    unsigned short* xout = (unsigned short*)(ws + WS_XT2);
#pragma unroll
    for (int i = 0; i < 8; ++i)
      __hip_atomic_store(xout + (size_t)(dbase + (i & 3) + 8 * (i >> 2)) * L_ + tok,
                         (unsigned short)(__float_as_uint(xo[i]) >> 16), __ATOMIC_RELAXED, __HIP_MEMORY_SCOPE_AGENT);
    s1 += __shfl_xor(s1, 32, 64);
    s2 += __shfl_xor(s2, 32, 64);
    float* sred = (float*)smem;
    sred[wave8 * 64 + lane] = hh ? s2 : s1;
    asm volatile("s_waitcnt lgkmcnt(0)" ::: "memory");
    __builtin_amdgcn_s_barrier();
    if (wave8 < 2) {
      const int w0 = wave8 * 4;
      const float t = sred[w0 * 64 + lane] + sred[(w0 + 1) * 64 + lane] + sred[(w0 + 2) * 64 + lane] + sred[(w0 + 3) * 64 + lane];
      (void)__hip_atomic_fetch_add((float*)(ws + WS_ST1) + 2 * (mt2 * 64 + wave8 * 32 + r32) + hh, t, __ATOMIC_RELAXED, __HIP_MEMORY_SCOPE_AGENT);
    }
    wait_vm_lgkm0<0>();
    __builtin_amdgcn_s_barrier();
    if (tid < CREP) (void)__hip_atomic_fetch_add(eflags + (tid * 16 + (blockIdx.x & 15)) * 32, 1u, __ATOMIC_RELAXED, __HIP_MEMORY_SCOPE_AGENT);
  }
  STAMP(KID, 15);
  }
}

extern "C" void kernel_launch(void* const* d_in, const int* in_sizes, int n_in,
                              void* d_out, int out_size, void* d_ws, size_t ws_size,
                              hipStream_t stream) {
  const unsigned short* x0 = (const unsigned short*)d_in[0];
  const unsigned short* logA = (const unsigned short*)d_in[1];
  const unsigned short* Aim = (const unsigned short*)d_in[2];
  const unsigned short* Cre = (const unsigned short*)d_in[3];
  const unsigned short* Cim = (const unsigned short*)d_in[4];
  const unsigned short* logdt = (const unsigned short*)d_in[5];
  const unsigned short* dskip = (const unsigned short*)d_in[6];
  const unsigned short* w1 = (const unsigned short*)d_in[8];
  const unsigned short* w2 = (const unsigned short*)d_in[9];
  char* ws = (char*)d_ws;
  (void)hipFuncSetAttribute((const void*)s4d_layer_kernel, hipFuncAttributeMaxDynamicSharedMemorySize, LAYER_LDS);
  prep_kernel<<<PREP_BLOCKS, 256, 0, stream>>>(x0, logA, Aim, Cre, Cim, logdt, w1, w2, ws, (const char*)d_out);
  LayerArgs a0 = {dskip, (unsigned short*)d_out, ws};
  s4d_layer_kernel<<<256, 512, LAYER_LDS, stream>>>(a0);
}
```

```cpp
#include <hip/hip_runtime.h>
#include <hip/hip_bf16.h>
#include <stdint.h>

typedef __attribute__((ext_vector_type(8))) __bf16 bf16x8;
typedef __attribute__((ext_vector_type(2))) __bf16 bf16x2;
typedef __attribute__((ext_vector_type(4))) short s16x4;
typedef __attribute__((ext_vector_type(2))) short s16x2;
typedef __attribute__((ext_vector_type(8))) short s16x8;
typedef __attribute__((ext_vector_type(8))) _Float16 f16x8;
typedef __attribute__((ext_vector_type(2))) _Float16 f16x2;
typedef __attribute__((ext_vector_type(2))) float f32x2;
typedef __attribute__((ext_vector_type(4))) float f32x4;
typedef __attribute__((ext_vector_type(16))) float f32x16;
typedef __attribute__((ext_vector_type(4))) unsigned u32x4;
typedef __attribute__((ext_vector_type(2))) unsigned u32x2;
#define LAS __attribute__((address_space(3)))
#define DI __device__ __forceinline__

constexpr int L_ = 2048, D_ = 512, N_ = 128, F_ = 1792;

constexpr size_t MiB = 1024 * 1024;
constexpr size_t WS_G    = 0;
constexpr size_t WS_XT0  = 2 * MiB;
constexpr size_t WS_XT1  = 4 * MiB;
constexpr size_t WS_XT2  = 6 * MiB;
constexpr size_t WS_XT3  = 8 * MiB;
constexpr size_t WS_W1T  = 10 * MiB;
constexpr size_t WS_W2T  = 12 * MiB;
constexpr size_t WS_H    = 14 * MiB;
constexpr size_t WS_H2   = 24 * MiB;
constexpr size_t WS_KINV = 22 * MiB;
constexpr size_t WS_W1S  = WS_KINV + 4096;
constexpr size_t WS_Z0   = 23 * MiB;
constexpr size_t WS_ST1  = WS_Z0;
constexpr size_t WS_SW   = WS_ST1 + 16384;
#define FLS 32
#define CREP 8
#define CFS (3 * CREP)
constexpr size_t WS_FL   = WS_SW + 16384;
constexpr size_t WS_CF   = WS_FL + 2048 * FLS;
constexpr size_t WS_ZBYTES = 16384 + 16384 + 2048 * FLS + 2048 * CFS;
constexpr size_t WS_SW0  = WS_Z0 + 512 * 1024;

#define STAMP(kid, i)
#define STAMPT(kid, i, T)
DI float bf_lo(unsigned u) { return __uint_as_float(u << 16); }
DI float bf_hi(unsigned u) { return __uint_as_float(u & 0xffff0000u); }
DI unsigned pk_bf16(float a, float b) {
  f32x2 v = {a, b};
  return __builtin_bit_cast(unsigned, __builtin_convertvector(v, bf16x2));
}
DI unsigned pk_f16(float a, float b) {
  f32x2 v = {a, b};
  return __builtin_bit_cast(unsigned, __builtin_convertvector(v, f16x2));
}
DI float rbf(float a) { return bf_lo(pk_bf16(a, 0.f)); }
DI float ld_bf(const unsigned short* p) { return __uint_as_float(((unsigned)*p) << 16); }
DI unsigned perm_lo(unsigned hi, unsigned lo) { return __builtin_amdgcn_perm(hi, lo, 0x05040100u); }
DI unsigned perm_hi(unsigned hi, unsigned lo) { return __builtin_amdgcn_perm(hi, lo, 0x07060302u); }
template <int CTRL> DI float dpp_add(float v) {
  return v + __uint_as_float(__builtin_amdgcn_update_dpp(0u, __float_as_uint(v), CTRL, 0xf, 0xf, true));
}
DI float wave_sum(float v) {
  v = dpp_add<0xB1>(v);
  v = dpp_add<0x4E>(v);
  v = dpp_add<0x141>(v);
  v = dpp_add<0x140>(v);
  const unsigned u = __float_as_uint(v);
  return (__uint_as_float(__builtin_amdgcn_readlane(u, 0)) + __uint_as_float(__builtin_amdgcn_readlane(u, 16))) +
         (__uint_as_float(__builtin_amdgcn_readlane(u, 32)) + __uint_as_float(__builtin_amdgcn_readlane(u, 48)));
}
#define ST_FLAGS ""
DI void st16(void* p, u32x4 v) { asm volatile("global_store_dwordx4 %0, %1, off " ST_FLAGS "\n\ts_nop 1" ::"v"(p), "v"(v) : "memory"); }
DI void st8(void* p, u32x2 v) { asm volatile("global_store_dwordx2 %0, %1, off " ST_FLAGS "\n\ts_nop 1" ::"v"(p), "v"(v) : "memory"); }
DI void st2(void* p, unsigned v) { asm volatile("global_store_short %0, %1, off " ST_FLAGS "\n\ts_nop 1" ::"v"(p), "v"(v) : "memory"); }
template <int N> DI void wait_vm() { asm volatile("s_waitcnt vmcnt(%0)" ::"n"(N) : "memory"); }
template <int OFF> DI void gload16(u32x4& dst, const char* p) {
  asm volatile("global_load_dwordx4 %0, %1, off offset:%2" : "=v"(dst) : "v"(p), "n"(OFF));
}
template <int N> DI void wait_vm4(u32x4& a, u32x4& b, u32x4& c, u32x4& d) {
  asm volatile("s_waitcnt vmcnt(%4)" : "+v"(a), "+v"(b), "+v"(c), "+v"(d) : "n"(N) : "memory");
}
template <int OFF> DI void tr_read(u32x2& d, unsigned addr) {
  asm volatile("ds_read_b64_tr_b16 %0, %1 offset:%2" : "=v"(d) : "v"(addr), "n"(OFF));
}
template <int N> DI void wait_lgkm8(u32x2* b) {
  asm volatile("s_waitcnt lgkmcnt(%8)"
               : "+v"(b[0]), "+v"(b[1]), "+v"(b[2]), "+v"(b[3]), "+v"(b[4]), "+v"(b[5]), "+v"(b[6]), "+v"(b[7])
               : "n"(N) : "memory");
}
template <int N> DI void wait_vm_lgkm0() { asm volatile("s_waitcnt vmcnt(%0) lgkmcnt(0)" ::"n"(N) : "memory"); }
template <int I, int E, class F> DI void static_for(F&& f) {
  if constexpr (I < E) { f(std::integral_constant<int, I>{}); static_for<I + 1, E>(f); }
}

constexpr int TROW = 528;

__host__ __device__ constexpr int tab_w(int idx) {
  return idx < 16 ? idx : (idx < 40 ? ((8 + ((idx - 16) & 7)) << (1 + ((idx - 16) >> 3))) : 128);
}
__host__ __device__ constexpr int ilog2c(int w) { return w <= 1 ? 0 : 1 + ilog2c(w >> 1); }
__host__ __device__ constexpr int tab_idx(int w) {
  return w < 16 ? w : (w == 128 ? 40 : 16 + 8 * (ilog2c(w) - 4) + ((w >> (ilog2c(w) - 3)) - 8));
}
DI int tab_idx_rt(int w) {
  if (w < 16) return w;
  int lg = 31 - __clz(w);
  return 16 + 8 * (lg - 4) + ((w >> (lg - 3)) - 8);
}

DI void retile_load(int u, const unsigned short* __restrict__ w1, const unsigned short* __restrict__ w2, u32x2* r, size_t& dst) {
  if (u < 28672) {
    const int fg = u % 448, o = u / 448;
    const int f = 4 * fg;
#pragma unroll
    for (int j = 0; j < 8; ++j) r[j] = *(const u32x2*)(w1 + (size_t)(8 * o + j) * F_ + f);
    dst = WS_W1T + ((size_t)(((f >> 5) * 32 + (o >> 1)) * 64 + (o & 1) * 32 + (f & 31))) * 16;
  } else {
    const int v = u - 28672;
    const int dg = v & 127, o = v >> 7;
    const int dd = 4 * dg, ks = o >> 1, hh = o & 1;
#pragma unroll
    for (int j = 0; j < 8; ++j)
      r[j] = *(const u32x2*)(w2 + (size_t)(16 * ks + 8 * (j >> 2) + 4 * hh + (j & 3)) * D_ + dd);
    dst = WS_W2T + ((size_t)(((dd >> 5) * 112 + ks) * 64 + hh * 32 + (dd & 31))) * 16;
  }
}
DI void st16_sc1(void* p, u32x4 v) { asm volatile("global_store_dwordx4 %0, %1, off sc1\n\ts_nop 1" ::"v"(p), "v"(v) : "memory"); }
DI void st8_sc1(void* p, u32x2 v) { asm volatile("global_store_dwordx2 %0, %1, off sc1\n\ts_nop 1" ::"v"(p), "v"(v) : "memory"); }
template <int K> DI unsigned quad_bcast(unsigned v) { return __builtin_amdgcn_update_dpp(0u, v, K * 0x55, 0xf, 0xf, true); }
DI void retile_store(const u32x2* r, char* ws, size_t dst, int lane, bool active) {
  u32x4 pc[4];
#pragma unroll
  for (int c = 0; c < 2; ++c) {
    pc[2 * c] = u32x4{perm_lo(r[1][c], r[0][c]), perm_lo(r[3][c], r[2][c]), perm_lo(r[5][c], r[4][c]), perm_lo(r[7][c], r[6][c])};
    pc[2 * c + 1] = u32x4{perm_hi(r[1][c], r[0][c]), perm_hi(r[3][c], r[2][c]), perm_hi(r[5][c], r[4][c]), perm_hi(r[7][c], r[6][c])};
  }
  const int ql = lane & 3;
  char* base = ws + dst - (size_t)ql * 64 + ql * 16;
  static_for<0, 4>([&](auto KK) {
    constexpr int k = KK;
    u32x4 o;
#pragma unroll
    for (int e = 0; e < 4; ++e) {
      const unsigned v0 = quad_bcast<k>(pc[0][e]), v1 = quad_bcast<k>(pc[1][e]), v2 = quad_bcast<k>(pc[2][e]), v3 = quad_bcast<k>(pc[3][e]);
      o[e] = ql == 0 ? v0 : (ql == 1 ? v1 : (ql == 2 ? v2 : v3));
    }
    if (active) st16_sc1(base + k * 64, o);
  });
}

#define LAYER_CODE 25600
#define PREP_CODE 14336
template <int BYTES> DI unsigned warm_code(const char* pc, int t) {
  const int o = t * 128;
  return *(const __attribute__((address_space(1))) unsigned*)(uintptr_t)(pc + (o < BYTES ? o : 0));
}
constexpr int PB_KGEN = 512, PB_XT = 64, PB_ST = 64, PB_RT = 112, PB_WS = 16;
constexpr int PREP_BLOCKS = PB_KGEN + PB_XT + PB_ST + PB_RT + PB_WS;

__global__ __launch_bounds__(256) void prep_kernel(
    const unsigned short* __restrict__ x0, const unsigned short* __restrict__ logA,
    const unsigned short* __restrict__ Aim, const unsigned short* __restrict__ Cre,
    const unsigned short* __restrict__ Cim, const unsigned short* __restrict__ logdt,
    const unsigned short* __restrict__ w1, const unsigned short* __restrict__ w2, char* __restrict__ ws) {
  __shared__ __attribute__((aligned(16))) char smem[82 * TROW + 1536];
  float* red = (float*)smem;
  const int tid = threadIdx.x, lane = tid & 63;
  const int wave = __builtin_amdgcn_readfirstlane(tid >> 6);
  int b = blockIdx.x;
  STAMP(0, 0);
  unsigned cw1 = 0u;
  if (b >= PB_KGEN && b < PB_KGEN + PB_XT) cw1 = warm_code<PREP_CODE>((const char*)__builtin_amdgcn_s_getpc(), tid);
  if (b < PB_KGEN) {
    const int d = ((b & 7) * 32 + ((b >> 3) & 31)) * 2 + (b >> 8);
    const int isH = wave >> 1;
    const int n = (wave & 1) * 64 + lane;
    const float pdt = ld_bf(logdt + d), pla = ld_bf(logA + d * N_ + n), pai = ld_bf(Aim + d * N_ + n);
    const float p0r = ld_bf(Cre + d * N_ + lane), p0i = ld_bf(Cim + d * N_ + lane);
    const float p1r = ld_bf(Cre + d * N_ + 64 + lane), p1i = ld_bf(Cim + d * N_ + 64 + lane);
    const float dt = rbf(expf(pdt));
    const float ar = -rbf(expf(pla)) * dt;
    const float ai = pai * (isH ? -dt : dt);
    const float c0r = p0r * dt, c0i = p0i * dt, c1r = p1r * dt, c1i = p1i * dt;
    const float S = wave_sum(2.f * (fabsf(c0r) + fabsf(c0i) + fabsf(c1r) + fabsf(c1i)));
    int ex = 0;
    (void)frexpf(S, &ex);
    if (!(S > 0.f) || !(S < 3.0e38f)) ex = 15;
    ex = max(-100, min(ex, 120));
    const float scale = ldexpf(1.f, 15 - ex);
    const float cr = (wave & 1) ? c1r : c0r, ci = (wave & 1) ? c1i : c0i;
    const float base = isH ? 16.f : 1.f;
    const float er = ar * 1.44269504088896340736f * base;
    const float IH = 0.15915494309189533577f;
    const float IL = (float)(0.15915494309189533577 - (double)0.15915494309189533577f);
    float fh = ai * IH;
    float fl = fmaf(ai, IH, -fh) + ai * IL;
    fh *= base; fl *= base;
    STAMP(0, 1);
    f32x2 Er2[8], Ei2[8];
#pragma unroll
    for (int k = 0; k < 8; ++k) {
      const float pw = (float)(1 << k);
      const float mag = __builtin_amdgcn_exp2f(er * pw);
      const float ph = __builtin_amdgcn_fractf(fh * pw) + fl * pw;
      const float re = mag * __builtin_amdgcn_cosf(ph), im = mag * __builtin_amdgcn_sinf(ph);
      Er2[k] = f32x2{re, re};
      Ei2[k] = f32x2{-im, im};
    }
    f32x2 tv[41];
    tv[0] = isH ? f32x2{2.f * scale * cr, -2.f * scale * ci} : f32x2{1.f, 0.f};
    char* trow = smem + isH * (41 * TROW) + n * 4;
    *(unsigned*)trow = __builtin_bit_cast(unsigned, __builtin_convertvector(tv[0], f16x2));
    static_for<1, 41>([&](auto I) {
      constexpr int idx = I;
      constexpr int w = tab_w(idx);
      constexpr int lg = ilog2c(w);
      constexpr int ridx = tab_idx(w - (1 << lg));
      const f32x2 sw = __builtin_shufflevector(tv[ridx], tv[ridx], 1, 0);
      tv[idx] = __builtin_elementwise_fma(Ei2[lg], sw, Er2[lg] * tv[ridx]);
      *(unsigned*)(trow + idx * TROW) = __builtin_bit_cast(unsigned, __builtin_convertvector(tv[idx], f16x2));
    });
    int cix[8];
    {
      const int l0 = 2040 - tid * 8;
      const int sh = max((31 - __clz(l0 | 1)) - 7, 0);
      const int one = sh > 0 ? 1 : 0;
#pragma unroll
      for (int e = 0; e < 8; ++e) {
        const int l = l0 + 7 - e;
        cix[e] = 128 * sh + ((l + ((1 << sh) >> 1) - one + ((l >> sh) & one)) >> sh);
      }
    }
    STAMP(0, 2);
    __syncthreads();
    _Float16* kc = (_Float16*)(smem + 82 * TROW);
    {
      const int s = wave, i16 = lane & 15, g = lane >> 4;
      const int hw = (s == 0) ? i16 : (i16 < 8 ? ((8 + i16) << s) : 128);
      const char* arow = smem + 41 * TROW + tab_idx_rt(hw) * TROW + g * 16;
      const char* brow = smem + tab_idx_rt(i16 << s) * TROW + g * 16;
      f16x8 af[8], bf[8];
#pragma unroll
      for (int ks = 0; ks < 8; ++ks) { af[ks] = *(const f16x8*)(arow + ks * 64); bf[ks] = *(const f16x8*)(brow + ks * 64); }
      f32x4 k0 = {0.f, 0.f, 0.f, 0.f}, k1 = {0.f, 0.f, 0.f, 0.f};
#pragma unroll
      for (int ks = 0; ks < 8; ks += 2) {
        k0 = __builtin_amdgcn_mfma_f32_16x16x32_f16(af[ks], bf[ks], k0, 0, 0, 0);
        k1 = __builtin_amdgcn_mfma_f32_16x16x32_f16(af[ks + 1], bf[ks + 1], k1, 0, 0, 0);
      }
#pragma unroll
      for (int r = 0; r < 4; ++r) {
        const int i = 4 * g + r;
        const int cidx = (s == 0) ? (16 * i + i16) : (256 + 128 * (s - 1) + 16 * i + i16);
        const bool ok = (s == 0) || (i < 8) || (s == 3 && i == 8 && i16 == 0);
        if (ok) kc[cidx] = (_Float16)(k0[r] + k1[r]);
      }
    }
    __syncthreads();
    STAMP(0, 3);
    unsigned short o[8];
#pragma unroll
    for (int e = 0; e < 8; ++e) o[e] = ((const unsigned short*)kc)[cix[e]];
    *(u32x4*)(ws + WS_G + (size_t)d * 4096 + tid * 16) =
        u32x4{(unsigned)o[0] | ((unsigned)o[1] << 16), (unsigned)o[2] | ((unsigned)o[3] << 16),
              (unsigned)o[4] | ((unsigned)o[5] << 16), (unsigned)o[6] | ((unsigned)o[7] << 16)};
    if (tid == 0) ((float*)(ws + WS_KINV))[d] = ldexpf(1.f, ex - 15);
    STAMP(0, 15);
    return;
  }
  b -= PB_KGEN;
  if (b < PB_XT) {
    unsigned long long lpc;
    asm volatile("s_getpc_b64 s[40:41]\n\ts_add_u32 s40, s40, s4d_layer_kernel@rel32@lo+4\n\t"
                 "s_addc_u32 s41, s41, s4d_layer_kernel@rel32@hi+12\n\ts_mov_b64 %0, s[40:41]"
                 : "=s"(lpc) :: "scc", "s40", "s41");
    const unsigned cw2 = warm_code<LAYER_CODE>((const char*)lpc, tid);
    const int tile = b * 4 + wave, tt = tile >> 3, ct = tile & 7;
    const int tg = lane & 7, cg = lane >> 3;
    const unsigned short* src = x0 + (size_t)(tt * 64 + tg * 8) * D_ + ct * 64 + cg * 8;
    u32x4 r[8];
#pragma unroll
    for (int j = 0; j < 8; ++j) r[j] = *(const u32x4*)(src + (size_t)j * D_);
    unsigned short* dst = (unsigned short*)(ws + WS_XT0) + (size_t)(ct * 64 + cg * 8) * L_ + tt * 64 + tg * 8;
#pragma unroll
    for (int c = 0; c < 4; ++c) {
      u32x4 lo = {perm_lo(r[1][c], r[0][c]), perm_lo(r[3][c], r[2][c]), perm_lo(r[5][c], r[4][c]), perm_lo(r[7][c], r[6][c])};
      u32x4 hi = {perm_hi(r[1][c], r[0][c]), perm_hi(r[3][c], r[2][c]), perm_hi(r[5][c], r[4][c]), perm_hi(r[7][c], r[6][c])};
      st16_sc1(dst + (size_t)(2 * c) * L_, lo);
      st16_sc1(dst + (size_t)(2 * c + 1) * L_, hi);
    }
    asm volatile("" ::"v"(cw1), "v"(cw2));
    STAMP(0, 15);
    return;
  }
  b -= PB_XT;
  if (b < PB_ST) {
    const int t0 = b * 32 + wave * 8;
    u32x4 r[8];
#pragma unroll
    for (int j = 0; j < 8; ++j) r[j] = *(const u32x4*)(x0 + (size_t)(t0 + j) * D_ + lane * 8);
    for (int i = b * 256 + tid; i < (int)(WS_ZBYTES / 16); i += PB_ST * 256) ((u32x4*)(ws + WS_Z0))[i] = u32x4{0u, 0u, 0u, 0u};
    asm volatile("" : "+v"(r[0]), "+v"(r[1]), "+v"(r[2]), "+v"(r[3]), "+v"(r[4]), "+v"(r[5]), "+v"(r[6]), "+v"(r[7]));
    unsigned word = 0u;
#pragma unroll
    for (int j = 0; j < 8; ++j) {
      float s = 0.f;
#pragma unroll
      for (int c = 0; c < 4; ++c) s += bf_lo(r[j][c]) + bf_hi(r[j][c]);
      const float mean = wave_sum(s) * (1.f / D_);
      float q = 0.f;
#pragma unroll
      for (int c = 0; c < 4; ++c) {
        const float a = bf_lo(r[j][c]) - mean, bb = bf_hi(r[j][c]) - mean;
        q += a * a + bb * bb;
      }
      const float var = wave_sum(q) * (1.f / D_);
      const unsigned wv = pk_bf16(mean, sqrtf(rbf(rbf(var) + 1e-5f)));
      if (lane == j) word = wv;
    }
    if (lane < 8) ((unsigned*)(ws + WS_SW0))[t0 + lane] = word;
    STAMP(0, 15);
    return;
  }
  b -= PB_ST;
  if (b < PB_RT) {
    u32x2 r0[8], r1[8];
    size_t d0_, d1_;
    retile_load(b * 512 + tid, w1, w2, r0, d0_);
    retile_load(b * 512 + 256 + tid, w1, w2, r1, d1_);
    asm volatile("" : "+v"(r1[0]), "+v"(r1[1]), "+v"(r1[2]), "+v"(r1[3]), "+v"(r1[4]), "+v"(r1[5]), "+v"(r1[6]), "+v"(r1[7]));
    retile_store(r0, ws, d0_, lane, true);
    retile_store(r1, ws, d1_, lane, true);
    STAMP(0, 15);
    return;
  }
  b -= PB_RT;
  {
    const int cg = tid % 14, rg = tid / 14;
    const int f0 = b * 112 + cg * 8;
    float s[8] = {0.f, 0.f, 0.f, 0.f, 0.f, 0.f, 0.f, 0.f};
    if (tid < 224) {
#pragma unroll
      for (int h = 0; h < 2; ++h) {
        u32x4 v[16];
#pragma unroll
        for (int j = 0; j < 16; ++j) v[j] = *(const u32x4*)(w1 + (size_t)(rg * 32 + h * 16 + j) * F_ + f0);
#pragma unroll
        for (int j = 0; j < 16; ++j)
#pragma unroll
          for (int c = 0; c < 4; ++c) { s[2 * c] += bf_lo(v[j][c]); s[2 * c + 1] += bf_hi(v[j][c]); }
      }
#pragma unroll
      for (int e = 0; e < 8; ++e) red[rg * 112 + cg * 8 + e] = s[e];
    }
    __syncthreads();
    if (tid < 112) {
      float t = 0.f;
#pragma unroll
      for (int r = 0; r < 16; ++r) t += red[r * 112 + tid];
      ((float*)(ws + WS_W1S))[b * 112 + tid] = t;
    }
    STAMP(0, 15);
  }
}

constexpr int CV_CS = 260 * 16;
constexpr int CV_KCH = 8 * CV_CS;
constexpr int CV_U0 = 2 * CV_KCH;
constexpr int CV_UCH = (256 + 2048) * 2;
constexpr int CV_XS = CV_U0 + 2 * CV_UCH;
constexpr int CV_RED = CV_XS + 2 * 8192;
constexpr int G1_R = 9;
constexpr int G2_R = 9;
constexpr int LAYER_LDS = 9 * 16384 + 1024;
static_assert(CV_RED + 16384 <= 9 * 16384, "conv LDS");
#define G1_W0 5
#define G2_W0 6
#define CV_PF1 16
#define CV_PF2 22
#define W1PRE 5
#define POLL_SLEEP 2
static_assert(G1_W0 <= G1_R - 2 && W1PRE <= G1_R, "ring safety");
__host__ __device__ constexpr int g2_iss(int i) {
  int v = G2_W0 + i;
  return v < 14 ? v : 14;
}
__host__ __device__ constexpr int g1_young(int c) {
  int n = 0;
  for (int j = c + 1; j < c + G1_W0 && j < 16; ++j) n += (j >= W1PRE) ? 4 : 2;
  return n;
}
template <int N> DI void wait_lgkm2(u32x2& a, u32x2& b) {
  asm volatile("s_waitcnt lgkmcnt(%2)" : "+v"(a), "+v"(b) : "n"(N) : "memory");
}
template <int N> DI void wait_lgkm3(u32x2& a, u32x2& b, u32x4& c) {
  asm volatile("s_waitcnt lgkmcnt(%3)" : "+v"(a), "+v"(b), "+v"(c) : "n"(N) : "memory");
}
template <int OFF> DI void lds_read16(u32x4& d, unsigned addr) {
  asm volatile("ds_read_b128 %0, %1 offset:%2" : "=v"(d) : "v"(addr), "n"(OFF));
}
DI void pin4(bf16x8* f) { asm volatile("" : "+v"(f[0]), "+v"(f[1]), "+v"(f[2]), "+v"(f[3])); }
DI void pin7(u32x4* f) { asm volatile("" : "+v"(f[0]), "+v"(f[1]), "+v"(f[2]), "+v"(f[3]), "+v"(f[4]), "+v"(f[5]), "+v"(f[6])); }
DI u32x4 ld16_sc1_wait(const void* p) {
  u32x4 v;
  asm volatile("global_load_dwordx4 %0, %1, off sc1\n\ts_waitcnt vmcnt(0)" : "=&v"(v) : "v"(p) : "memory");
  return v;
}

struct LayerArgs {
  const unsigned short* dskip;
  unsigned short* out;
  char* ws;
};

extern "C" __global__ __launch_bounds__(512, 2) void s4d_layer_kernel(LayerArgs p) {
  const unsigned cw = warm_code<LAYER_CODE>((const char*)__builtin_amdgcn_s_getpc(), threadIdx.x);
  extern __shared__ __attribute__((aligned(16))) char smem[];
  u32x4 wr[28];
#pragma unroll 1
  for (int LAYER = 0; LAYER < 2; ++LAYER) {
  int tid = threadIdx.x;
  asm volatile("" : "+v"(tid));
  const int lane = tid & 63;
  const int wave8 = __builtin_amdgcn_readfirstlane(tid >> 6);
  const int wave = wave8 & 3;
  const int bx = blockIdx.x & 7, bi = blockIdx.x >> 3;
  const int r32 = lane & 31, hh = lane >> 5;
  char* ws = p.ws;
  const char* w1t = ws + WS_W1T;
  const char* w2t = ws + WS_W2T;
  const int mt2 = bx * 4 + (bi & 3), nt2 = bi >> 2;
  const int ni = wave8 & 1, kq = wave8 >> 1;
  const int mi_f = kq >> 1, kh_f = kq & 1;
  const int tok = mt2 * 64 + mi_f * 32 + r32;
  const int dbase = nt2 * 64 + ni * 32 + 4 * hh + 16 * kh_f;
  unsigned short xr[8];
  const char* w2src = w2t + (size_t)(nt2 * 2 + ni) * 112 * 1024 + kq * 2048 + lane * 16;
  auto pf_w2 = [&](auto J) {
    constexpr int j = J;
    wr[j] = *(const u32x4*)(w2src + (j >> 1) * 8192 + (j & 1) * 1024);
  };
  const int mt = (bx >> 1) * 4 + (bi & 3), nt = (bx & 1) * 7 + (bi >> 2);
  const int ft = nt * 4 + wave;
  const int KID = 1 + 3 * LAYER;
  const unsigned short* xin = (const unsigned short*)(ws + (LAYER ? WS_XT2 : WS_XT0));
  unsigned short* xt = (unsigned short*)(ws + (LAYER ? WS_XT3 : WS_XT1));
  char* hbuf = ws + (LAYER ? WS_H2 : WS_H);
  unsigned* sw = (unsigned*)(ws + WS_SW) + LAYER * 2048;
  unsigned* flags = (unsigned*)(ws + WS_FL) + LAYER * 256 * FLS;
  unsigned* cflags = (unsigned*)(ws + WS_CF) + LAYER * (CREP * 16 * 32);
  unsigned* eflags = (unsigned*)(ws + WS_CF) + 2 * (CREP * 16 * 32);
  const unsigned short* xrsrc = xt + ((size_t)(tok >> 7) * D_ + dbase) * 128 + (tok & 127);
  auto pf_xr = [&](auto J) {
    constexpr int j = J;
    xr[j] = __hip_atomic_load(xrsrc + ((j & 3) + 8 * (j >> 2)) * 128, __ATOMIC_RELAXED, __HIP_MEMORY_SCOPE_AGENT);
  };
  STAMP(KID, 0);
  {
    const int d0 = 2 * (bx * 32 + bi);
    const int gz = tid & 255, gc = tid >> 8;
    const int t4 = tid * 4;
    const int ch = wave8 >> 2, half = wave8 & 1, qs = (wave8 >> 1) & 1;
    float xf[2][4], m[4], sd[4];
    const unsigned dsk2 = *(const unsigned*)(p.dskip + d0);
    const char* gp = ws + WS_G + (size_t)(d0 + gc) * 4096 + gz * 16;
    const u32x4 ga = *(const u32x4*)gp;
    const u32x4 gb = *(const u32x4*)(gp + 16);
    const float kscale = ((const float*)(ws + WS_KINV))[d0 + ch];
    u32x4 swv0 = {0u, 0u, 0u, 0u};
    u32x2 xv0 = {0u, 0u}, xv1 = {0u, 0u};
    if (LAYER == 0) {
      swv0 = *(const u32x4*)(ws + WS_SW0 + t4 * 4);
      xv0 = *(const u32x2*)(xin + (size_t)d0 * L_ + t4);
      xv1 = *(const u32x2*)(xin + (size_t)(d0 + 1) * L_ + t4);
    }
    unsigned zz = 0u;
    asm volatile("" : "+v"(zz));
    if (tid < 16) *(u32x4*)(smem + (tid >> 3) * CV_KCH + (tid & 7) * CV_CS + 256 * 16) = u32x4{zz, zz, zz, zz};
    if (tid >= 64 && tid < 128) {
      const int q = tid - 64;
      *(u32x4*)(smem + CV_U0 + (q >> 5) * CV_UCH + (q & 31) * 16) = u32x4{zz, zz, zz, zz};
    }
    if (tid == 128) *(unsigned*)(smem + 9 * 16384) = 0u;
    {
      const unsigned msk = (gz < 255) ? 0xffffffffu : 0u;
      unsigned w[8] = {ga[0], ga[1], ga[2], ga[3], gb[0] & msk, gb[1] & msk, gb[2] & msk, gb[3] & msk};
      unsigned o[7];
#pragma unroll
      for (int k = 0; k < 7; ++k) o[k] = __builtin_amdgcn_alignbit(w[k + 1], w[k], 16);
      char* dst = smem + gc * CV_KCH + gz * 16;
#pragma unroll
      for (int mm = 0; mm < 4; ++mm) {
        *(u32x4*)(dst + (2 * mm) * CV_CS) = u32x4{w[mm], w[mm + 1], w[mm + 2], w[mm + 3]};
        *(u32x4*)(dst + (2 * mm + 1) * CV_CS) = u32x4{o[mm], o[mm + 1], o[mm + 2], o[mm + 3]};
      }
    }
    f32x4 st0 = {0.f, 0.f, 0.f, 0.f}, st1 = {0.f, 0.f, 0.f, 0.f};
    if (LAYER != 0) {
      if (wave8 == 7) {
        const unsigned* cp = eflags + ((blockIdx.x % CREP) * 16 + (lane & 15)) * 32;
        for (int it = 0; it < (1 << 20); ++it) {
          const unsigned f = __hip_atomic_load(cp, __ATOMIC_RELAXED, __HIP_MEMORY_SCOPE_AGENT);
          if (__all(f >= 16u)) break;
          __builtin_amdgcn_s_sleep(POLL_SLEEP);
        }
      }
      __builtin_amdgcn_s_barrier();
      STAMP(KID, 12);
      const auto rs_st = __builtin_amdgcn_make_buffer_rsrc((void*)(ws + WS_ST1), 0, 16384, 0x00020000);
      const auto rs_x = __builtin_amdgcn_make_buffer_rsrc((void*)xin, 0, L_ * D_ * 2, 0x00020000);
      st0 = __builtin_bit_cast(f32x4, __builtin_amdgcn_raw_buffer_load_b128(rs_st, t4 * 8, 0, 16  ));
      st1 = __builtin_bit_cast(f32x4, __builtin_amdgcn_raw_buffer_load_b128(rs_st, t4 * 8 + 16, 0, 16));
      xv0 = __builtin_amdgcn_raw_buffer_load_b64(rs_x, (d0 * L_ + t4) * 2, 0, 16);
      xv1 = __builtin_amdgcn_raw_buffer_load_b64(rs_x, ((d0 + 1) * L_ + t4) * 2, 0, 16);
    }
#pragma unroll
    for (int r = 0; r < 4; ++r) {
      {
        const unsigned wv = swv0[r];
        const float a = (r < 2) ? st0[(r & 1) * 2] : st1[(r & 1) * 2];
        const float bq = (r < 2) ? st0[(r & 1) * 2 + 1] : st1[(r & 1) * 2 + 1];
        const float mean = a * (1.f / D_), var = fmaxf(bq * (1.f / D_) - mean * mean, 0.f);
        const float m1 = rbf(mean), sd1 = rbf(__builtin_amdgcn_sqrtf(rbf(rbf(var) + 1e-5f)));
        m[r] = LAYER ? m1 : bf_lo(wv);
        sd[r] = LAYER ? sd1 : bf_hi(wv);
      }
      const unsigned w0 = xv0[r >> 1], w1_ = xv1[r >> 1];
      xf[0][r] = (r & 1) ? bf_hi(w0) : bf_lo(w0);
      xf[1][r] = (r & 1) ? bf_hi(w1_) : bf_lo(w1_);
    }
    {
      float rs[4];
#pragma unroll
      for (int r = 0; r < 4; ++r) rs[r] = __builtin_amdgcn_rcpf(sd[r]);
#pragma unroll
      for (int c = 0; c < 2; ++c) {
        const float dsk = c ? bf_hi(dsk2) : bf_lo(dsk2);
        float uu[4];
        unsigned xs[4];
#pragma unroll
        for (int r = 0; r < 4; ++r) {
          const float x = xf[c][r];
          const float dd = rbf(x - m[r]);
          float q = dd * rs[r];
          q = fmaf(fmaf(-q, sd[r], dd), rs[r], q);
          uu[r] = rbf(q);
          xs[r] = pk_bf16(x, uu[r] * dsk);
        }
        *(u32x2*)(smem + CV_U0 + c * CV_UCH + (256 + t4) * 2) = u32x2{pk_f16(uu[0], uu[1]), pk_f16(uu[2], uu[3])};
        *(u32x4*)(smem + CV_XS + c * 8192 + t4 * 4) = u32x4{xs[0], xs[1], xs[2], xs[3]};
      }
    }
    asm volatile("" ::"v"(cw));
    __syncthreads();
    STAMP(KID, 1);
    const int j16 = lane & 15, g = lane >> 4;
    const int cc = (7 - j16) & 7;
    const int zl = (2031 - j16 - cc) / 8 + g;
    const char* abase = smem + ch * CV_KCH + cc * CV_CS + (zl - 32 * 7 - 4 * (4 * half + 3)) * 16;
    const char* bbase = smem + CV_U0 + ch * CV_UCH + 480 + 32 * j16 + 16 * g - 64 * (4 * half + 3);
    f32x4 acc[4];
#pragma unroll
    for (int q = 0; q < 4; ++q) acc[q] = f32x4{0.f, 0.f, 0.f, 0.f};
    auto body = [&](auto QS, auto PFF) {
      constexpr int S = QS;
      constexpr bool PF = PFF;
      constexpr int Q[4] = {S ? 6 : 7, S ? 5 : 4, S ? 2 : 3, S ? 1 : 0};
      constexpr int NF = S ? 7 : 8;
      f16x8 A[2][NF], B[2][NF];
      auto ld = [&](auto IT) {
        constexpr int it = IT;
#pragma unroll
        for (int k = 0; k < NF; ++k) {
          A[it & 1][k] = *(const f16x8*)(abase + (7 - k) * 512 + (3 - it) * 64);
          B[it & 1][NF - 1 - k] = *(const f16x8*)(bbase + (NF - 1 - k) * 512 + (3 - it) * 64);
        }
      };
      ld(std::integral_constant<int, 0>{});
      __builtin_amdgcn_sched_barrier(0);
      static_for<0, 4>([&](auto IT) {
        constexpr int it = IT;
        if constexpr (it + 1 < 4) ld(std::integral_constant<int, it + 1>{});
        if constexpr (PF) static_for<it * (CV_PF1 / 4), (it + 1) * (CV_PF1 / 4)>([&](auto J) { pf_w2(J); });
#pragma unroll
        for (int b1 = 0; b1 < NF; ++b1)
#pragma unroll
          for (int e = 0; e < 4; ++e)
            if (b1 <= Q[e]) acc[e] = __builtin_amdgcn_mfma_f32_16x16x32_f16(A[it & 1][b1], B[it & 1][Q[e] - b1], acc[e], 0, 0, 0);
#pragma unroll
        for (int k = 0; k < 18; ++k) {
          __builtin_amdgcn_sched_group_barrier(0x8, 1, 0);
          if (it + 1 < 4 && k >= 2 && k - 2 < 2 * NF) __builtin_amdgcn_sched_group_barrier(0x100, 1, 0);
          if (PF && CV_PF1 > 0 && (k % (16 / (CV_PF1 / 4))) == 1 && k / (16 / (CV_PF1 / 4)) < CV_PF1 / 4)
            __builtin_amdgcn_sched_group_barrier(0x20, 1, 0);
        }
        __builtin_amdgcn_sched_barrier(0);
      });
    };
    if (LAYER == 0) {
      if (qs) body(std::integral_constant<int, 1>{}, std::true_type{}); else body(std::integral_constant<int, 0>{}, std::true_type{});
    } else {
      if (qs) body(std::integral_constant<int, 1>{}, std::false_type{}); else body(std::integral_constant<int, 0>{}, std::false_type{});
    }
    f32x4 keep[2];
    {
      char* wrp = smem + CV_RED + wave8 * 2048 + lane * 16;
#pragma unroll
      for (int e = 0; e < 2; ++e) {
        f32x4 give;
#pragma unroll
        for (int r = 0; r < 4; ++r) {
          const float lo = acc[e][r], hi = acc[2 + e][r];
          give[r] = half ? lo : hi;
          keep[e][r] = half ? hi : lo;
        }
        *(f32x4*)(wrp + e * 1024) = give;
      }
    }
    asm volatile("s_waitcnt lgkmcnt(0)" ::: "memory");
    __builtin_amdgcn_s_barrier();
    STAMP(KID, 2);
    {
      const char* rd = smem + CV_RED + (wave8 ^ 1) * 2048 + lane * 16;
      const int toff = 16 * j16 + 4 * g;
#pragma unroll
      for (int e = 0; e < 2; ++e) {
        const int q = half ? (e ? (qs ? 1 : 0) : (qs ? 2 : 3)) : (e ? (qs ? 5 : 4) : (qs ? 6 : 7));
        const int t = 256 * q + toff;
        const f32x4 o = *(const f32x4*)(rd + e * 1024);
        const u32x4 xs = *(const u32x4*)(smem + CV_XS + ch * 8192 + t * 4);
        const f32x4 y = keep[e] + o;
        float xo[4];
#pragma unroll
        for (int r = 0; r < 4; ++r) {
          const unsigned w = xs[r];
          xo[r] = bf_lo(w) + rbf(rbf(y[r] * kscale) + bf_hi(w));
        }
        st8_sc1(xt + ((size_t)(t >> 7) * D_ + d0 + ch) * 128 + (t & 127), u32x2{pk_bf16(xo[0], xo[1]), pk_bf16(xo[2], xo[3])});
      }
    }
    wait_vm_lgkm0<0>();
    __builtin_amdgcn_s_barrier();
    if (tid < CREP) (void)__hip_atomic_fetch_add(cflags + (tid * 16 + (d0 >> 5)) * 32, 1u, __ATOMIC_RELAXED, __HIP_MEMORY_SCOPE_AGENT);
    STAMP(KID, 3);
  }
  const int grow = lane >> 4, gslot = lane & 15;
  const unsigned short* xsrc = xt + ((size_t)mt * D_ + wave * 8 + grow) * 128 + ((gslot ^ (grow << 2)) * 8);
  const char* wsrc = w1t + (size_t)ft * 32 * 1024 + lane * 16;
  auto issue_w = [&](int c) {
    char* slot = smem + (c % G1_R) * 16384;
#pragma unroll
    for (int i = 0; i < 2; ++i)
      __builtin_amdgcn_global_load_lds((const unsigned*)(wsrc + (size_t)(c * 2 + i) * 1024),
                                       (LAS unsigned*)(slot + 8192 + (wave * 2 + i) * 1024), 16, 0, 0);
  };
  auto issue_x = [&](int c) {
    char* slot = smem + (c % G1_R) * 16384;
#pragma unroll
    for (int i = 0; i < 2; ++i)
      __builtin_amdgcn_global_load_lds((const unsigned*)(xsrc + (size_t)(c * 32 + i * 4) * 128),
                                       (LAS unsigned*)(slot + (wave * 8 + i * 4) * 256), 16, 0, 16  );
  };
  if (bi < 28 && wave8 >= 4) {
    if (wave8 < 6)
      __builtin_amdgcn_global_load_lds((const unsigned*)(ws + WS_W1S) + nt * 128 + wave * 64 + lane,
                                       (LAS unsigned*)(smem + 9 * 16384 + 256 + wave * 256), 4, 0, 0);
#pragma unroll
    for (int c = 0; c < W1PRE; ++c) issue_w(c);
  }
  __builtin_amdgcn_sched_barrier(0);
  if (LAYER == 0) static_for<CV_PF1, 28>([&](auto J) { pf_w2(J); });
  __builtin_amdgcn_sched_barrier(0);
  if (wave8 == 7) {
    const unsigned* cp = cflags + ((blockIdx.x % CREP) * 16 + (lane & 15)) * 32;
    for (int it = 0; it < (1 << 20); ++it) {
      const unsigned f = __hip_atomic_load(cp, __ATOMIC_RELAXED, __HIP_MEMORY_SCOPE_AGENT);
      if (__all(f >= 16u)) break;
      __builtin_amdgcn_s_sleep(POLL_SLEEP);
    }
  }
  __builtin_amdgcn_s_barrier();
  STAMP(KID, 4);
  if (bi >= 28) {
    const int ht = bx * 4 + (bi - 28);
    const int cgp = wave8 * 2 + hh;
    const auto rsrc = __builtin_amdgcn_make_buffer_rsrc((void*)xt, 0, L_ * D_ * 2, 0x00020000);
    const int off0 = ((ht >> 1) * D_ * 128 + (ht & 1) * 64 + 2 * r32) * 2;
    const unsigned pv = __builtin_amdgcn_raw_buffer_load_b32(rsrc, off0, 0, 16  );
    unsigned v[32];
#pragma unroll
    for (int j = 0; j < 32; ++j) v[j] = __builtin_amdgcn_raw_buffer_load_b32(rsrc, off0 + (cgp * 32 + j) * 256, 0, 16  );
    static_for<0, 8>([&](auto J) { pf_xr(J); });
    const f32x2 piv = {bf_lo(pv), bf_hi(pv)};
    f32x2 s1 = {0.f, 0.f}, s2 = {0.f, 0.f};
#pragma unroll
    for (int j = 0; j < 32; ++j) {
      const f32x2 dd = f32x2{bf_lo(v[j]), bf_hi(v[j])} - piv;
      s1 += dd;
      s2 = __builtin_elementwise_fma(dd, dd, s2);
    }
    f32x4* red = (f32x4*)smem;
    red[cgp * 32 + r32] = f32x4{s1[0], s2[0], s1[1], s2[1]};
    asm volatile("s_waitcnt lgkmcnt(0)" ::: "memory");
    __builtin_amdgcn_s_barrier();
    if (tid < 64) {
      const f32x2* r2 = (const f32x2*)smem;
      float a = 0.f, q = 0.f;
#pragma unroll
      for (int w = 0; w < 16; ++w) { const f32x2 t = r2[w * 64 + tid]; a += t[0]; q += t[1]; }
      const unsigned pw = __shfl(pv, tid >> 1, 64);
      const float dm = a * (1.f / D_);
      const float var = fmaxf(q * (1.f / D_) - dm * dm, 0.f);
      const float mm = ((tid & 1) ? bf_hi(pw) : bf_lo(pw)) + dm;
      const float sdv = __builtin_amdgcn_sqrtf(rbf(rbf(var) + 1e-5f));
      __hip_atomic_store(sw + ht * 64 + tid, pk_bf16(mm, sdv), __ATOMIC_RELAXED, __HIP_MEMORY_SCOPE_AGENT);
    }
    STAMP(KID, 12);
  } else if (wave8 >= 4) {
#pragma unroll
    for (int c = 0; c < G1_W0; ++c) { issue_x(c); if (c >= W1PRE) issue_w(c); }
    static_for<0, 16>([&](auto C) {
      constexpr int c = C;
      constexpr int extra = (c + G1_W0 > 16) ? 8 : 0;
      wait_vm<g1_young(c) + extra>();
      __builtin_amdgcn_s_barrier();
      if constexpr (c + G1_W0 < 16) { issue_x(c + G1_W0); if (c + G1_W0 >= W1PRE) issue_w(c + G1_W0); }
      if constexpr (c + G1_W0 == 16) {
        __builtin_amdgcn_sched_barrier(0);
        static_for<0, 8>([&](auto J) { pf_xr(J); });
        __builtin_amdgcn_sched_barrier(0);
      }
    });
  } else {
    const int blk = (lane >> 4) & 1, q4 = (lane & 15) >> 2, p4 = lane & 3;
    const unsigned sbase = (unsigned)(uintptr_t)(LAS char*)smem;
    unsigned* lcnt = (unsigned*)(smem + 9 * 16384);
    unsigned tb[3][4], ta[3];
#pragma unroll
    for (int ct = 0; ct < 4; ++ct) {
      tb[0][ct] = sbase + (unsigned)((8 * hh + q4) * 256 + (((4 * ct + 2 * blk + (p4 >> 1)) ^ (q4 << 2)) * 16) + 8 * (p4 & 1));
      tb[1][ct] = tb[0][ct] + 65536u; tb[2][ct] = tb[0][ct] + 131072u;
    }
    ta[0] = sbase + 8192 + wave * 2048 + lane * 16; ta[1] = ta[0] + 65536u; ta[2] = ta[0] + 131072u;
    f32x16 acc[4];
#pragma unroll
    for (int ct = 0; ct < 4; ++ct)
#pragma unroll
      for (int i = 0; i < 16; ++i) acc[ct][i] = 0.f;
    constexpr int LA = 6;
    u32x2 bb[8][2];
    u32x4 af[3];
    unsigned swv[4];
    f32x4 ws4[4];
    auto issue_pair = [&](auto NN) {
      constexpr int n = NN, ks = n >> 2, ct = n & 3;
      constexpr int so = ((ks >> 1) % G1_R) * 16384, seg = so >> 16, o = (so & 65535) + (ks & 1) * 4096;
      if constexpr (ct == 0) lds_read16<(so & 65535) + (ks & 1) * 1024>(af[ks % 3], ta[seg]);
      tr_read<o>(bb[n & 7][0], tb[seg][ct]);
      tr_read<o + 1024>(bb[n & 7][1], tb[seg][ct]);
    };
    __builtin_amdgcn_s_barrier();
    STAMP(KID, 5);
    static_for<0, LA>([&](auto NN) { issue_pair(NN); });
    static_for<0, 128>([&](auto NN) {
      constexpr int n = NN, ks = n >> 2, ct = n & 3;
      constexpr int last = (n + LA - 1 < 127) ? (n + LA - 1) : 127;
      constexpr int younger = 2 * (last - n) + ((last >> 2) - (n >> 2));
      if constexpr (ct == 0) wait_lgkm3<younger>(bb[n & 7][0], bb[n & 7][1], af[ks % 3]);
      else wait_lgkm2<younger>(bb[n & 7][0], bb[n & 7][1]);
      const u32x4 bu = {bb[n & 7][0][0], bb[n & 7][0][1], bb[n & 7][1][0], bb[n & 7][1][1]};
      acc[ct] = __builtin_amdgcn_mfma_f32_32x32x16_bf16(__builtin_bit_cast(bf16x8, af[ks % 3]), __builtin_bit_cast(bf16x8, bu), acc[ct], 0, 0, 0);
      __builtin_amdgcn_sched_barrier(0);
      if constexpr (n + LA < 128) {
        if constexpr ((n + LA) % 8 == 0) __builtin_amdgcn_s_barrier();
        issue_pair(std::integral_constant<int, n + LA>{});
      }
      if constexpr (n >= 104 && n < 108)
        swv[n - 104] = __hip_atomic_load(sw + mt * 128 + (n - 104) * 32 + r32, __ATOMIC_RELAXED, __HIP_MEMORY_SCOPE_AGENT);
      __builtin_amdgcn_sched_barrier(0);
    });
    STAMP(KID, 6);
    for (int it = 0; it < (1 << 20); ++it) {
      if (__all(swv[0] != 0u && swv[1] != 0u && swv[2] != 0u && swv[3] != 0u)) break;
      __builtin_amdgcn_s_sleep(1);
#pragma unroll
      for (int ct = 0; ct < 4; ++ct)
        swv[ct] = __hip_atomic_load(sw + mt * 128 + ct * 32 + r32, __ATOMIC_RELAXED, __HIP_MEMORY_SCOPE_AGENT);
    }
    asm volatile("" : "+v"(swv[0]), "+v"(swv[1]), "+v"(swv[2]), "+v"(swv[3]));
#pragma unroll
    for (int q = 0; q < 4; ++q) ws4[q] = *(const f32x4*)(smem + 9 * 16384 + 256 + (wave * 32 + 8 * q + 4 * hh) * 4);
#pragma unroll
    for (int ct = 0; ct < 4; ++ct) {
      const float rs = __builtin_amdgcn_rcpf(bf_hi(swv[ct]));
      const float cm = -bf_lo(swv[ct]) * rs;
      char* dst = hbuf + ((size_t)((mt * 4 + ct) * 112 + ft * 2) * 64 + lane) * 16;
      const f32x2 c2 = {cm, cm}, r2 = {rs, rs};
#pragma unroll
      for (int s = 0; s < 2; ++s) {
        u32x4 o;
#pragma unroll
        for (int j = 0; j < 4; ++j) {
          const int reg = 8 * s + 2 * j;
          const f32x2 wsum = {ws4[reg >> 2][reg & 3], ws4[reg >> 2][(reg & 3) + 1]};
          const f32x2 a2 = {acc[ct][reg], acc[ct][reg + 1]};
          const f32x2 v2 = __builtin_elementwise_fma(a2, r2, c2 * wsum);
          const s16x2 pv = __builtin_bit_cast(s16x2, __builtin_convertvector(v2, bf16x2));
          const s16x2 z = {0, 0};
          o[j] = __builtin_bit_cast(unsigned, __builtin_elementwise_max(pv, z));
        }
        st16_sc1(dst + s * 1024, o);
      }
    }
    STAMP(KID, 7);
    wait_vm<0>();
    if (lane == 0) {
      const unsigned old = __hip_atomic_fetch_add(lcnt, 1u, __ATOMIC_RELAXED, __HIP_MEMORY_SCOPE_WORKGROUP);
      if (old == 3u) __hip_atomic_store(flags + (mt * 16 + nt) * FLS, 1u, __ATOMIC_RELAXED, __HIP_MEMORY_SCOPE_AGENT);
    }
    STAMP(KID, 8);
    static_for<0, 8>([&](auto J) { pf_xr(J); });
  }
  pin7(wr); pin7(wr + 7); pin7(wr + 14); pin7(wr + 21);
  {
    unsigned x0_ = xr[0], x1_ = xr[1], x2_ = xr[2], x3_ = xr[3], x4_ = xr[4], x5_ = xr[5], x6_ = xr[6], x7_ = xr[7];
    asm volatile("" : "+v"(x0_), "+v"(x1_), "+v"(x2_), "+v"(x3_), "+v"(x4_), "+v"(x5_), "+v"(x6_), "+v"(x7_));
    xr[0] = x0_; xr[1] = x1_; xr[2] = x2_; xr[3] = x3_; xr[4] = x4_; xr[5] = x5_; xr[6] = x6_; xr[7] = x7_;
  }
  __builtin_amdgcn_s_barrier();
  if (wave8 == 7) {
    const unsigned* fp = flags + ((mt2 >> 1) * 16 + (lane < 14 ? lane : 0)) * FLS;
    for (int it = 0; it < (1 << 20); ++it) {
      const unsigned f = __hip_atomic_load(fp, __ATOMIC_RELAXED, __HIP_MEMORY_SCOPE_AGENT);
      if (__all(f != 0u)) break;
      __builtin_amdgcn_s_sleep(POLL_SLEEP);
    }
  }
  asm volatile("s_waitcnt lgkmcnt(0)" ::: "memory");
  __builtin_amdgcn_s_barrier();
  STAMP(KID, 9);
  const char* hsrc = hbuf + (size_t)(mt2 * 2) * 112 * 1024 + wave8 * 1024 + lane * 16;
  char* dstb = smem + wave8 * 2048;
  auto issue2 = [&](int c) {
#pragma unroll
    for (int mi = 0; mi < 2; ++mi)
      __builtin_amdgcn_global_load_lds((const unsigned*)(hsrc + (size_t)mi * 112 * 1024 + c * 8192),
                                       (LAS unsigned*)(dstb + (c % G2_R) * 16384 + mi * 1024), 16, 0, 16  );
  };
  f32x16 acc[2];
#pragma unroll
  for (int i = 0; i < 16; ++i) { acc[0][i] = 0.f; acc[1][i] = 0.f; }
  const char* rdh = smem + kq * 4096 + lane * 16;
  bf16x8 hf[2][4];
#pragma unroll
  for (int c = 0; c < g2_iss(0); ++c) issue2(c);
  static_for<0, 15>([&](auto C) {
    constexpr int c = C;
    auto mma = [&](auto JJ) {
      constexpr int j = JJ;
      if constexpr (c >= 1) {
        constexpr int pc = c - 1;
#pragma unroll
        for (int mi = 0; mi < 2; ++mi)
          acc[mi] = __builtin_amdgcn_mfma_f32_32x32x16_bf16(__builtin_bit_cast(bf16x8, wr[2 * pc + j]), hf[pc & 1][j * 2 + mi], acc[mi], 0, 0, 0);
      }
    };
    mma(std::integral_constant<int, 0>{});
    __builtin_amdgcn_sched_barrier(0);
    if constexpr (c < 14) {
      wait_vm_lgkm0<2 * (g2_iss(c) - (c + 1))>();
      __builtin_amdgcn_s_barrier();
      if constexpr (c == 0) STAMP(KID, 10);
#pragma unroll
      for (int e = 0; e < 4; ++e) hf[c & 1][e] = *(const bf16x8*)(rdh + (c % G2_R) * 16384 + e * 1024);
    }
    __builtin_amdgcn_sched_barrier(0);
    mma(std::integral_constant<int, 1>{});
    __builtin_amdgcn_sched_barrier(0);
    if constexpr (c < 14) {
#pragma unroll
      for (int cn = g2_iss(c); cn < g2_iss(c + 1); ++cn) issue2(cn);
    }
    __builtin_amdgcn_sched_barrier(0);
  });
  STAMP(KID, 11);
  char* red = smem + 5 * 16384;
  {
    char* dst = red + (ni * 4 + kq * 8) * 0 + (ni * 16 + kq) * 2048 + lane * 16;
#pragma unroll
    for (int pc = 0; pc < 4; ++pc) {
      f32x4 g0, g1;
#pragma unroll
      for (int i = 0; i < 4; ++i) { g0[i] = acc[pc >> 1][8 * (pc & 1) + i]; g1[i] = acc[pc >> 1][8 * (pc & 1) + 4 + i]; }
      *(f32x4*)(dst + pc * 8192) = g0;
      *(f32x4*)(dst + pc * 8192 + 1024) = g1;
    }
  }
  asm volatile("s_waitcnt lgkmcnt(0)" ::: "memory");
  __builtin_amdgcn_s_barrier();
  float mine[8];
  {
    const char* src = red + (ni * 16 + kq * 4) * 2048 + lane * 16;
    f32x4 o[8];
#pragma unroll
    for (int sl = 0; sl < 8; ++sl) o[sl] = *(const f32x4*)(src + sl * 1024);
    asm volatile("" : "+v"(o[0]), "+v"(o[1]), "+v"(o[2]), "+v"(o[3]), "+v"(o[4]), "+v"(o[5]), "+v"(o[6]), "+v"(o[7]));
    const f32x4 t0 = (o[0] + o[2]) + (o[4] + o[6]), t1 = (o[1] + o[3]) + (o[5] + o[7]);
#pragma unroll
    for (int i = 0; i < 4; ++i) { mine[i] = t0[i]; mine[4 + i] = t1[i]; }
  }
  float xo[8];
  float s1 = 0.f, s2 = 0.f;
#pragma unroll
  for (int i = 0; i < 8; ++i) {
    const float v = rbf(__uint_as_float(((unsigned)xr[i]) << 16) + rbf(mine[i]));
    xo[i] = v;
    s1 += v; s2 += v * v;
  }
  if (LAYER == 1) {
#pragma unroll
    for (int q = 0; q < 2; ++q)
      *(u32x2*)(p.out + (size_t)tok * D_ + dbase + 8 * q) =
          u32x2{pk_bf16(xo[4 * q], xo[4 * q + 1]), pk_bf16(xo[4 * q + 2], xo[4 * q + 3])};
  } else {
    unsigned short* xout = (unsigned short*)(ws + WS_XT2);
#pragma unroll
    for (int i = 0; i < 8; ++i)
      __hip_atomic_store(xout + (size_t)(dbase + (i & 3) + 8 * (i >> 2)) * L_ + tok,
                         (unsigned short)(__float_as_uint(xo[i]) >> 16), __ATOMIC_RELAXED, __HIP_MEMORY_SCOPE_AGENT);
    s1 += __shfl_xor(s1, 32, 64);
    s2 += __shfl_xor(s2, 32, 64);
    float* sred = (float*)smem;
    sred[wave8 * 64 + lane] = hh ? s2 : s1;
    asm volatile("s_waitcnt lgkmcnt(0)" ::: "memory");
    __builtin_amdgcn_s_barrier();
    if (wave8 < 2) {
      const int w0 = wave8 * 4;
      const float t = sred[w0 * 64 + lane] + sred[(w0 + 1) * 64 + lane] + sred[(w0 + 2) * 64 + lane] + sred[(w0 + 3) * 64 + lane];
      (void)__hip_atomic_fetch_add((float*)(ws + WS_ST1) + 2 * (mt2 * 64 + wave8 * 32 + r32) + hh, t, __ATOMIC_RELAXED, __HIP_MEMORY_SCOPE_AGENT);
    }
    wait_vm_lgkm0<0>();
    __builtin_amdgcn_s_barrier();
    if (tid < CREP) (void)__hip_atomic_fetch_add(eflags + (tid * 16 + (blockIdx.x & 15)) * 32, 1u, __ATOMIC_RELAXED, __HIP_MEMORY_SCOPE_AGENT);
  }
  STAMP(KID, 15);
  }
}

extern "C" void kernel_launch(void* const* d_in, const int* in_sizes, int n_in,
                              void* d_out, int out_size, void* d_ws, size_t ws_size,
                              hipStream_t stream) {
  const unsigned short* x0 = (const unsigned short*)d_in[0];
  const unsigned short* logA = (const unsigned short*)d_in[1];
  const unsigned short* Aim = (const unsigned short*)d_in[2];
  const unsigned short* Cre = (const unsigned short*)d_in[3];
  const unsigned short* Cim = (const unsigned short*)d_in[4];
  const unsigned short* logdt = (const unsigned short*)d_in[5];
  const unsigned short* dskip = (const unsigned short*)d_in[6];
  const unsigned short* w1 = (const unsigned short*)d_in[8];
  const unsigned short* w2 = (const unsigned short*)d_in[9];
  char* ws = (char*)d_ws;
  (void)hipFuncSetAttribute((const void*)s4d_layer_kernel, hipFuncAttributeMaxDynamicSharedMemorySize, LAYER_LDS);
  prep_kernel<<<PREP_BLOCKS, 256, 0, stream>>>(x0, logA, Aim, Cre, Cim, logdt, w1, w2, ws);
  LayerArgs a0 = {dskip, (unsigned short*)d_out, ws};
  s4d_layer_kernel<<<256, 512, LAYER_LDS, stream>>>(a0);
}
```

```cpp
#include <hip/hip_runtime.h>
#include <hip/hip_bf16.h>
#include <stdint.h>

typedef __attribute__((ext_vector_type(8))) __bf16 bf16x8;
typedef __attribute__((ext_vector_type(2))) __bf16 bf16x2;
typedef __attribute__((ext_vector_type(4))) short s16x4;
typedef __attribute__((ext_vector_type(2))) short s16x2;
typedef __attribute__((ext_vector_type(8))) short s16x8;
typedef __attribute__((ext_vector_type(8))) _Float16 f16x8;
typedef __attribute__((ext_vector_type(2))) _Float16 f16x2;
typedef __attribute__((ext_vector_type(2))) float f32x2;
typedef __attribute__((ext_vector_type(4))) float f32x4;
typedef __attribute__((ext_vector_type(16))) float f32x16;
typedef __attribute__((ext_vector_type(4))) unsigned u32x4;
typedef __attribute__((ext_vector_type(2))) unsigned u32x2;
#define LAS __attribute__((address_space(3)))
#define DI __device__ __forceinline__

constexpr int L_ = 2048, D_ = 512, N_ = 128, F_ = 1792;

constexpr size_t MiB = 1024 * 1024;
constexpr size_t WS_G    = 0;
constexpr size_t WS_XT0  = 2 * MiB;
constexpr size_t WS_XT1  = 4 * MiB;
constexpr size_t WS_XT2  = 6 * MiB;
constexpr size_t WS_XT3  = 8 * MiB;
constexpr size_t WS_W1T  = 10 * MiB;
constexpr size_t WS_W2T  = 12 * MiB;
constexpr size_t WS_H    = 14 * MiB;
constexpr size_t WS_H2   = 24 * MiB;
constexpr size_t WS_KINV = 22 * MiB;
constexpr size_t WS_W1S  = WS_KINV + 4096;
constexpr size_t WS_Z0   = 23 * MiB;
constexpr size_t WS_ST1  = WS_Z0;
constexpr size_t WS_SW   = WS_ST1 + 16384;
#define FLS 32
#define CREP 8
#define CFS (3 * CREP)
constexpr size_t WS_FL   = WS_SW + 16384;
constexpr size_t WS_CF   = WS_FL + 2048 * FLS;
constexpr size_t WS_ZBYTES = 16384 + 16384 + 2048 * FLS + 2048 * CFS;
constexpr size_t WS_SW0  = WS_Z0 + 512 * 1024;

DI float bf_lo(unsigned u) { return __uint_as_float(u << 16); }
DI float bf_hi(unsigned u) { return __uint_as_float(u & 0xffff0000u); }
DI unsigned pk_bf16(float a, float b) {
  f32x2 v = {a, b};
  return __builtin_bit_cast(unsigned, __builtin_convertvector(v, bf16x2));
}
DI unsigned pk_f16(float a, float b) {
  f32x2 v = {a, b};
  return __builtin_bit_cast(unsigned, __builtin_convertvector(v, f16x2));
}
DI float rbf(float a) { return bf_lo(pk_bf16(a, 0.f)); }
DI float ld_bf(const unsigned short* p) { return __uint_as_float(((unsigned)*p) << 16); }
DI unsigned perm_lo(unsigned hi, unsigned lo) { return __builtin_amdgcn_perm(hi, lo, 0x05040100u); }
DI unsigned perm_hi(unsigned hi, unsigned lo) { return __builtin_amdgcn_perm(hi, lo, 0x07060302u); }
template <int CTRL> DI float dpp_add(float v) {
  return v + __uint_as_float(__builtin_amdgcn_update_dpp(0u, __float_as_uint(v), CTRL, 0xf, 0xf, true));
}
DI float wave_sum(float v) {
  v = dpp_add<0xB1>(v);
  v = dpp_add<0x4E>(v);
  v = dpp_add<0x141>(v);
  v = dpp_add<0x140>(v);
  const unsigned u = __float_as_uint(v);
  return (__uint_as_float(__builtin_amdgcn_readlane(u, 0)) + __uint_as_float(__builtin_amdgcn_readlane(u, 16))) +
         (__uint_as_float(__builtin_amdgcn_readlane(u, 32)) + __uint_as_float(__builtin_amdgcn_readlane(u, 48)));
}
#define ST_FLAGS ""
DI void st16(void* p, u32x4 v) { asm volatile("global_store_dwordx4 %0, %1, off " ST_FLAGS "\n\ts_nop 1" ::"v"(p), "v"(v) : "memory"); }
DI void st8(void* p, u32x2 v) { asm volatile("global_store_dwordx2 %0, %1, off " ST_FLAGS "\n\ts_nop 1" ::"v"(p), "v"(v) : "memory"); }
DI void st2(void* p, unsigned v) { asm volatile("global_store_short %0, %1, off " ST_FLAGS "\n\ts_nop 1" ::"v"(p), "v"(v) : "memory"); }
template <int N> DI void wait_vm() { asm volatile("s_waitcnt vmcnt(%0)" ::"n"(N) : "memory"); }
template <int OFF> DI void gload16(u32x4& dst, const char* p) {
  asm volatile("global_load_dwordx4 %0, %1, off offset:%2" : "=v"(dst) : "v"(p), "n"(OFF));
}
template <int N> DI void wait_vm4(u32x4& a, u32x4& b, u32x4& c, u32x4& d) {
  asm volatile("s_waitcnt vmcnt(%4)" : "+v"(a), "+v"(b), "+v"(c), "+v"(d) : "n"(N) : "memory");
}
template <int OFF> DI void tr_read(u32x2& d, unsigned addr) {
  asm volatile("ds_read_b64_tr_b16 %0, %1 offset:%2" : "=v"(d) : "v"(addr), "n"(OFF));
}
template <int N> DI void wait_lgkm8(u32x2* b) {
  asm volatile("s_waitcnt lgkmcnt(%8)"
               : "+v"(b[0]), "+v"(b[1]), "+v"(b[2]), "+v"(b[3]), "+v"(b[4]), "+v"(b[5]), "+v"(b[6]), "+v"(b[7])
               : "n"(N) : "memory");
}
template <int N> DI void wait_vm_lgkm0() { asm volatile("s_waitcnt vmcnt(%0) lgkmcnt(0)" ::"n"(N) : "memory"); }
template <int I, int E, class F> DI void static_for(F&& f) {
  if constexpr (I < E) { f(std::integral_constant<int, I>{}); static_for<I + 1, E>(f); }
}

constexpr int TROW = 528;

__host__ __device__ constexpr int tab_w(int idx) {
  return idx < 16 ? idx : (idx < 40 ? ((8 + ((idx - 16) & 7)) << (1 + ((idx - 16) >> 3))) : 128);
}
__host__ __device__ constexpr int ilog2c(int w) { return w <= 1 ? 0 : 1 + ilog2c(w >> 1); }
__host__ __device__ constexpr int tab_idx(int w) {
  return w < 16 ? w : (w == 128 ? 40 : 16 + 8 * (ilog2c(w) - 4) + ((w >> (ilog2c(w) - 3)) - 8));
}
DI int tab_idx_rt(int w) {
  if (w < 16) return w;
  int lg = 31 - __clz(w);
  return 16 + 8 * (lg - 4) + ((w >> (lg - 3)) - 8);
}

DI void retile_load(int u, const unsigned short* __restrict__ w1, const unsigned short* __restrict__ w2, u32x2* r, size_t& dst) {
  if (u < 28672) {
    const int fg = u % 448, o = u / 448;
    const int f = 4 * fg;
#pragma unroll
    for (int j = 0; j < 8; ++j) r[j] = *(const u32x2*)(w1 + (size_t)(8 * o + j) * F_ + f);
    dst = WS_W1T + ((size_t)(((f >> 5) * 32 + (o >> 1)) * 64 + (o & 1) * 32 + (f & 31))) * 16;
  } else {
    const int v = u - 28672;
    const int dg = v & 127, o = v >> 7;
    const int dd = 4 * dg, ks = o >> 1, hh = o & 1;
#pragma unroll
    for (int j = 0; j < 8; ++j)
      r[j] = *(const u32x2*)(w2 + (size_t)(16 * ks + 8 * (j >> 2) + 4 * hh + (j & 3)) * D_ + dd);
    dst = WS_W2T + ((size_t)(((dd >> 5) * 112 + ks) * 64 + hh * 32 + (dd & 31))) * 16;
  }
}
DI void st16_sc1(void* p, u32x4 v) { asm volatile("global_store_dwordx4 %0, %1, off sc1\n\ts_nop 1" ::"v"(p), "v"(v) : "memory"); }
DI void st8_sc1(void* p, u32x2 v) { asm volatile("global_store_dwordx2 %0, %1, off sc1\n\ts_nop 1" ::"v"(p), "v"(v) : "memory"); }
template <int K> DI unsigned quad_bcast(unsigned v) { return __builtin_amdgcn_update_dpp(0u, v, K * 0x55, 0xf, 0xf, true); }
DI void retile_store(const u32x2* r, char* ws, size_t dst, int lane, bool active) {
  u32x4 pc[4];
#pragma unroll
  for (int c = 0; c < 2; ++c) {
    pc[2 * c] = u32x4{perm_lo(r[1][c], r[0][c]), perm_lo(r[3][c], r[2][c]), perm_lo(r[5][c], r[4][c]), perm_lo(r[7][c], r[6][c])};
    pc[2 * c + 1] = u32x4{perm_hi(r[1][c], r[0][c]), perm_hi(r[3][c], r[2][c]), perm_hi(r[5][c], r[4][c]), perm_hi(r[7][c], r[6][c])};
  }
  const int ql = lane & 3;
  char* base = ws + dst - (size_t)ql * 64 + ql * 16;
  static_for<0, 4>([&](auto KK) {
    constexpr int k = KK;
    u32x4 o;
#pragma unroll
    for (int e = 0; e < 4; ++e) {
      const unsigned v0 = quad_bcast<k>(pc[0][e]), v1 = quad_bcast<k>(pc[1][e]), v2 = quad_bcast<k>(pc[2][e]), v3 = quad_bcast<k>(pc[3][e]);
      o[e] = ql == 0 ? v0 : (ql == 1 ? v1 : (ql == 2 ? v2 : v3));
    }
    if (active) st16_sc1(base + k * 64, o);
  });
}

#define LAYER_CODE 24576
#define PREP_CODE 14336
template <int BYTES> DI unsigned warm_code(const char* pc, int t) {
  const int o = t * 128;
  return *(const __attribute__((address_space(1))) unsigned*)(uintptr_t)(pc + (o < BYTES ? o : 0));
}
#define KSQ 2
#define STT 8
#define XTT 2
constexpr int PB_KGEN = 512, PB_XT = 64 / XTT, PB_ST = 512 / STT, PB_RT = 112, PB_WS = 32;
constexpr int PREP_BLOCKS = PB_KGEN + PB_XT + PB_ST + PB_RT + PB_WS;

__global__ __launch_bounds__(256) void prep_kernel(
    const unsigned short* __restrict__ x0, const unsigned short* __restrict__ logA,
    const unsigned short* __restrict__ Aim, const unsigned short* __restrict__ Cre,
    const unsigned short* __restrict__ Cim, const unsigned short* __restrict__ logdt,
    const unsigned short* __restrict__ w1, const unsigned short* __restrict__ w2, char* __restrict__ ws) {
  __shared__ __attribute__((aligned(16))) char smem[82 * TROW + 1536];
  float* red = (float*)smem;
  const int tid = threadIdx.x, lane = tid & 63;
  const int wave = __builtin_amdgcn_readfirstlane(tid >> 6);
  int b = blockIdx.x;
  unsigned cw1 = 0u;
  if (b >= PB_KGEN && b < PB_KGEN + PB_XT) cw1 = warm_code<PREP_CODE>((const char*)__builtin_amdgcn_s_getpc(), tid);
  if (b < PB_KGEN) {
    const int d = ((b & 7) * 32 + ((b >> 3) & 31)) * 2 + (b >> 8);
    const int isH = wave >> 1;
    const int n = (wave & 1) * 64 + lane;
    const float pdt = ld_bf(logdt + d), pla = ld_bf(logA + d * N_ + n), pai = ld_bf(Aim + d * N_ + n);
    const float p0r = ld_bf(Cre + d * N_ + lane), p0i = ld_bf(Cim + d * N_ + lane);
    const float p1r = ld_bf(Cre + d * N_ + 64 + lane), p1i = ld_bf(Cim + d * N_ + 64 + lane);
    const float dt = rbf(expf(pdt));
    const float ar = -rbf(expf(pla)) * dt;
    const float ai = pai * (isH ? -dt : dt);
    const float c0r = p0r * dt, c0i = p0i * dt, c1r = p1r * dt, c1i = p1i * dt;
    const float S = wave_sum(2.f * (fabsf(c0r) + fabsf(c0i) + fabsf(c1r) + fabsf(c1i)));
    int ex = 0;
    (void)frexpf(S, &ex);
    if (!(S > 0.f) || !(S < 3.0e38f)) ex = 15;
    ex = max(-100, min(ex, 120));
    const float scale = ldexpf(1.f, 15 - ex);
    const float cr = (wave & 1) ? c1r : c0r, ci = (wave & 1) ? c1i : c0i;
    const float base = isH ? 16.f : 1.f;
    const float er = ar * 1.44269504088896340736f * base;
    const float IH = 0.15915494309189533577f;
    const float IL = (float)(0.15915494309189533577 - (double)0.15915494309189533577f);
    float fh = ai * IH;
    float fl = fmaf(ai, IH, -fh) + ai * IL;
    fh *= base; fl *= base;
    f32x2 Er2[8], Ei2[8];
#pragma unroll
    for (int k = 0; k < 8; k += KSQ) {
      const float pw = (float)(1 << k);
      const float mag = __builtin_amdgcn_exp2f(er * pw);
      const float ph = __builtin_amdgcn_fractf(fh * pw) + fl * pw;
      float re = mag * __builtin_amdgcn_cosf(ph), im = mag * __builtin_amdgcn_sinf(ph);
#pragma unroll
      for (int j = 0; j < KSQ; ++j) {
        Er2[k + j] = f32x2{re, re};
        Ei2[k + j] = f32x2{-im, im};
        const float r2 = fmaf(re, re, -im * im), i2 = (re + re) * im;
        re = r2; im = i2;
      }
    }
    f32x2 tv[41];
    tv[0] = isH ? f32x2{2.f * scale * cr, -2.f * scale * ci} : f32x2{1.f, 0.f};
    char* trow = smem + isH * (41 * TROW) + n * 4;
    *(unsigned*)trow = __builtin_bit_cast(unsigned, __builtin_convertvector(tv[0], f16x2));
    static_for<1, 41>([&](auto I) {
      constexpr int idx = I;
      constexpr int w = tab_w(idx);
      constexpr int lg = ilog2c(w);
      constexpr int ridx = tab_idx(w - (1 << lg));
      const f32x2 sw = __builtin_shufflevector(tv[ridx], tv[ridx], 1, 0);
      tv[idx] = __builtin_elementwise_fma(Ei2[lg], sw, Er2[lg] * tv[ridx]);
      *(unsigned*)(trow + idx * TROW) = __builtin_bit_cast(unsigned, __builtin_convertvector(tv[idx], f16x2));
    });
    int cix[8];
    {
      const int l0 = 2040 - tid * 8;
      const int sh = max((31 - __clz(l0 | 1)) - 7, 0);
      const int one = sh > 0 ? 1 : 0;
#pragma unroll
      for (int e = 0; e < 8; ++e) {
        const int l = l0 + 7 - e;
        cix[e] = 128 * sh + ((l + ((1 << sh) >> 1) - one + ((l >> sh) & one)) >> sh);
      }
    }
    __syncthreads();
    _Float16* kc = (_Float16*)(smem + 82 * TROW);
    {
      const int s = wave, i16 = lane & 15, g = lane >> 4;
      const int hw = (s == 0) ? i16 : (i16 < 8 ? ((8 + i16) << s) : 128);
      const char* arow = smem + 41 * TROW + tab_idx_rt(hw) * TROW + g * 16;
      const char* brow = smem + tab_idx_rt(i16 << s) * TROW + g * 16;
      f16x8 af[8], bf[8];
#pragma unroll
      for (int ks = 0; ks < 8; ++ks) { af[ks] = *(const f16x8*)(arow + ks * 64); bf[ks] = *(const f16x8*)(brow + ks * 64); }
      f32x4 k0 = {0.f, 0.f, 0.f, 0.f}, k1 = {0.f, 0.f, 0.f, 0.f};
#pragma unroll
      for (int ks = 0; ks < 8; ks += 2) {
        k0 = __builtin_amdgcn_mfma_f32_16x16x32_f16(af[ks], bf[ks], k0, 0, 0, 0);
        k1 = __builtin_amdgcn_mfma_f32_16x16x32_f16(af[ks + 1], bf[ks + 1], k1, 0, 0, 0);
      }
#pragma unroll
      for (int r = 0; r < 4; ++r) {
        const int i = 4 * g + r;
        const int cidx = (s == 0) ? (16 * i + i16) : (256 + 128 * (s - 1) + 16 * i + i16);
        const bool ok = (s == 0) || (i < 8) || (s == 3 && i == 8 && i16 == 0);
        if (ok) kc[cidx] = (_Float16)(k0[r] + k1[r]);
      }
    }
    __syncthreads();
    unsigned short o[8];
#pragma unroll
    for (int e = 0; e < 8; ++e) o[e] = ((const unsigned short*)kc)[cix[e]];
    *(u32x4*)(ws + WS_G + (size_t)d * 4096 + tid * 16) =
        u32x4{(unsigned)o[0] | ((unsigned)o[1] << 16), (unsigned)o[2] | ((unsigned)o[3] << 16),
              (unsigned)o[4] | ((unsigned)o[5] << 16), (unsigned)o[6] | ((unsigned)o[7] << 16)};
    if (tid == 0) ((float*)(ws + WS_KINV))[d] = ldexpf(1.f, ex - 15);
    return;
  }
  b -= PB_KGEN;
  if (b < PB_XT) {
    unsigned long long lpc;
    asm volatile("s_getpc_b64 s[40:41]\n\ts_add_u32 s40, s40, s4d_layer_kernel@rel32@lo+4\n\t"
                 "s_addc_u32 s41, s41, s4d_layer_kernel@rel32@hi+12\n\ts_mov_b64 %0, s[40:41]"
                 : "=s"(lpc) :: "scc", "s40", "s41");
    const unsigned cw2 = warm_code<LAYER_CODE>((const char*)lpc, tid);
    const int tg = lane & 7, cg = lane >> 3;
    u32x4 r[XTT][8];
#pragma unroll
    for (int i = 0; i < XTT; ++i) {
      const int tile = (b * 4 + wave) * XTT + i, tt = tile >> 3, ct = tile & 7;
      const unsigned short* src = x0 + (size_t)(tt * 64 + tg * 8) * D_ + ct * 64 + cg * 8;
#pragma unroll
      for (int j = 0; j < 8; ++j) r[i][j] = *(const u32x4*)(src + (size_t)j * D_);
    }
    asm volatile("" : "+v"(r[XTT - 1][0]), "+v"(r[XTT - 1][1]), "+v"(r[XTT - 1][2]), "+v"(r[XTT - 1][3]),
                      "+v"(r[XTT - 1][4]), "+v"(r[XTT - 1][5]), "+v"(r[XTT - 1][6]), "+v"(r[XTT - 1][7]));
#pragma unroll
    for (int i = 0; i < XTT; ++i) {
      const int tile = (b * 4 + wave) * XTT + i, tt = tile >> 3, ct = tile & 7;
      unsigned short* dst = (unsigned short*)(ws + WS_XT0) + (size_t)(ct * 64 + cg * 8) * L_ + tt * 64 + tg * 8;
#pragma unroll
      for (int c = 0; c < 4; ++c) {
        st16_sc1(dst + (size_t)(2 * c) * L_, u32x4{perm_lo(r[i][1][c], r[i][0][c]), perm_lo(r[i][3][c], r[i][2][c]), perm_lo(r[i][5][c], r[i][4][c]), perm_lo(r[i][7][c], r[i][6][c])});
        st16_sc1(dst + (size_t)(2 * c + 1) * L_, u32x4{perm_hi(r[i][1][c], r[i][0][c]), perm_hi(r[i][3][c], r[i][2][c]), perm_hi(r[i][5][c], r[i][4][c]), perm_hi(r[i][7][c], r[i][6][c])});
      }
    }
    asm volatile("" ::"v"(cw1), "v"(cw2));
    return;
  }
  b -= PB_XT;
  if (b < PB_ST) {
    const int t0 = (b * 4 + wave) * STT;
    u32x4 r[8];
#pragma unroll
    for (int j = 0; j < 8; ++j) r[j] = *(const u32x4*)(x0 + (size_t)(t0 + (j % STT)) * D_ + lane * 8);
    for (int i = b * 256 + tid; i < (int)(WS_ZBYTES / 16); i += PB_ST * 256) ((u32x4*)(ws + WS_Z0))[i] = u32x4{0u, 0u, 0u, 0u};
    asm volatile("" : "+v"(r[0]), "+v"(r[1]), "+v"(r[2]), "+v"(r[3]), "+v"(r[4]), "+v"(r[5]), "+v"(r[6]), "+v"(r[7]));
    unsigned word = 0u;
#pragma unroll
    for (int j = 0; j < STT; ++j) {
      float s = 0.f;
#pragma unroll
      for (int c = 0; c < 4; ++c) s += bf_lo(r[j][c]) + bf_hi(r[j][c]);
      const float mean = wave_sum(s) * (1.f / D_);
      float q = 0.f;
#pragma unroll
      for (int c = 0; c < 4; ++c) {
        const float a = bf_lo(r[j][c]) - mean, bb = bf_hi(r[j][c]) - mean;
        q += a * a + bb * bb;
      }
      const float var = wave_sum(q) * (1.f / D_);
      const unsigned wv = pk_bf16(mean, sqrtf(rbf(rbf(var) + 1e-5f)));
      if (lane == j) word = wv;
    }
    if (lane < STT) ((unsigned*)(ws + WS_SW0))[t0 + lane] = word;
    return;
  }
  b -= PB_ST;
  if (b < PB_RT) {
    constexpr int NU = 57344 / (PB_RT * 256);
    u32x2 rr[NU][8];
    size_t dd_[NU];
#pragma unroll
    for (int i = 0; i < NU; ++i) retile_load((b * NU + i) * 256 + tid, w1, w2, rr[i], dd_[i]);
    asm volatile("" : "+v"(rr[NU - 1][0]), "+v"(rr[NU - 1][1]), "+v"(rr[NU - 1][2]), "+v"(rr[NU - 1][3]),
                      "+v"(rr[NU - 1][4]), "+v"(rr[NU - 1][5]), "+v"(rr[NU - 1][6]), "+v"(rr[NU - 1][7]));
#pragma unroll
    for (int i = 0; i < NU; ++i) retile_store(rr[i], ws, dd_[i], lane, true);
    return;
  }
  b -= PB_RT;
  {
    const int cg = tid % 7, rg = tid / 7;
    const int f0 = b * 56 + cg * 8;
    if (tid < 224) {
      u32x4 v[16];
#pragma unroll
      for (int j = 0; j < 16; ++j) v[j] = *(const u32x4*)(w1 + (size_t)(rg * 16 + j) * F_ + f0);
      float s[8] = {0.f, 0.f, 0.f, 0.f, 0.f, 0.f, 0.f, 0.f};
#pragma unroll
      for (int j = 0; j < 16; ++j)
#pragma unroll
        for (int c = 0; c < 4; ++c) { s[2 * c] += bf_lo(v[j][c]); s[2 * c + 1] += bf_hi(v[j][c]); }
#pragma unroll
      for (int e = 0; e < 8; ++e) red[rg * 56 + cg * 8 + e] = s[e];
    }
    __syncthreads();
    if (tid < 56) {
      float t = 0.f;
#pragma unroll
      for (int r = 0; r < 32; ++r) t += red[r * 56 + tid];
      ((float*)(ws + WS_W1S))[b * 56 + tid] = t;
    }
  }
}

constexpr int CV_CS = 260 * 16;
constexpr int CV_KCH = 8 * CV_CS;
constexpr int CV_U0 = 2 * CV_KCH;
constexpr int CV_UCH = (256 + 2048) * 2;
constexpr int CV_XS = CV_U0 + 2 * CV_UCH;
constexpr int CV_RED = CV_XS + 2 * 8192;
constexpr int G1_R = 9;
constexpr int G2_R = 9;
constexpr int LAYER_LDS = 9 * 16384 + 1024;
static_assert(CV_RED + 16384 <= 9 * 16384, "conv LDS");
#define G1_W0 5
#define G2_W0 6
#define CV_PF1 16
#define CV_PF2 22
#define W1PRE 5
#define POLL_SLEEP 2
static_assert(G1_W0 <= G1_R - 2 && W1PRE <= G1_R, "ring safety");
__host__ __device__ constexpr int g2_iss(int i) {
  int v = G2_W0 + i;
  return v < 14 ? v : 14;
}
__host__ __device__ constexpr int g1_young(int c) {
  int n = 0;
  for (int j = c + 1; j < c + G1_W0 && j < 16; ++j) n += (j >= W1PRE) ? 4 : 2;
  return n;
}
template <int N> DI void wait_lgkm2(u32x2& a, u32x2& b) {
  asm volatile("s_waitcnt lgkmcnt(%2)" : "+v"(a), "+v"(b) : "n"(N) : "memory");
}
template <int N> DI void wait_lgkm3(u32x2& a, u32x2& b, u32x4& c) {
  asm volatile("s_waitcnt lgkmcnt(%3)" : "+v"(a), "+v"(b), "+v"(c) : "n"(N) : "memory");
}
template <int OFF> DI void lds_read16(u32x4& d, unsigned addr) {
  asm volatile("ds_read_b128 %0, %1 offset:%2" : "=v"(d) : "v"(addr), "n"(OFF));
}
DI void pin4(bf16x8* f) { asm volatile("" : "+v"(f[0]), "+v"(f[1]), "+v"(f[2]), "+v"(f[3])); }
DI void pin7(u32x4* f) { asm volatile("" : "+v"(f[0]), "+v"(f[1]), "+v"(f[2]), "+v"(f[3]), "+v"(f[4]), "+v"(f[5]), "+v"(f[6])); }
DI u32x4 ld16_sc1_wait(const void* p) {
  u32x4 v;
  asm volatile("global_load_dwordx4 %0, %1, off sc1\n\ts_waitcnt vmcnt(0)" : "=&v"(v) : "v"(p) : "memory");
  return v;
}

struct LayerArgs {
  const unsigned short* dskip;
  unsigned short* out;
  char* ws;
};

extern "C" __global__ __launch_bounds__(512, 2) void s4d_layer_kernel(LayerArgs p) {
  const unsigned cw = warm_code<LAYER_CODE>((const char*)__builtin_amdgcn_s_getpc(), threadIdx.x);
  extern __shared__ __attribute__((aligned(16))) char smem[];
  u32x4 wr[28];
#pragma unroll 1
  for (int LAYER = 0; LAYER < 2; ++LAYER) {
  int tid = threadIdx.x;
  asm volatile("" : "+v"(tid));
  const int lane = tid & 63;
  const int wave8 = __builtin_amdgcn_readfirstlane(tid >> 6);
  const int wave = wave8 & 3;
  const int bx = blockIdx.x & 7, bi = blockIdx.x >> 3;
  const int r32 = lane & 31, hh = lane >> 5;
  char* ws = p.ws;
  const char* w1t = ws + WS_W1T;
  const char* w2t = ws + WS_W2T;
  const int mt2 = bx * 4 + (bi & 3), nt2 = bi >> 2;
  const int ni = wave8 & 1, kq = wave8 >> 1;
  const int mi_f = kq >> 1, kh_f = kq & 1;
  const int tok = mt2 * 64 + mi_f * 32 + r32;
  const int dbase = nt2 * 64 + ni * 32 + 4 * hh + 16 * kh_f;
  unsigned short xr[8];
  const char* w2src = w2t + (size_t)(nt2 * 2 + ni) * 112 * 1024 + kq * 2048 + lane * 16;
  auto pf_w2 = [&](auto J) {
    constexpr int j = J;
    wr[j] = *(const u32x4*)(w2src + (j >> 1) * 8192 + (j & 1) * 1024);
  };
  const int mt = (bx >> 1) * 4 + (bi & 3), nt = (bx & 1) * 7 + (bi >> 2);
  const int ft = nt * 4 + wave;
  const unsigned short* xin = (const unsigned short*)(ws + (LAYER ? WS_XT2 : WS_XT0));
  unsigned short* xt = (unsigned short*)(ws + (LAYER ? WS_XT3 : WS_XT1));
  char* hbuf = ws + (LAYER ? WS_H2 : WS_H);
  unsigned* sw = (unsigned*)(ws + WS_SW) + LAYER * 2048;
  unsigned* flags = (unsigned*)(ws + WS_FL) + LAYER * 256 * FLS;
  unsigned* cflags = (unsigned*)(ws + WS_CF) + LAYER * (CREP * 16 * 32);
  unsigned* eflags = (unsigned*)(ws + WS_CF) + 2 * (CREP * 16 * 32);
  const unsigned short* xrsrc = xt + ((size_t)(tok >> 7) * D_ + dbase) * 128 + (tok & 127);
  auto pf_xr = [&](auto J) {
    constexpr int j = J;
    xr[j] = __hip_atomic_load(xrsrc + ((j & 3) + 8 * (j >> 2)) * 128, __ATOMIC_RELAXED, __HIP_MEMORY_SCOPE_AGENT);
  };
  {
    const int d0 = 2 * (bx * 32 + bi);
    const int gz = tid & 255, gc = tid >> 8;
    const int t4 = tid * 4;
    const int ch = wave8 >> 2, half = wave8 & 1, qs = (wave8 >> 1) & 1;
    float xf[2][4], m[4], sd[4];
    const unsigned dsk2 = *(const unsigned*)(p.dskip + d0);
    const char* gp = ws + WS_G + (size_t)(d0 + gc) * 4096 + gz * 16;
    const u32x4 ga = *(const u32x4*)gp;
    const u32x4 gb = *(const u32x4*)(gp + 16);
    const float kscale = ((const float*)(ws + WS_KINV))[d0 + ch];
    u32x4 swv0 = {0u, 0u, 0u, 0u};
    u32x2 xv0 = {0u, 0u}, xv1 = {0u, 0u};
    if (LAYER == 0) {
      swv0 = *(const u32x4*)(ws + WS_SW0 + t4 * 4);
      xv0 = *(const u32x2*)(xin + (size_t)d0 * L_ + t4);
      xv1 = *(const u32x2*)(xin + (size_t)(d0 + 1) * L_ + t4);
    }
    unsigned zz = 0u;
    asm volatile("" : "+v"(zz));
    if (tid < 16) *(u32x4*)(smem + (tid >> 3) * CV_KCH + (tid & 7) * CV_CS + 256 * 16) = u32x4{zz, zz, zz, zz};
    if (tid >= 64 && tid < 128) {
      const int q = tid - 64;
      *(u32x4*)(smem + CV_U0 + (q >> 5) * CV_UCH + (q & 31) * 16) = u32x4{zz, zz, zz, zz};
    }
    if (tid == 128) *(unsigned*)(smem + 9 * 16384) = 0u;
    {
      const unsigned msk = (gz < 255) ? 0xffffffffu : 0u;
      unsigned w[8] = {ga[0], ga[1], ga[2], ga[3], gb[0] & msk, gb[1] & msk, gb[2] & msk, gb[3] & msk};
      unsigned o[7];
#pragma unroll
      for (int k = 0; k < 7; ++k) o[k] = __builtin_amdgcn_alignbit(w[k + 1], w[k], 16);
      char* dst = smem + gc * CV_KCH + gz * 16;
#pragma unroll
      for (int mm = 0; mm < 4; ++mm) {
        *(u32x4*)(dst + (2 * mm) * CV_CS) = u32x4{w[mm], w[mm + 1], w[mm + 2], w[mm + 3]};
        *(u32x4*)(dst + (2 * mm + 1) * CV_CS) = u32x4{o[mm], o[mm + 1], o[mm + 2], o[mm + 3]};
      }
    }
    f32x4 st0 = {0.f, 0.f, 0.f, 0.f}, st1 = {0.f, 0.f, 0.f, 0.f};
    if (LAYER != 0) {
      if (wave8 == 7) {
        const unsigned* cp = eflags + ((blockIdx.x % CREP) * 16 + (lane & 15)) * 32;
        for (int it = 0; it < (1 << 20); ++it) {
          const unsigned f = __hip_atomic_load(cp, __ATOMIC_RELAXED, __HIP_MEMORY_SCOPE_AGENT);
          if (__all(f >= 16u)) break;
          __builtin_amdgcn_s_sleep(POLL_SLEEP);
        }
      }
      __builtin_amdgcn_s_barrier();
      const auto rs_st = __builtin_amdgcn_make_buffer_rsrc((void*)(ws + WS_ST1), 0, 16384, 0x00020000);
      const auto rs_x = __builtin_amdgcn_make_buffer_rsrc((void*)xin, 0, L_ * D_ * 2, 0x00020000);
      st0 = __builtin_bit_cast(f32x4, __builtin_amdgcn_raw_buffer_load_b128(rs_st, t4 * 8, 0, 16  ));
      st1 = __builtin_bit_cast(f32x4, __builtin_amdgcn_raw_buffer_load_b128(rs_st, t4 * 8 + 16, 0, 16));
      xv0 = __builtin_amdgcn_raw_buffer_load_b64(rs_x, (d0 * L_ + t4) * 2, 0, 16);
      xv1 = __builtin_amdgcn_raw_buffer_load_b64(rs_x, ((d0 + 1) * L_ + t4) * 2, 0, 16);
    }
#pragma unroll
    for (int r = 0; r < 4; ++r) {
      {
        const unsigned wv = swv0[r];
        const float a = (r < 2) ? st0[(r & 1) * 2] : st1[(r & 1) * 2];
        const float bq = (r < 2) ? st0[(r & 1) * 2 + 1] : st1[(r & 1) * 2 + 1];
        const float mean = a * (1.f / D_), var = fmaxf(bq * (1.f / D_) - mean * mean, 0.f);
        const float m1 = rbf(mean), sd1 = rbf(__builtin_amdgcn_sqrtf(rbf(rbf(var) + 1e-5f)));
        m[r] = LAYER ? m1 : bf_lo(wv);
        sd[r] = LAYER ? sd1 : bf_hi(wv);
      }
      const unsigned w0 = xv0[r >> 1], w1_ = xv1[r >> 1];
      xf[0][r] = (r & 1) ? bf_hi(w0) : bf_lo(w0);
      xf[1][r] = (r & 1) ? bf_hi(w1_) : bf_lo(w1_);
    }
    {
      float rs[4];
#pragma unroll
      for (int r = 0; r < 4; ++r) rs[r] = __builtin_amdgcn_rcpf(sd[r]);
#pragma unroll
      for (int c = 0; c < 2; ++c) {
        const float dsk = c ? bf_hi(dsk2) : bf_lo(dsk2);
        float uu[4];
        unsigned xs[4];
#pragma unroll
        for (int r = 0; r < 4; ++r) {
          const float x = xf[c][r];
          const float dd = rbf(x - m[r]);
          float q = dd * rs[r];
          q = fmaf(fmaf(-q, sd[r], dd), rs[r], q);
          uu[r] = rbf(q);
          xs[r] = pk_bf16(x, uu[r] * dsk);
        }
        *(u32x2*)(smem + CV_U0 + c * CV_UCH + (256 + t4) * 2) = u32x2{pk_f16(uu[0], uu[1]), pk_f16(uu[2], uu[3])};
        *(u32x4*)(smem + CV_XS + c * 8192 + t4 * 4) = u32x4{xs[0], xs[1], xs[2], xs[3]};
      }
    }
    asm volatile("" ::"v"(cw));
    __syncthreads();
    const int j16 = lane & 15, g = lane >> 4;
    const int cc = (7 - j16) & 7;
    const int zl = (2031 - j16 - cc) / 8 + g;
    const char* abase = smem + ch * CV_KCH + cc * CV_CS + (zl - 32 * 7 - 4 * (4 * half + 3)) * 16;
    const char* bbase = smem + CV_U0 + ch * CV_UCH + 480 + 32 * j16 + 16 * g - 64 * (4 * half + 3);
    f32x4 acc[4];
#pragma unroll
    for (int q = 0; q < 4; ++q) acc[q] = f32x4{0.f, 0.f, 0.f, 0.f};
    auto body = [&](auto QS, auto PFF) {
      constexpr int S = QS;
      constexpr bool PF = PFF;
      constexpr int Q[4] = {S ? 6 : 7, S ? 5 : 4, S ? 2 : 3, S ? 1 : 0};
      constexpr int NF = S ? 7 : 8;
      f16x8 A[2][NF], B[2][NF];
      auto ld = [&](auto IT) {
        constexpr int it = IT;
#pragma unroll
        for (int k = 0; k < NF; ++k) {
          A[it & 1][k] = *(const f16x8*)(abase + (7 - k) * 512 + (3 - it) * 64);
          B[it & 1][NF - 1 - k] = *(const f16x8*)(bbase + (NF - 1 - k) * 512 + (3 - it) * 64);
        }
      };
      ld(std::integral_constant<int, 0>{});
      __builtin_amdgcn_sched_barrier(0);
      static_for<0, 4>([&](auto IT) {
        constexpr int it = IT;
        if constexpr (it + 1 < 4) ld(std::integral_constant<int, it + 1>{});
        if constexpr (PF) static_for<it * (CV_PF1 / 4), (it + 1) * (CV_PF1 / 4)>([&](auto J) { pf_w2(J); });
#pragma unroll
        for (int b1 = 0; b1 < NF; ++b1)
#pragma unroll
          for (int e = 0; e < 4; ++e)
            if (b1 <= Q[e]) acc[e] = __builtin_amdgcn_mfma_f32_16x16x32_f16(A[it & 1][b1], B[it & 1][Q[e] - b1], acc[e], 0, 0, 0);
#pragma unroll
        for (int k = 0; k < 18; ++k) {
          __builtin_amdgcn_sched_group_barrier(0x8, 1, 0);
          if (it + 1 < 4 && k >= 2 && k - 2 < 2 * NF) __builtin_amdgcn_sched_group_barrier(0x100, 1, 0);
          if (PF && CV_PF1 > 0 && (k % (16 / (CV_PF1 / 4))) == 1 && k / (16 / (CV_PF1 / 4)) < CV_PF1 / 4)
            __builtin_amdgcn_sched_group_barrier(0x20, 1, 0);
        }
        __builtin_amdgcn_sched_barrier(0);
      });
    };
    if (LAYER == 0) {
      if (qs) body(std::integral_constant<int, 1>{}, std::true_type{}); else body(std::integral_constant<int, 0>{}, std::true_type{});
    } else {
      if (qs) body(std::integral_constant<int, 1>{}, std::false_type{}); else body(std::integral_constant<int, 0>{}, std::false_type{});
    }
    f32x4 keep[2];
    {
      char* wrp = smem + CV_RED + wave8 * 2048 + lane * 16;
#pragma unroll
      for (int e = 0; e < 2; ++e) {
        f32x4 give;
#pragma unroll
        for (int r = 0; r < 4; ++r) {
          const float lo = acc[e][r], hi = acc[2 + e][r];
          give[r] = half ? lo : hi;
          keep[e][r] = half ? hi : lo;
        }
        *(f32x4*)(wrp + e * 1024) = give;
      }
    }
    asm volatile("s_waitcnt lgkmcnt(0)" ::: "memory");
    __builtin_amdgcn_s_barrier();
    {
      const char* rd = smem + CV_RED + (wave8 ^ 1) * 2048 + lane * 16;
      const int toff = 16 * j16 + 4 * g;
#pragma unroll
      for (int e = 0; e < 2; ++e) {
        const int q = half ? (e ? (qs ? 1 : 0) : (qs ? 2 : 3)) : (e ? (qs ? 5 : 4) : (qs ? 6 : 7));
        const int t = 256 * q + toff;
        const f32x4 o = *(const f32x4*)(rd + e * 1024);
        const u32x4 xs = *(const u32x4*)(smem + CV_XS + ch * 8192 + t * 4);
        const f32x4 y = keep[e] + o;
        float xo[4];
#pragma unroll
        for (int r = 0; r < 4; ++r) {
          const unsigned w = xs[r];
          xo[r] = bf_lo(w) + rbf(rbf(y[r] * kscale) + bf_hi(w));
        }
        st8_sc1(xt + ((size_t)(t >> 7) * D_ + d0 + ch) * 128 + (t & 127), u32x2{pk_bf16(xo[0], xo[1]), pk_bf16(xo[2], xo[3])});
      }
    }
    wait_vm_lgkm0<0>();
    __builtin_amdgcn_s_barrier();
    if (tid < CREP) (void)__hip_atomic_fetch_add(cflags + (tid * 16 + (d0 >> 5)) * 32, 1u, __ATOMIC_RELAXED, __HIP_MEMORY_SCOPE_AGENT);
  }
  const int grow = lane >> 4, gslot = lane & 15;
  const unsigned short* xsrc = xt + ((size_t)mt * D_ + wave * 8 + grow) * 128 + ((gslot ^ (grow << 2)) * 8);
  const char* wsrc = w1t + (size_t)ft * 32 * 1024 + lane * 16;
  auto issue_w = [&](int c) {
    char* slot = smem + (c % G1_R) * 16384;
#pragma unroll
    for (int i = 0; i < 2; ++i)
      __builtin_amdgcn_global_load_lds((const unsigned*)(wsrc + (size_t)(c * 2 + i) * 1024),
                                       (LAS unsigned*)(slot + 8192 + (wave * 2 + i) * 1024), 16, 0, 0);
  };
  auto issue_x = [&](int c) {
    char* slot = smem + (c % G1_R) * 16384;
#pragma unroll
    for (int i = 0; i < 2; ++i)
      __builtin_amdgcn_global_load_lds((const unsigned*)(xsrc + (size_t)(c * 32 + i * 4) * 128),
                                       (LAS unsigned*)(slot + (wave * 8 + i * 4) * 256), 16, 0, 16  );
  };
  if (bi < 28 && wave8 >= 4) {
    if (wave8 < 6)
      __builtin_amdgcn_global_load_lds((const unsigned*)(ws + WS_W1S) + nt * 128 + wave * 64 + lane,
                                       (LAS unsigned*)(smem + 9 * 16384 + 256 + wave * 256), 4, 0, 0);
#pragma unroll
    for (int c = 0; c < W1PRE; ++c) issue_w(c);
  }
  __builtin_amdgcn_sched_barrier(0);
  if (LAYER == 0) static_for<CV_PF1, 28>([&](auto J) { pf_w2(J); });
  __builtin_amdgcn_sched_barrier(0);
  if (wave8 == 7) {
    const unsigned* cp = cflags + ((blockIdx.x % CREP) * 16 + (lane & 15)) * 32;
    for (int it = 0; it < (1 << 20); ++it) {
      const unsigned f = __hip_atomic_load(cp, __ATOMIC_RELAXED, __HIP_MEMORY_SCOPE_AGENT);
      if (__all(f >= 16u)) break;
      __builtin_amdgcn_s_sleep(POLL_SLEEP);
    }
  }
  __builtin_amdgcn_s_barrier();
  if (bi >= 28) {
    const int ht = bx * 4 + (bi - 28);
    const int cgp = wave8 * 2 + hh;
    const auto rsrc = __builtin_amdgcn_make_buffer_rsrc((void*)xt, 0, L_ * D_ * 2, 0x00020000);
    const int off0 = ((ht >> 1) * D_ * 128 + (ht & 1) * 64 + 2 * r32) * 2;
    const unsigned pv = __builtin_amdgcn_raw_buffer_load_b32(rsrc, off0, 0, 16  );
    unsigned v[32];
#pragma unroll
    for (int j = 0; j < 32; ++j) v[j] = __builtin_amdgcn_raw_buffer_load_b32(rsrc, off0 + (cgp * 32 + j) * 256, 0, 16  );
    static_for<0, 8>([&](auto J) { pf_xr(J); });
    const f32x2 piv = {bf_lo(pv), bf_hi(pv)};
    f32x2 s1 = {0.f, 0.f}, s2 = {0.f, 0.f};
#pragma unroll
    for (int j = 0; j < 32; ++j) {
      const f32x2 dd = f32x2{bf_lo(v[j]), bf_hi(v[j])} - piv;
      s1 += dd;
      s2 = __builtin_elementwise_fma(dd, dd, s2);
    }
    f32x4* red = (f32x4*)smem;
    red[cgp * 32 + r32] = f32x4{s1[0], s2[0], s1[1], s2[1]};
    asm volatile("s_waitcnt lgkmcnt(0)" ::: "memory");
    __builtin_amdgcn_s_barrier();
    if (tid < 64) {
      const f32x2* r2 = (const f32x2*)smem;
      float a = 0.f, q = 0.f;
#pragma unroll
      for (int w = 0; w < 16; ++w) { const f32x2 t = r2[w * 64 + tid]; a += t[0]; q += t[1]; }
      const unsigned pw = __shfl(pv, tid >> 1, 64);
      const float dm = a * (1.f / D_);
      const float var = fmaxf(q * (1.f / D_) - dm * dm, 0.f);
      const float mm = ((tid & 1) ? bf_hi(pw) : bf_lo(pw)) + dm;
      const float sdv = __builtin_amdgcn_sqrtf(rbf(rbf(var) + 1e-5f));
      __hip_atomic_store(sw + ht * 64 + tid, pk_bf16(mm, sdv), __ATOMIC_RELAXED, __HIP_MEMORY_SCOPE_AGENT);
    }
  } else if (wave8 >= 4) {
#pragma unroll
    for (int c = 0; c < G1_W0; ++c) { issue_x(c); if (c >= W1PRE) issue_w(c); }
    static_for<0, 16>([&](auto C) {
      constexpr int c = C;
      constexpr int extra = (c + G1_W0 > 16) ? 8 : 0;
      wait_vm<g1_young(c) + extra>();
      __builtin_amdgcn_s_barrier();
      if constexpr (c + G1_W0 < 16) { issue_x(c + G1_W0); if (c + G1_W0 >= W1PRE) issue_w(c + G1_W0); }
      if constexpr (c + G1_W0 == 16) {
        __builtin_amdgcn_sched_barrier(0);
        static_for<0, 8>([&](auto J) { pf_xr(J); });
        __builtin_amdgcn_sched_barrier(0);
      }
    });
  } else {
    const int blk = (lane >> 4) & 1, q4 = (lane & 15) >> 2, p4 = lane & 3;
    const unsigned sbase = (unsigned)(uintptr_t)(LAS char*)smem;
    unsigned* lcnt = (unsigned*)(smem + 9 * 16384);
    unsigned tb[3][4], ta[3];
#pragma unroll
    for (int ct = 0; ct < 4; ++ct) {
      tb[0][ct] = sbase + (unsigned)((8 * hh + q4) * 256 + (((4 * ct + 2 * blk + (p4 >> 1)) ^ (q4 << 2)) * 16) + 8 * (p4 & 1));
      tb[1][ct] = tb[0][ct] + 65536u; tb[2][ct] = tb[0][ct] + 131072u;
    }
    ta[0] = sbase + 8192 + wave * 2048 + lane * 16; ta[1] = ta[0] + 65536u; ta[2] = ta[0] + 131072u;
    f32x16 acc[4];
#pragma unroll
    for (int ct = 0; ct < 4; ++ct)
#pragma unroll
      for (int i = 0; i < 16; ++i) acc[ct][i] = 0.f;
    constexpr int LA = 6;
    u32x2 bb[8][2];
    u32x4 af[3];
    unsigned swv[4];
    f32x4 ws4[4];
    auto issue_pair = [&](auto NN) {
      constexpr int n = NN, ks = n >> 2, ct = n & 3;
      constexpr int so = ((ks >> 1) % G1_R) * 16384, seg = so >> 16, o = (so & 65535) + (ks & 1) * 4096;
      if constexpr (ct == 0) lds_read16<(so & 65535) + (ks & 1) * 1024>(af[ks % 3], ta[seg]);
      tr_read<o>(bb[n & 7][0], tb[seg][ct]);
      tr_read<o + 1024>(bb[n & 7][1], tb[seg][ct]);
    };
    __builtin_amdgcn_s_barrier();
    static_for<0, LA>([&](auto NN) { issue_pair(NN); });
    static_for<0, 128>([&](auto NN) {
      constexpr int n = NN, ks = n >> 2, ct = n & 3;
      constexpr int last = (n + LA - 1 < 127) ? (n + LA - 1) : 127;
      constexpr int younger = 2 * (last - n) + ((last >> 2) - (n >> 2));
      if constexpr (ct == 0) wait_lgkm3<younger>(bb[n & 7][0], bb[n & 7][1], af[ks % 3]);
      else wait_lgkm2<younger>(bb[n & 7][0], bb[n & 7][1]);
      const u32x4 bu = {bb[n & 7][0][0], bb[n & 7][0][1], bb[n & 7][1][0], bb[n & 7][1][1]};
      acc[ct] = __builtin_amdgcn_mfma_f32_32x32x16_bf16(__builtin_bit_cast(bf16x8, af[ks % 3]), __builtin_bit_cast(bf16x8, bu), acc[ct], 0, 0, 0);
      __builtin_amdgcn_sched_barrier(0);
      if constexpr (n + LA < 128) {
        if constexpr ((n + LA) % 8 == 0) __builtin_amdgcn_s_barrier();
        issue_pair(std::integral_constant<int, n + LA>{});
      }
      if constexpr (n >= 104 && n < 108)
        swv[n - 104] = __hip_atomic_load(sw + mt * 128 + (n - 104) * 32 + r32, __ATOMIC_RELAXED, __HIP_MEMORY_SCOPE_AGENT);
      __builtin_amdgcn_sched_barrier(0);
    });
    for (int it = 0; it < (1 << 20); ++it) {
      if (__all(swv[0] != 0u && swv[1] != 0u && swv[2] != 0u && swv[3] != 0u)) break;
      __builtin_amdgcn_s_sleep(1);
#pragma unroll
      for (int ct = 0; ct < 4; ++ct)
        swv[ct] = __hip_atomic_load(sw + mt * 128 + ct * 32 + r32, __ATOMIC_RELAXED, __HIP_MEMORY_SCOPE_AGENT);
    }
    asm volatile("" : "+v"(swv[0]), "+v"(swv[1]), "+v"(swv[2]), "+v"(swv[3]));
#pragma unroll
    for (int q = 0; q < 4; ++q) ws4[q] = *(const f32x4*)(smem + 9 * 16384 + 256 + (wave * 32 + 8 * q + 4 * hh) * 4);
#pragma unroll
    for (int ct = 0; ct < 4; ++ct) {
      const float rs = __builtin_amdgcn_rcpf(bf_hi(swv[ct]));
      const float cm = -bf_lo(swv[ct]) * rs;
      char* dst = hbuf + ((size_t)((mt * 4 + ct) * 112 + ft * 2) * 64 + lane) * 16;
      const f32x2 c2 = {cm, cm}, r2 = {rs, rs};
#pragma unroll
      for (int s = 0; s < 2; ++s) {
        u32x4 o;
#pragma unroll
        for (int j = 0; j < 4; ++j) {
          const int reg = 8 * s + 2 * j;
          const f32x2 wsum = {ws4[reg >> 2][reg & 3], ws4[reg >> 2][(reg & 3) + 1]};
          const f32x2 a2 = {acc[ct][reg], acc[ct][reg + 1]};
          const f32x2 v2 = __builtin_elementwise_fma(a2, r2, c2 * wsum);
          const s16x2 pv = __builtin_bit_cast(s16x2, __builtin_convertvector(v2, bf16x2));
          const s16x2 z = {0, 0};
          o[j] = __builtin_bit_cast(unsigned, __builtin_elementwise_max(pv, z));
        }
        st16_sc1(dst + s * 1024, o);
      }
    }
    wait_vm<0>();
    if (lane == 0) {
      const unsigned old = __hip_atomic_fetch_add(lcnt, 1u, __ATOMIC_RELAXED, __HIP_MEMORY_SCOPE_WORKGROUP);
      if (old == 3u) __hip_atomic_store(flags + (mt * 16 + nt) * FLS, 1u, __ATOMIC_RELAXED, __HIP_MEMORY_SCOPE_AGENT);
    }
    static_for<0, 8>([&](auto J) { pf_xr(J); });
  }
  pin7(wr); pin7(wr + 7); pin7(wr + 14); pin7(wr + 21);
  {
    unsigned x0_ = xr[0], x1_ = xr[1], x2_ = xr[2], x3_ = xr[3], x4_ = xr[4], x5_ = xr[5], x6_ = xr[6], x7_ = xr[7];
    asm volatile("" : "+v"(x0_), "+v"(x1_), "+v"(x2_), "+v"(x3_), "+v"(x4_), "+v"(x5_), "+v"(x6_), "+v"(x7_));
    xr[0] = x0_; xr[1] = x1_; xr[2] = x2_; xr[3] = x3_; xr[4] = x4_; xr[5] = x5_; xr[6] = x6_; xr[7] = x7_;
  }
  __builtin_amdgcn_s_barrier();
  if (wave8 == 7) {
    const unsigned* fp = flags + ((mt2 >> 1) * 16 + (lane < 14 ? lane : 0)) * FLS;
    for (int it = 0; it < (1 << 20); ++it) {
      const unsigned f = __hip_atomic_load(fp, __ATOMIC_RELAXED, __HIP_MEMORY_SCOPE_AGENT);
      if (__all(f != 0u)) break;
      __builtin_amdgcn_s_sleep(POLL_SLEEP);
    }
  }
  asm volatile("s_waitcnt lgkmcnt(0)" ::: "memory");
  __builtin_amdgcn_s_barrier();
  const char* hsrc = hbuf + (size_t)(mt2 * 2) * 112 * 1024 + wave8 * 1024 + lane * 16;
  char* dstb = smem + wave8 * 2048;
  auto issue2 = [&](int c) {
#pragma unroll
    for (int mi = 0; mi < 2; ++mi)
      __builtin_amdgcn_global_load_lds((const unsigned*)(hsrc + (size_t)mi * 112 * 1024 + c * 8192),
                                       (LAS unsigned*)(dstb + (c % G2_R) * 16384 + mi * 1024), 16, 0, 16  );
  };
  f32x16 acc[2];
#pragma unroll
  for (int i = 0; i < 16; ++i) { acc[0][i] = 0.f; acc[1][i] = 0.f; }
  const char* rdh = smem + kq * 4096 + lane * 16;
  bf16x8 hf[2][4];
#pragma unroll
  for (int c = 0; c < g2_iss(0); ++c) issue2(c);
  static_for<0, 15>([&](auto C) {
    constexpr int c = C;
    auto mma = [&](auto JJ) {
      constexpr int j = JJ;
      if constexpr (c >= 1) {
        constexpr int pc = c - 1;
#pragma unroll
        for (int mi = 0; mi < 2; ++mi)
          acc[mi] = __builtin_amdgcn_mfma_f32_32x32x16_bf16(__builtin_bit_cast(bf16x8, wr[2 * pc + j]), hf[pc & 1][j * 2 + mi], acc[mi], 0, 0, 0);
      }
    };
    mma(std::integral_constant<int, 0>{});
    __builtin_amdgcn_sched_barrier(0);
    if constexpr (c < 14) {
      wait_vm_lgkm0<2 * (g2_iss(c) - (c + 1))>();
      __builtin_amdgcn_s_barrier();
#pragma unroll
      for (int e = 0; e < 4; ++e) hf[c & 1][e] = *(const bf16x8*)(rdh + (c % G2_R) * 16384 + e * 1024);
    }
    __builtin_amdgcn_sched_barrier(0);
    mma(std::integral_constant<int, 1>{});
    __builtin_amdgcn_sched_barrier(0);
    if constexpr (c < 14) {
#pragma unroll
      for (int cn = g2_iss(c); cn < g2_iss(c + 1); ++cn) issue2(cn);
    }
    __builtin_amdgcn_sched_barrier(0);
  });
  char* red = smem + 5 * 16384;
  {
    char* dst = red + (ni * 4 + kq * 8) * 0 + (ni * 16 + kq) * 2048 + lane * 16;
#pragma unroll
    for (int pc = 0; pc < 4; ++pc) {
      f32x4 g0, g1;
#pragma unroll
      for (int i = 0; i < 4; ++i) { g0[i] = acc[pc >> 1][8 * (pc & 1) + i]; g1[i] = acc[pc >> 1][8 * (pc & 1) + 4 + i]; }
      *(f32x4*)(dst + pc * 8192) = g0;
      *(f32x4*)(dst + pc * 8192 + 1024) = g1;
    }
  }
  asm volatile("s_waitcnt lgkmcnt(0)" ::: "memory");
  __builtin_amdgcn_s_barrier();
  float mine[8];
  {
    const char* src = red + (ni * 16 + kq * 4) * 2048 + lane * 16;
    f32x4 o[8];
#pragma unroll
    for (int sl = 0; sl < 8; ++sl) o[sl] = *(const f32x4*)(src + sl * 1024);
    asm volatile("" : "+v"(o[0]), "+v"(o[1]), "+v"(o[2]), "+v"(o[3]), "+v"(o[4]), "+v"(o[5]), "+v"(o[6]), "+v"(o[7]));
    const f32x4 t0 = (o[0] + o[2]) + (o[4] + o[6]), t1 = (o[1] + o[3]) + (o[5] + o[7]);
#pragma unroll
    for (int i = 0; i < 4; ++i) { mine[i] = t0[i]; mine[4 + i] = t1[i]; }
  }
  float xo[8];
  float s1 = 0.f, s2 = 0.f;
#pragma unroll
  for (int i = 0; i < 8; ++i) {
    const float v = rbf(__uint_as_float(((unsigned)xr[i]) << 16) + rbf(mine[i]));
    xo[i] = v;
    s1 += v; s2 += v * v;
  }
  if (LAYER == 1) {
#pragma unroll
    for (int q = 0; q < 2; ++q)
      *(u32x2*)(p.out + (size_t)tok * D_ + dbase + 8 * q) =
          u32x2{pk_bf16(xo[4 * q], xo[4 * q + 1]), pk_bf16(xo[4 * q + 2], xo[4 * q + 3])};
  } else {
    unsigned short* xout = (unsigned short*)(ws + WS_XT2);
#pragma unroll
    for (int i = 0; i < 8; ++i)
      __hip_atomic_store(xout + (size_t)(dbase + (i & 3) + 8 * (i >> 2)) * L_ + tok,
                         (unsigned short)(__float_as_uint(xo[i]) >> 16), __ATOMIC_RELAXED, __HIP_MEMORY_SCOPE_AGENT);
    s1 += __shfl_xor(s1, 32, 64);
    s2 += __shfl_xor(s2, 32, 64);
    float* sred = (float*)smem;
    sred[wave8 * 64 + lane] = hh ? s2 : s1;
    asm volatile("s_waitcnt lgkmcnt(0)" ::: "memory");
    __builtin_amdgcn_s_barrier();
    if (wave8 < 2) {
      const int w0 = wave8 * 4;
      const float t = sred[w0 * 64 + lane] + sred[(w0 + 1) * 64 + lane] + sred[(w0 + 2) * 64 + lane] + sred[(w0 + 3) * 64 + lane];
      (void)__hip_atomic_fetch_add((float*)(ws + WS_ST1) + 2 * (mt2 * 64 + wave8 * 32 + r32) + hh, t, __ATOMIC_RELAXED, __HIP_MEMORY_SCOPE_AGENT);
    }
    wait_vm_lgkm0<0>();
    __builtin_amdgcn_s_barrier();
    if (tid < CREP) (void)__hip_atomic_fetch_add(eflags + (tid * 16 + (blockIdx.x & 15)) * 32, 1u, __ATOMIC_RELAXED, __HIP_MEMORY_SCOPE_AGENT);
  }
  }
}

extern "C" void kernel_launch(void* const* d_in, const int* in_sizes, int n_in,
                              void* d_out, int out_size, void* d_ws, size_t ws_size,
                              hipStream_t stream) {
  const unsigned short* x0 = (const unsigned short*)d_in[0];
  const unsigned short* logA = (const unsigned short*)d_in[1];
  const unsigned short* Aim = (const unsigned short*)d_in[2];
  const unsigned short* Cre = (const unsigned short*)d_in[3];
  const unsigned short* Cim = (const unsigned short*)d_in[4];
  const unsigned short* logdt = (const unsigned short*)d_in[5];
  const unsigned short* dskip = (const unsigned short*)d_in[6];
  const unsigned short* w1 = (const unsigned short*)d_in[8];
  const unsigned short* w2 = (const unsigned short*)d_in[9];
  char* ws = (char*)d_ws;
  (void)hipFuncSetAttribute((const void*)s4d_layer_kernel, hipFuncAttributeMaxDynamicSharedMemorySize, LAYER_LDS);
  prep_kernel<<<PREP_BLOCKS, 256, 0, stream>>>(x0, logA, Aim, Cre, Cim, logdt, w1, w2, ws);
  LayerArgs a0 = {dskip, (unsigned short*)d_out, ws};
  s4d_layer_kernel<<<256, 512, LAYER_LDS, stream>>>(a0);
}
```

```cpp
#include <hip/hip_runtime.h>
#include <hip/hip_bf16.h>
#include <stdint.h>

typedef __attribute__((ext_vector_type(8))) __bf16 bf16x8;
typedef __attribute__((ext_vector_type(2))) __bf16 bf16x2;
typedef __attribute__((ext_vector_type(4))) short s16x4;
typedef __attribute__((ext_vector_type(2))) short s16x2;
typedef __attribute__((ext_vector_type(8))) short s16x8;
typedef __attribute__((ext_vector_type(8))) _Float16 f16x8;
typedef __attribute__((ext_vector_type(2))) _Float16 f16x2;
typedef __attribute__((ext_vector_type(2))) float f32x2;
typedef __attribute__((ext_vector_type(4))) float f32x4;
typedef __attribute__((ext_vector_type(16))) float f32x16;
typedef __attribute__((ext_vector_type(4))) unsigned u32x4;
typedef __attribute__((ext_vector_type(2))) unsigned u32x2;
#define LAS __attribute__((address_space(3)))
#define DI __device__ __forceinline__

constexpr int L_ = 2048, D_ = 512, N_ = 128, F_ = 1792;

constexpr size_t MiB = 1024 * 1024;
constexpr size_t WS_G    = 0;
constexpr size_t WS_XT0  = 2 * MiB;
constexpr size_t WS_XT1  = 4 * MiB;
constexpr size_t WS_XT2  = 6 * MiB;
constexpr size_t WS_XT3  = 8 * MiB;
constexpr size_t WS_W1T  = 10 * MiB;
constexpr size_t WS_W2T  = 12 * MiB;
constexpr size_t WS_H    = 14 * MiB;
constexpr size_t WS_H2   = 24 * MiB;
constexpr size_t WS_KINV = 22 * MiB;
constexpr size_t WS_W1S  = WS_KINV + 4096;
constexpr size_t WS_Z0   = 23 * MiB;
constexpr size_t WS_ST1  = WS_Z0;
constexpr size_t WS_SW   = WS_ST1 + 16384;
#define FLS 32
#define CREP 8
#define CFS (3 * CREP)
constexpr size_t WS_FL   = WS_SW + 16384;
constexpr size_t WS_CF   = WS_FL + 2048 * FLS;
constexpr size_t WS_ZBYTES = 16384 + 16384 + 2048 * FLS + 2048 * CFS;
constexpr size_t WS_SW0  = WS_Z0 + 512 * 1024;

DI float bf_lo(unsigned u) { return __uint_as_float(u << 16); }
DI float bf_hi(unsigned u) { return __uint_as_float(u & 0xffff0000u); }
DI unsigned pk_bf16(float a, float b) {
  f32x2 v = {a, b};
  return __builtin_bit_cast(unsigned, __builtin_convertvector(v, bf16x2));
}
DI unsigned pk_f16(float a, float b) {
  f32x2 v = {a, b};
  return __builtin_bit_cast(unsigned, __builtin_convertvector(v, f16x2));
}
DI float rbf(float a) { return bf_lo(pk_bf16(a, 0.f)); }
DI float ld_bf(const unsigned short* p) { return __uint_as_float(((unsigned)*p) << 16); }
DI unsigned perm_lo(unsigned hi, unsigned lo) { return __builtin_amdgcn_perm(hi, lo, 0x05040100u); }
DI unsigned perm_hi(unsigned hi, unsigned lo) { return __builtin_amdgcn_perm(hi, lo, 0x07060302u); }
template <int CTRL> DI float dpp_add(float v) {
  return v + __uint_as_float(__builtin_amdgcn_update_dpp(0u, __float_as_uint(v), CTRL, 0xf, 0xf, true));
}
DI float wave_sum(float v) {
  v = dpp_add<0xB1>(v);
  v = dpp_add<0x4E>(v);
  v = dpp_add<0x141>(v);
  v = dpp_add<0x140>(v);
  const unsigned u = __float_as_uint(v);
  return (__uint_as_float(__builtin_amdgcn_readlane(u, 0)) + __uint_as_float(__builtin_amdgcn_readlane(u, 16))) +
         (__uint_as_float(__builtin_amdgcn_readlane(u, 32)) + __uint_as_float(__builtin_amdgcn_readlane(u, 48)));
}
template <int N> DI void wait_vm() { asm volatile("s_waitcnt vmcnt(%0)" ::"n"(N) : "memory"); }
template <int OFF> DI void tr_read(u32x2& d, unsigned addr) {
  asm volatile("ds_read_b64_tr_b16 %0, %1 offset:%2" : "=v"(d) : "v"(addr), "n"(OFF));
}
template <int N> DI void wait_vm_lgkm0() { asm volatile("s_waitcnt vmcnt(%0) lgkmcnt(0)" ::"n"(N) : "memory"); }
template <int I, int E, class F> DI void static_for(F&& f) {
  if constexpr (I < E) { f(std::integral_constant<int, I>{}); static_for<I + 1, E>(f); }
}

constexpr int TROW = 528;

__host__ __device__ constexpr int tab_w(int idx) {
  return idx < 16 ? idx : (idx < 40 ? ((8 + ((idx - 16) & 7)) << (1 + ((idx - 16) >> 3))) : 128);
}
__host__ __device__ constexpr int ilog2c(int w) { return w <= 1 ? 0 : 1 + ilog2c(w >> 1); }
__host__ __device__ constexpr int tab_idx(int w) {
  return w < 16 ? w : (w == 128 ? 40 : 16 + 8 * (ilog2c(w) - 4) + ((w >> (ilog2c(w) - 3)) - 8));
}
DI int tab_idx_rt(int w) {
  if (w < 16) return w;
  int lg = 31 - __clz(w);
  return 16 + 8 * (lg - 4) + ((w >> (lg - 3)) - 8);
}

DI void retile_load(int u, const unsigned short* __restrict__ w1, const unsigned short* __restrict__ w2, u32x2* r, size_t& dst) {
  if (u < 28672) {
    const int fg = u % 448, o = u / 448;
    const int f = 4 * fg;
#pragma unroll
    for (int j = 0; j < 8; ++j) r[j] = *(const u32x2*)(w1 + (size_t)(8 * o + j) * F_ + f);
    dst = WS_W1T + ((size_t)(((f >> 5) * 32 + (o >> 1)) * 64 + (o & 1) * 32 + (f & 31))) * 16;
  } else {
    const int v = u - 28672;
    const int dg = v & 127, o = v >> 7;
    const int dd = 4 * dg, ks = o >> 1, hh = o & 1;
#pragma unroll
    for (int j = 0; j < 8; ++j)
      r[j] = *(const u32x2*)(w2 + (size_t)(16 * ks + 8 * (j >> 2) + 4 * hh + (j & 3)) * D_ + dd);
    dst = WS_W2T + ((size_t)(((dd >> 5) * 112 + ks) * 64 + hh * 32 + (dd & 31))) * 16;
  }
}
DI void st16_sc1(void* p, u32x4 v) { asm volatile("global_store_dwordx4 %0, %1, off sc1\n\ts_nop 1" ::"v"(p), "v"(v) : "memory"); }
DI void st8_sc1(void* p, u32x2 v) { asm volatile("global_store_dwordx2 %0, %1, off sc1\n\ts_nop 1" ::"v"(p), "v"(v) : "memory"); }
template <int K> DI unsigned quad_bcast(unsigned v) { return __builtin_amdgcn_update_dpp(0u, v, K * 0x55, 0xf, 0xf, true); }
DI void retile_store(const u32x2* r, char* ws, size_t dst, int lane, bool active) {
  u32x4 pc[4];
#pragma unroll
  for (int c = 0; c < 2; ++c) {
    pc[2 * c] = u32x4{perm_lo(r[1][c], r[0][c]), perm_lo(r[3][c], r[2][c]), perm_lo(r[5][c], r[4][c]), perm_lo(r[7][c], r[6][c])};
    pc[2 * c + 1] = u32x4{perm_hi(r[1][c], r[0][c]), perm_hi(r[3][c], r[2][c]), perm_hi(r[5][c], r[4][c]), perm_hi(r[7][c], r[6][c])};
  }
  const int ql = lane & 3;
  char* base = ws + dst - (size_t)ql * 64 + ql * 16;
  static_for<0, 4>([&](auto KK) {
    constexpr int k = KK;
    u32x4 o;
#pragma unroll
    for (int e = 0; e < 4; ++e) {
      const unsigned v0 = quad_bcast<k>(pc[0][e]), v1 = quad_bcast<k>(pc[1][e]), v2 = quad_bcast<k>(pc[2][e]), v3 = quad_bcast<k>(pc[3][e]);
      o[e] = ql == 0 ? v0 : (ql == 1 ? v1 : (ql == 2 ? v2 : v3));
    }
    if (active) st16_sc1(base + k * 64, o);
  });
}

#define LAYER_CODE 24576
#define PREP_CODE 14336
template <int BYTES> DI unsigned warm_code(const char* pc, int t) {
  const int o = t * 128;
  return *(const __attribute__((address_space(1))) unsigned*)(uintptr_t)(pc + (o < BYTES ? o : 0));
}
#define KSQ 2
#define STT 8
#define XTT 2
constexpr int PB_KGEN = 512, PB_XT = 64 / XTT, PB_ST = 512 / STT, PB_RT = 112, PB_WS = 32;
constexpr int PREP_BLOCKS = PB_KGEN + PB_XT + PB_ST + PB_RT + PB_WS;

__global__ __launch_bounds__(256) void prep_kernel(
    const unsigned short* __restrict__ x0, const unsigned short* __restrict__ logA,
    const unsigned short* __restrict__ Aim, const unsigned short* __restrict__ Cre,
    const unsigned short* __restrict__ Cim, const unsigned short* __restrict__ logdt,
    const unsigned short* __restrict__ w1, const unsigned short* __restrict__ w2, char* __restrict__ ws) {
  __shared__ __attribute__((aligned(16))) char smem[82 * TROW + 1536];
  float* red = (float*)smem;
  const int tid = threadIdx.x, lane = tid & 63;
  const int wave = __builtin_amdgcn_readfirstlane(tid >> 6);
  int b = blockIdx.x;
  unsigned cw1 = 0u;
  if (b >= PB_KGEN && b < PB_KGEN + PB_XT) cw1 = warm_code<PREP_CODE>((const char*)__builtin_amdgcn_s_getpc(), tid);
  if (b < PB_KGEN) {
    const int d = ((b & 7) * 32 + ((b >> 3) & 31)) * 2 + (b >> 8);
    const int isH = wave >> 1;
    const int n = (wave & 1) * 64 + lane;
    const float pdt = ld_bf(logdt + d), pla = ld_bf(logA + d * N_ + n), pai = ld_bf(Aim + d * N_ + n);
    const float p0r = ld_bf(Cre + d * N_ + lane), p0i = ld_bf(Cim + d * N_ + lane);
    const float p1r = ld_bf(Cre + d * N_ + 64 + lane), p1i = ld_bf(Cim + d * N_ + 64 + lane);
    const float dt = rbf(expf(pdt));
    const float ar = -rbf(expf(pla)) * dt;
    const float ai = pai * (isH ? -dt : dt);
    const float c0r = p0r * dt, c0i = p0i * dt, c1r = p1r * dt, c1i = p1i * dt;
    const float S = wave_sum(2.f * (fabsf(c0r) + fabsf(c0i) + fabsf(c1r) + fabsf(c1i)));
    int ex = 0;
    (void)frexpf(S, &ex);
    if (!(S > 0.f) || !(S < 3.0e38f)) ex = 15;
    ex = max(-100, min(ex, 120));
    const float scale = ldexpf(1.f, 15 - ex);
    const float cr = (wave & 1) ? c1r : c0r, ci = (wave & 1) ? c1i : c0i;
    const float base = isH ? 16.f : 1.f;
    const float er = ar * 1.44269504088896340736f * base;
    const float IH = 0.15915494309189533577f;
    const float IL = (float)(0.15915494309189533577 - (double)0.15915494309189533577f);
    float fh = ai * IH;
    float fl = fmaf(ai, IH, -fh) + ai * IL;
    fh *= base; fl *= base;
    f32x2 Er2[8], Ei2[8];
#pragma unroll
    for (int k = 0; k < 8; k += KSQ) {
      const float pw = (float)(1 << k);
      const float mag = __builtin_amdgcn_exp2f(er * pw);
      const float ph = __builtin_amdgcn_fractf(fh * pw) + fl * pw;
      float re = mag * __builtin_amdgcn_cosf(ph), im = mag * __builtin_amdgcn_sinf(ph);
#pragma unroll
      for (int j = 0; j < KSQ; ++j) {
        Er2[k + j] = f32x2{re, re};
        Ei2[k + j] = f32x2{-im, im};
        const float r2 = fmaf(re, re, -im * im), i2 = (re + re) * im;
        re = r2; im = i2;
      }
    }
    f32x2 tv[41];
    tv[0] = isH ? f32x2{2.f * scale * cr, -2.f * scale * ci} : f32x2{1.f, 0.f};
    char* trow = smem + isH * (41 * TROW) + n * 4;
    *(unsigned*)trow = __builtin_bit_cast(unsigned, __builtin_convertvector(tv[0], f16x2));
    static_for<1, 41>([&](auto I) {
      constexpr int idx = I;
      constexpr int w = tab_w(idx);
      constexpr int lg = ilog2c(w);
      constexpr int ridx = tab_idx(w - (1 << lg));
      const f32x2 sw = __builtin_shufflevector(tv[ridx], tv[ridx], 1, 0);
      tv[idx] = __builtin_elementwise_fma(Ei2[lg], sw, Er2[lg] * tv[ridx]);
      *(unsigned*)(trow + idx * TROW) = __builtin_bit_cast(unsigned, __builtin_convertvector(tv[idx], f16x2));
    });
    int cix[8];
    {
      const int l0 = 2040 - tid * 8;
      const int sh = max((31 - __clz(l0 | 1)) - 7, 0);
      const int one = sh > 0 ? 1 : 0;
#pragma unroll
      for (int e = 0; e < 8; ++e) {
        const int l = l0 + 7 - e;
        cix[e] = 128 * sh + ((l + ((1 << sh) >> 1) - one + ((l >> sh) & one)) >> sh);
      }
    }
    __syncthreads();
    _Float16* kc = (_Float16*)(smem + 82 * TROW);
    {
      const int s = wave, i16 = lane & 15, g = lane >> 4;
      const int hw = (s == 0) ? i16 : (i16 < 8 ? ((8 + i16) << s) : 128);
      const char* arow = smem + 41 * TROW + tab_idx_rt(hw) * TROW + g * 16;
      const char* brow = smem + tab_idx_rt(i16 << s) * TROW + g * 16;
      f16x8 af[8], bf[8];
#pragma unroll
      for (int ks = 0; ks < 8; ++ks) { af[ks] = *(const f16x8*)(arow + ks * 64); bf[ks] = *(const f16x8*)(brow + ks * 64); }
      f32x4 k0 = {0.f, 0.f, 0.f, 0.f}, k1 = {0.f, 0.f, 0.f, 0.f};
#pragma unroll
      for (int ks = 0; ks < 8; ks += 2) {
        k0 = __builtin_amdgcn_mfma_f32_16x16x32_f16(af[ks], bf[ks], k0, 0, 0, 0);
        k1 = __builtin_amdgcn_mfma_f32_16x16x32_f16(af[ks + 1], bf[ks + 1], k1, 0, 0, 0);
      }
#pragma unroll
      for (int r = 0; r < 4; ++r) {
        const int i = 4 * g + r;
        const int cidx = (s == 0) ? (16 * i + i16) : (256 + 128 * (s - 1) + 16 * i + i16);
        const bool ok = (s == 0) || (i < 8) || (s == 3 && i == 8 && i16 == 0);
        if (ok) kc[cidx] = (_Float16)(k0[r] + k1[r]);
      }
    }
    __syncthreads();
    unsigned short o[8];
#pragma unroll
    for (int e = 0; e < 8; ++e) o[e] = ((const unsigned short*)kc)[cix[e]];
    *(u32x4*)(ws + WS_G + (size_t)d * 4096 + tid * 16) =
        u32x4{(unsigned)o[0] | ((unsigned)o[1] << 16), (unsigned)o[2] | ((unsigned)o[3] << 16),
              (unsigned)o[4] | ((unsigned)o[5] << 16), (unsigned)o[6] | ((unsigned)o[7] << 16)};
    if (tid == 0) ((float*)(ws + WS_KINV))[d] = ldexpf(1.f, ex - 15);
    return;
  }
  b -= PB_KGEN;
  if (b < PB_XT) {
    unsigned long long lpc;
    asm volatile("s_getpc_b64 s[40:41]\n\ts_add_u32 s40, s40, s4d_layer_kernel@rel32@lo+4\n\t"
                 "s_addc_u32 s41, s41, s4d_layer_kernel@rel32@hi+12\n\ts_mov_b64 %0, s[40:41]"
                 : "=s"(lpc) :: "scc", "s40", "s41");
    const unsigned cw2 = warm_code<LAYER_CODE>((const char*)lpc, tid);
    const int tg = lane & 7, cg = lane >> 3;
    u32x4 r[XTT][8];
#pragma unroll
    for (int i = 0; i < XTT; ++i) {
      const int tile = (b * 4 + wave) * XTT + i, tt = tile >> 3, ct = tile & 7;
      const unsigned short* src = x0 + (size_t)(tt * 64 + tg * 8) * D_ + ct * 64 + cg * 8;
#pragma unroll
      for (int j = 0; j < 8; ++j) r[i][j] = *(const u32x4*)(src + (size_t)j * D_);
    }
    asm volatile("" : "+v"(r[XTT - 1][0]), "+v"(r[XTT - 1][1]), "+v"(r[XTT - 1][2]), "+v"(r[XTT - 1][3]),
                      "+v"(r[XTT - 1][4]), "+v"(r[XTT - 1][5]), "+v"(r[XTT - 1][6]), "+v"(r[XTT - 1][7]));
#pragma unroll
    for (int i = 0; i < XTT; ++i) {
      const int tile = (b * 4 + wave) * XTT + i, tt = tile >> 3, ct = tile & 7;
      unsigned short* dst = (unsigned short*)(ws + WS_XT0) + (size_t)(ct * 64 + cg * 8) * L_ + tt * 64 + tg * 8;
#pragma unroll
      for (int c = 0; c < 4; ++c) {
        st16_sc1(dst + (size_t)(2 * c) * L_, u32x4{perm_lo(r[i][1][c], r[i][0][c]), perm_lo(r[i][3][c], r[i][2][c]), perm_lo(r[i][5][c], r[i][4][c]), perm_lo(r[i][7][c], r[i][6][c])});
        st16_sc1(dst + (size_t)(2 * c + 1) * L_, u32x4{perm_hi(r[i][1][c], r[i][0][c]), perm_hi(r[i][3][c], r[i][2][c]), perm_hi(r[i][5][c], r[i][4][c]), perm_hi(r[i][7][c], r[i][6][c])});
      }
    }
    asm volatile("" ::"v"(cw1), "v"(cw2));
    return;
  }
  b -= PB_XT;
  if (b < PB_ST) {
    const int t0 = (b * 4 + wave) * STT;
    u32x4 r[8];
#pragma unroll
    for (int j = 0; j < 8; ++j) r[j] = *(const u32x4*)(x0 + (size_t)(t0 + (j % STT)) * D_ + lane * 8);
    for (int i = b * 256 + tid; i < (int)(WS_ZBYTES / 16); i += PB_ST * 256) ((u32x4*)(ws + WS_Z0))[i] = u32x4{0u, 0u, 0u, 0u};
    asm volatile("" : "+v"(r[0]), "+v"(r[1]), "+v"(r[2]), "+v"(r[3]), "+v"(r[4]), "+v"(r[5]), "+v"(r[6]), "+v"(r[7]));
    unsigned word = 0u;
#pragma unroll
    for (int j = 0; j < STT; ++j) {
      float s = 0.f;
#pragma unroll
      for (int c = 0; c < 4; ++c) s += bf_lo(r[j][c]) + bf_hi(r[j][c]);
      const float mean = wave_sum(s) * (1.f / D_);
      float q = 0.f;
#pragma unroll
      for (int c = 0; c < 4; ++c) {
        const float a = bf_lo(r[j][c]) - mean, bb = bf_hi(r[j][c]) - mean;
        q += a * a + bb * bb;
      }
      const float var = wave_sum(q) * (1.f / D_);
      const unsigned wv = pk_bf16(mean, sqrtf(rbf(rbf(var) + 1e-5f)));
      if (lane == j) word = wv;
    }
    if (lane < STT) ((unsigned*)(ws + WS_SW0))[t0 + lane] = word;
    return;
  }
  b -= PB_ST;
  if (b < PB_RT) {
    constexpr int NU = 57344 / (PB_RT * 256);
    u32x2 rr[NU][8];
    size_t dd_[NU];
#pragma unroll
    for (int i = 0; i < NU; ++i) retile_load((b * NU + i) * 256 + tid, w1, w2, rr[i], dd_[i]);
    asm volatile("" : "+v"(rr[NU - 1][0]), "+v"(rr[NU - 1][1]), "+v"(rr[NU - 1][2]), "+v"(rr[NU - 1][3]),
                      "+v"(rr[NU - 1][4]), "+v"(rr[NU - 1][5]), "+v"(rr[NU - 1][6]), "+v"(rr[NU - 1][7]));
#pragma unroll
    for (int i = 0; i < NU; ++i) retile_store(rr[i], ws, dd_[i], lane, true);
    return;
  }
  b -= PB_RT;
  {
    const int cg = tid % 7, rg = tid / 7;
    const int f0 = b * 56 + cg * 8;
    if (tid < 224) {
      u32x4 v[16];
#pragma unroll
      for (int j = 0; j < 16; ++j) v[j] = *(const u32x4*)(w1 + (size_t)(rg * 16 + j) * F_ + f0);
      float s[8] = {0.f, 0.f, 0.f, 0.f, 0.f, 0.f, 0.f, 0.f};
#pragma unroll
      for (int j = 0; j < 16; ++j)
#pragma unroll
        for (int c = 0; c < 4; ++c) { s[2 * c] += bf_lo(v[j][c]); s[2 * c + 1] += bf_hi(v[j][c]); }
#pragma unroll
      for (int e = 0; e < 8; ++e) red[rg * 56 + cg * 8 + e] = s[e];
    }
    __syncthreads();
    if (tid < 56) {
      float t = 0.f;
#pragma unroll
      for (int r = 0; r < 32; ++r) t += red[r * 56 + tid];
      ((float*)(ws + WS_W1S))[b * 56 + tid] = t;
    }
  }
}

constexpr int CV_CS = 260 * 16;
constexpr int CV_KCH = 8 * CV_CS;
constexpr int CV_U0 = 2 * CV_KCH;
constexpr int CV_UCH = (256 + 2048) * 2;
constexpr int CV_XS = CV_U0 + 2 * CV_UCH;
constexpr int CV_RED = CV_XS + 2 * 8192;
constexpr int G1_R = 9;
constexpr int G2_R = 9;
constexpr int LAYER_LDS = 9 * 16384 + 1024;
static_assert(CV_RED + 16384 <= 9 * 16384, "conv LDS");
#define G1_W0 5
#define G2_W0 6
#define CV_PF1 16
#define W1PRE 5
#define POLL_SLEEP 2
static_assert(G1_W0 <= G1_R - 2 && W1PRE <= G1_R, "ring safety");
__host__ __device__ constexpr int g2_iss(int i) {
  int v = G2_W0 + i;
  return v < 14 ? v : 14;
}
__host__ __device__ constexpr int g1_young(int c) {
  int n = 0;
  for (int j = c + 1; j < c + G1_W0 && j < 16; ++j) n += (j >= W1PRE) ? 4 : 2;
  return n;
}
template <int N> DI void wait_lgkm2(u32x2& a, u32x2& b) {
  asm volatile("s_waitcnt lgkmcnt(%2)" : "+v"(a), "+v"(b) : "n"(N) : "memory");
}
template <int N> DI void wait_lgkm3(u32x2& a, u32x2& b, u32x4& c) {
  asm volatile("s_waitcnt lgkmcnt(%3)" : "+v"(a), "+v"(b), "+v"(c) : "n"(N) : "memory");
}
template <int OFF> DI void lds_read16(u32x4& d, unsigned addr) {
  asm volatile("ds_read_b128 %0, %1 offset:%2" : "=v"(d) : "v"(addr), "n"(OFF));
}
DI void pin7(u32x4* f) { asm volatile("" : "+v"(f[0]), "+v"(f[1]), "+v"(f[2]), "+v"(f[3]), "+v"(f[4]), "+v"(f[5]), "+v"(f[6])); }

struct LayerArgs {
  const unsigned short* dskip;
  unsigned short* out;
  char* ws;
};

extern "C" __global__ __launch_bounds__(512, 2) void s4d_layer_kernel(LayerArgs p) {
  const unsigned cw = warm_code<LAYER_CODE>((const char*)__builtin_amdgcn_s_getpc(), threadIdx.x);
  extern __shared__ __attribute__((aligned(16))) char smem[];
  u32x4 wr[28];
#pragma unroll 1
  for (int LAYER = 0; LAYER < 2; ++LAYER) {
  int tid = threadIdx.x;
  asm volatile("" : "+v"(tid));
  const int lane = tid & 63;
  const int wave8 = __builtin_amdgcn_readfirstlane(tid >> 6);
  const int wave = wave8 & 3;
  const int bx = blockIdx.x & 7, bi = blockIdx.x >> 3;
  const int r32 = lane & 31, hh = lane >> 5;
  char* ws = p.ws;
  const char* w1t = ws + WS_W1T;
  const char* w2t = ws + WS_W2T;
  const int mt2 = bx * 4 + (bi & 3), nt2 = bi >> 2;
  const int ni = wave8 & 1, kq = wave8 >> 1;
  const int mi_f = kq >> 1, kh_f = kq & 1;
  const int tok = mt2 * 64 + mi_f * 32 + r32;
  const int dbase = nt2 * 64 + ni * 32 + 4 * hh + 16 * kh_f;
  unsigned short xr[8];
  const char* w2src = w2t + (size_t)(nt2 * 2 + ni) * 112 * 1024 + kq * 2048 + lane * 16;
  auto pf_w2 = [&](auto J) {
    constexpr int j = J;
    wr[j] = *(const u32x4*)(w2src + (j >> 1) * 8192 + (j & 1) * 1024);
  };
  const int mt = (bx >> 1) * 4 + (bi & 3), nt = (bx & 1) * 7 + (bi >> 2);
  const int ft = nt * 4 + wave;
  const unsigned short* xin = (const unsigned short*)(ws + (LAYER ? WS_XT2 : WS_XT0));
  unsigned short* xt = (unsigned short*)(ws + (LAYER ? WS_XT3 : WS_XT1));
  char* hbuf = ws + (LAYER ? WS_H2 : WS_H);
  unsigned* sw = (unsigned*)(ws + WS_SW) + LAYER * 2048;
  unsigned* flags = (unsigned*)(ws + WS_FL) + LAYER * 256 * FLS;
  unsigned* cflags = (unsigned*)(ws + WS_CF) + LAYER * (CREP * 16 * 32);
  unsigned* eflags = (unsigned*)(ws + WS_CF) + 2 * (CREP * 16 * 32);
  const unsigned short* xrsrc = xt + ((size_t)(tok >> 7) * D_ + dbase) * 128 + (tok & 127);
  auto pf_xr = [&](auto J) {
    constexpr int j = J;
    xr[j] = __hip_atomic_load(xrsrc + ((j & 3) + 8 * (j >> 2)) * 128, __ATOMIC_RELAXED, __HIP_MEMORY_SCOPE_AGENT);
  };
  {
    const int d0 = 2 * (bx * 32 + bi);
    const int gz = tid & 255, gc = tid >> 8;
    const int t4 = tid * 4;
    const int ch = wave8 >> 2, half = wave8 & 1, qs = (wave8 >> 1) & 1;
    float xf[2][4], m[4], sd[4];
    const unsigned dsk2 = *(const unsigned*)(p.dskip + d0);
    const char* gp = ws + WS_G + (size_t)(d0 + gc) * 4096 + gz * 16;
    const u32x4 ga = *(const u32x4*)gp;
    const u32x4 gb = *(const u32x4*)(gp + 16);
    const float kscale = ((const float*)(ws + WS_KINV))[d0 + ch];
    u32x4 swv0 = {0u, 0u, 0u, 0u};
    u32x2 xv0 = {0u, 0u}, xv1 = {0u, 0u};
    if (LAYER == 0) {
      swv0 = *(const u32x4*)(ws + WS_SW0 + t4 * 4);
      xv0 = *(const u32x2*)(xin + (size_t)d0 * L_ + t4);
      xv1 = *(const u32x2*)(xin + (size_t)(d0 + 1) * L_ + t4);
    }
    unsigned zz = 0u;
    asm volatile("" : "+v"(zz));
    if (tid < 16) *(u32x4*)(smem + (tid >> 3) * CV_KCH + (tid & 7) * CV_CS + 256 * 16) = u32x4{zz, zz, zz, zz};
    if (tid >= 64 && tid < 128) {
      const int q = tid - 64;
      *(u32x4*)(smem + CV_U0 + (q >> 5) * CV_UCH + (q & 31) * 16) = u32x4{zz, zz, zz, zz};
    }
    if (tid == 128) *(unsigned*)(smem + 9 * 16384) = 0u;
    {
      const unsigned msk = (gz < 255) ? 0xffffffffu : 0u;
      unsigned w[8] = {ga[0], ga[1], ga[2], ga[3], gb[0] & msk, gb[1] & msk, gb[2] & msk, gb[3] & msk};
      unsigned o[7];
#pragma unroll
      for (int k = 0; k < 7; ++k) o[k] = __builtin_amdgcn_alignbit(w[k + 1], w[k], 16);
      char* dst = smem + gc * CV_KCH + gz * 16;
#pragma unroll
      for (int mm = 0; mm < 4; ++mm) {
        *(u32x4*)(dst + (2 * mm) * CV_CS) = u32x4{w[mm], w[mm + 1], w[mm + 2], w[mm + 3]};
        *(u32x4*)(dst + (2 * mm + 1) * CV_CS) = u32x4{o[mm], o[mm + 1], o[mm + 2], o[mm + 3]};
      }
    }
    f32x4 st0 = {0.f, 0.f, 0.f, 0.f}, st1 = {0.f, 0.f, 0.f, 0.f};
    if (LAYER != 0) {
      if (wave8 == 7) {
        const unsigned* cp = eflags + ((blockIdx.x % CREP) * 16 + (lane & 15)) * 32;
        for (int it = 0; it < (1 << 20); ++it) {
          const unsigned f = __hip_atomic_load(cp, __ATOMIC_RELAXED, __HIP_MEMORY_SCOPE_AGENT);
          if (__all(f >= 16u)) break;
          __builtin_amdgcn_s_sleep(POLL_SLEEP);
        }
      }
      __builtin_amdgcn_s_barrier();
      const auto rs_st = __builtin_amdgcn_make_buffer_rsrc((void*)(ws + WS_ST1), 0, 16384, 0x00020000);
      const auto rs_x = __builtin_amdgcn_make_buffer_rsrc((void*)xin, 0, L_ * D_ * 2, 0x00020000);
      st0 = __builtin_bit_cast(f32x4, __builtin_amdgcn_raw_buffer_load_b128(rs_st, t4 * 8, 0, 16  ));
      st1 = __builtin_bit_cast(f32x4, __builtin_amdgcn_raw_buffer_load_b128(rs_st, t4 * 8 + 16, 0, 16));
      xv0 = __builtin_amdgcn_raw_buffer_load_b64(rs_x, (d0 * L_ + t4) * 2, 0, 16);
      xv1 = __builtin_amdgcn_raw_buffer_load_b64(rs_x, ((d0 + 1) * L_ + t4) * 2, 0, 16);
    }
#pragma unroll
    for (int r = 0; r < 4; ++r) {
      {
        const unsigned wv = swv0[r];
        const float a = (r < 2) ? st0[(r & 1) * 2] : st1[(r & 1) * 2];
        const float bq = (r < 2) ? st0[(r & 1) * 2 + 1] : st1[(r & 1) * 2 + 1];
        const float mean = a * (1.f / D_), var = fmaxf(bq * (1.f / D_) - mean * mean, 0.f);
        const float m1 = rbf(mean), sd1 = rbf(__builtin_amdgcn_sqrtf(rbf(rbf(var) + 1e-5f)));
        m[r] = LAYER ? m1 : bf_lo(wv);
        sd[r] = LAYER ? sd1 : bf_hi(wv);
      }
      const unsigned w0 = xv0[r >> 1], w1_ = xv1[r >> 1];
      xf[0][r] = (r & 1) ? bf_hi(w0) : bf_lo(w0);
      xf[1][r] = (r & 1) ? bf_hi(w1_) : bf_lo(w1_);
    }
    {
      float rs[4];
#pragma unroll
      for (int r = 0; r < 4; ++r) rs[r] = __builtin_amdgcn_rcpf(sd[r]);
#pragma unroll
      for (int c = 0; c < 2; ++c) {
        const float dsk = c ? bf_hi(dsk2) : bf_lo(dsk2);
        float uu[4];
        unsigned xs[4];
#pragma unroll
        for (int r = 0; r < 4; ++r) {
          const float x = xf[c][r];
          const float dd = rbf(x - m[r]);
          float q = dd * rs[r];
          q = fmaf(fmaf(-q, sd[r], dd), rs[r], q);
          uu[r] = rbf(q);
          xs[r] = pk_bf16(x, uu[r] * dsk);
        }
        *(u32x2*)(smem + CV_U0 + c * CV_UCH + (256 + t4) * 2) = u32x2{pk_f16(uu[0], uu[1]), pk_f16(uu[2], uu[3])};
        *(u32x4*)(smem + CV_XS + c * 8192 + t4 * 4) = u32x4{xs[0], xs[1], xs[2], xs[3]};
      }
    }
    asm volatile("" ::"v"(cw));
    __syncthreads();
    const int j16 = lane & 15, g = lane >> 4;
    const int cc = (7 - j16) & 7;
    const int zl = (2031 - j16 - cc) / 8 + g;
    const char* abase = smem + ch * CV_KCH + cc * CV_CS + (zl - 32 * 7 - 4 * (4 * half + 3)) * 16;
    const char* bbase = smem + CV_U0 + ch * CV_UCH + 480 + 32 * j16 + 16 * g - 64 * (4 * half + 3);
    f32x4 acc[4];
#pragma unroll
    for (int q = 0; q < 4; ++q) acc[q] = f32x4{0.f, 0.f, 0.f, 0.f};
    auto body = [&](auto QS, auto PFF) {
      constexpr int S = QS;
      constexpr bool PF = PFF;
      constexpr int Q[4] = {S ? 6 : 7, S ? 5 : 4, S ? 2 : 3, S ? 1 : 0};
      constexpr int NF = S ? 7 : 8;
      f16x8 A[2][NF], B[2][NF];
      auto ld = [&](auto IT) {
        constexpr int it = IT;
#pragma unroll
        for (int k = 0; k < NF; ++k) {
          A[it & 1][k] = *(const f16x8*)(abase + (7 - k) * 512 + (3 - it) * 64);
          B[it & 1][NF - 1 - k] = *(const f16x8*)(bbase + (NF - 1 - k) * 512 + (3 - it) * 64);
        }
      };
      ld(std::integral_constant<int, 0>{});
      __builtin_amdgcn_sched_barrier(0);
      static_for<0, 4>([&](auto IT) {
        constexpr int it = IT;
        if constexpr (it + 1 < 4) ld(std::integral_constant<int, it + 1>{});
        if constexpr (PF) static_for<it * (CV_PF1 / 4), (it + 1) * (CV_PF1 / 4)>([&](auto J) { pf_w2(J); });
#pragma unroll
        for (int b1 = 0; b1 < NF; ++b1)
#pragma unroll
          for (int e = 0; e < 4; ++e)
            if (b1 <= Q[e]) acc[e] = __builtin_amdgcn_mfma_f32_16x16x32_f16(A[it & 1][b1], B[it & 1][Q[e] - b1], acc[e], 0, 0, 0);
#pragma unroll
        for (int k = 0; k < 18; ++k) {
          __builtin_amdgcn_sched_group_barrier(0x8, 1, 0);
          if (it + 1 < 4 && k >= 2 && k - 2 < 2 * NF) __builtin_amdgcn_sched_group_barrier(0x100, 1, 0);
          if (PF && CV_PF1 > 0 && (k % (16 / (CV_PF1 / 4))) == 1 && k / (16 / (CV_PF1 / 4)) < CV_PF1 / 4)
            __builtin_amdgcn_sched_group_barrier(0x20, 1, 0);
        }
        __builtin_amdgcn_sched_barrier(0);
      });
    };
    if (LAYER == 0) {
      if (qs) body(std::integral_constant<int, 1>{}, std::true_type{}); else body(std::integral_constant<int, 0>{}, std::true_type{});
    } else {
      if (qs) body(std::integral_constant<int, 1>{}, std::false_type{}); else body(std::integral_constant<int, 0>{}, std::false_type{});
    }
    f32x4 keep[2];
    {
      char* wrp = smem + CV_RED + wave8 * 2048 + lane * 16;
#pragma unroll
      for (int e = 0; e < 2; ++e) {
        f32x4 give;
#pragma unroll
        for (int r = 0; r < 4; ++r) {
          const float lo = acc[e][r], hi = acc[2 + e][r];
          give[r] = half ? lo : hi;
          keep[e][r] = half ? hi : lo;
        }
        *(f32x4*)(wrp + e * 1024) = give;
      }
    }
    asm volatile("s_waitcnt lgkmcnt(0)" ::: "memory");
    __builtin_amdgcn_s_barrier();
    {
      const char* rd = smem + CV_RED + (wave8 ^ 1) * 2048 + lane * 16;
      const int toff = 16 * j16 + 4 * g;
#pragma unroll
      for (int e = 0; e < 2; ++e) {
        const int q = half ? (e ? (qs ? 1 : 0) : (qs ? 2 : 3)) : (e ? (qs ? 5 : 4) : (qs ? 6 : 7));
        const int t = 256 * q + toff;
        const f32x4 o = *(const f32x4*)(rd + e * 1024);
        const u32x4 xs = *(const u32x4*)(smem + CV_XS + ch * 8192 + t * 4);
        const f32x4 y = keep[e] + o;
        float xo[4];
#pragma unroll
        for (int r = 0; r < 4; ++r) {
          const unsigned w = xs[r];
          xo[r] = bf_lo(w) + rbf(rbf(y[r] * kscale) + bf_hi(w));
        }
        st8_sc1(xt + ((size_t)(t >> 7) * D_ + d0 + ch) * 128 + (t & 127), u32x2{pk_bf16(xo[0], xo[1]), pk_bf16(xo[2], xo[3])});
      }
    }
    wait_vm_lgkm0<0>();
    __builtin_amdgcn_s_barrier();
    if (tid < CREP) (void)__hip_atomic_fetch_add(cflags + (tid * 16 + (d0 >> 5)) * 32, 1u, __ATOMIC_RELAXED, __HIP_MEMORY_SCOPE_AGENT);
  }
  const int grow = lane >> 4, gslot = lane & 15;
  const unsigned short* xsrc = xt + ((size_t)mt * D_ + wave * 8 + grow) * 128 + ((gslot ^ (grow << 2)) * 8);
  const char* wsrc = w1t + (size_t)ft * 32 * 1024 + lane * 16;
  auto issue_w = [&](int c) {
    char* slot = smem + (c % G1_R) * 16384;
#pragma unroll
    for (int i = 0; i < 2; ++i)
      __builtin_amdgcn_global_load_lds((const unsigned*)(wsrc + (size_t)(c * 2 + i) * 1024),
                                       (LAS unsigned*)(slot + 8192 + (wave * 2 + i) * 1024), 16, 0, 0);
  };
  auto issue_x = [&](int c) {
    char* slot = smem + (c % G1_R) * 16384;
#pragma unroll
    for (int i = 0; i < 2; ++i)
      __builtin_amdgcn_global_load_lds((const unsigned*)(xsrc + (size_t)(c * 32 + i * 4) * 128),
                                       (LAS unsigned*)(slot + (wave * 8 + i * 4) * 256), 16, 0, 16  );
  };
  if (bi < 28 && wave8 >= 4) {
    if (wave8 < 6)
      __builtin_amdgcn_global_load_lds((const unsigned*)(ws + WS_W1S) + nt * 128 + wave * 64 + lane,
                                       (LAS unsigned*)(smem + 9 * 16384 + 256 + wave * 256), 4, 0, 0);
#pragma unroll
    for (int c = 0; c < W1PRE; ++c) issue_w(c);
  }
  __builtin_amdgcn_sched_barrier(0);
  if (LAYER == 0) static_for<CV_PF1, 28>([&](auto J) { pf_w2(J); });
  __builtin_amdgcn_sched_barrier(0);
  if (wave8 == 7) {
    const unsigned* cp = cflags + ((blockIdx.x % CREP) * 16 + (lane & 15)) * 32;
    for (int it = 0; it < (1 << 20); ++it) {
      const unsigned f = __hip_atomic_load(cp, __ATOMIC_RELAXED, __HIP_MEMORY_SCOPE_AGENT);
      if (__all(f >= 16u)) break;
      __builtin_amdgcn_s_sleep(POLL_SLEEP);
    }
  }
  __builtin_amdgcn_s_barrier();
  if (bi >= 28) {
    const int ht = bx * 4 + (bi - 28);
    const int cgp = wave8 * 2 + hh;
    const auto rsrc = __builtin_amdgcn_make_buffer_rsrc((void*)xt, 0, L_ * D_ * 2, 0x00020000);
    const int off0 = ((ht >> 1) * D_ * 128 + (ht & 1) * 64 + 2 * r32) * 2;
    const unsigned pv = __builtin_amdgcn_raw_buffer_load_b32(rsrc, off0, 0, 16  );
    unsigned v[32];
#pragma unroll
    for (int j = 0; j < 32; ++j) v[j] = __builtin_amdgcn_raw_buffer_load_b32(rsrc, off0 + (cgp * 32 + j) * 256, 0, 16  );
    static_for<0, 8>([&](auto J) { pf_xr(J); });
    const f32x2 piv = {bf_lo(pv), bf_hi(pv)};
    f32x2 s1 = {0.f, 0.f}, s2 = {0.f, 0.f};
#pragma unroll
    for (int j = 0; j < 32; ++j) {
      const f32x2 dd = f32x2{bf_lo(v[j]), bf_hi(v[j])} - piv;
      s1 += dd;
      s2 = __builtin_elementwise_fma(dd, dd, s2);
    }
    f32x4* red = (f32x4*)smem;
    red[cgp * 32 + r32] = f32x4{s1[0], s2[0], s1[1], s2[1]};
    asm volatile("s_waitcnt lgkmcnt(0)" ::: "memory");
    __builtin_amdgcn_s_barrier();
    if (tid < 64) {
      const f32x2* r2 = (const f32x2*)smem;
      float a = 0.f, q = 0.f;
#pragma unroll
      for (int w = 0; w < 16; ++w) { const f32x2 t = r2[w * 64 + tid]; a += t[0]; q += t[1]; }
      const unsigned pw = __shfl(pv, tid >> 1, 64);
      const float dm = a * (1.f / D_);
      const float var = fmaxf(q * (1.f / D_) - dm * dm, 0.f);
      const float mm = ((tid & 1) ? bf_hi(pw) : bf_lo(pw)) + dm;
      const float sdv = __builtin_amdgcn_sqrtf(rbf(rbf(var) + 1e-5f));
      __hip_atomic_store(sw + ht * 64 + tid, pk_bf16(mm, sdv), __ATOMIC_RELAXED, __HIP_MEMORY_SCOPE_AGENT);
    }
  } else if (wave8 >= 4) {
#pragma unroll
    for (int c = 0; c < G1_W0; ++c) { issue_x(c); if (c >= W1PRE) issue_w(c); }
    static_for<0, 16>([&](auto C) {
      constexpr int c = C;
      constexpr int extra = (c + G1_W0 > 16) ? 8 : 0;
      wait_vm<g1_young(c) + extra>();
      __builtin_amdgcn_s_barrier();
      if constexpr (c + G1_W0 < 16) { issue_x(c + G1_W0); if (c + G1_W0 >= W1PRE) issue_w(c + G1_W0); }
      if constexpr (c + G1_W0 == 16) {
        __builtin_amdgcn_sched_barrier(0);
        static_for<0, 8>([&](auto J) { pf_xr(J); });
        __builtin_amdgcn_sched_barrier(0);
      }
    });
  } else {
    const int blk = (lane >> 4) & 1, q4 = (lane & 15) >> 2, p4 = lane & 3;
    const unsigned sbase = (unsigned)(uintptr_t)(LAS char*)smem;
    unsigned* lcnt = (unsigned*)(smem + 9 * 16384);
    unsigned tb[3][4], ta[3];
#pragma unroll
    for (int ct = 0; ct < 4; ++ct) {
      tb[0][ct] = sbase + (unsigned)((8 * hh + q4) * 256 + (((4 * ct + 2 * blk + (p4 >> 1)) ^ (q4 << 2)) * 16) + 8 * (p4 & 1));
      tb[1][ct] = tb[0][ct] + 65536u; tb[2][ct] = tb[0][ct] + 131072u;
    }
    ta[0] = sbase + 8192 + wave * 2048 + lane * 16; ta[1] = ta[0] + 65536u; ta[2] = ta[0] + 131072u;
    f32x16 acc[4];
#pragma unroll
    for (int ct = 0; ct < 4; ++ct)
#pragma unroll
      for (int i = 0; i < 16; ++i) acc[ct][i] = 0.f;
    constexpr int LA = 6;
    u32x2 bb[8][2];
    u32x4 af[3];
    unsigned swv[4];
    f32x4 ws4[4];
    auto issue_pair = [&](auto NN) {
      constexpr int n = NN, ks = n >> 2, ct = n & 3;
      constexpr int so = ((ks >> 1) % G1_R) * 16384, seg = so >> 16, o = (so & 65535) + (ks & 1) * 4096;
      if constexpr (ct == 0) lds_read16<(so & 65535) + (ks & 1) * 1024>(af[ks % 3], ta[seg]);
      tr_read<o>(bb[n & 7][0], tb[seg][ct]);
      tr_read<o + 1024>(bb[n & 7][1], tb[seg][ct]);
    };
    __builtin_amdgcn_s_barrier();
    static_for<0, LA>([&](auto NN) { issue_pair(NN); });
    static_for<0, 128>([&](auto NN) {
      constexpr int n = NN, ks = n >> 2, ct = n & 3;
      constexpr int last = (n + LA - 1 < 127) ? (n + LA - 1) : 127;
      constexpr int younger = 2 * (last - n) + ((last >> 2) - (n >> 2));
      if constexpr (ct == 0) wait_lgkm3<younger>(bb[n & 7][0], bb[n & 7][1], af[ks % 3]);
      else wait_lgkm2<younger>(bb[n & 7][0], bb[n & 7][1]);
      const u32x4 bu = {bb[n & 7][0][0], bb[n & 7][0][1], bb[n & 7][1][0], bb[n & 7][1][1]};
      acc[ct] = __builtin_amdgcn_mfma_f32_32x32x16_bf16(__builtin_bit_cast(bf16x8, af[ks % 3]), __builtin_bit_cast(bf16x8, bu), acc[ct], 0, 0, 0);
      __builtin_amdgcn_sched_barrier(0);
      if constexpr (n + LA < 128) {
        if constexpr ((n + LA) % 8 == 0) __builtin_amdgcn_s_barrier();
        issue_pair(std::integral_constant<int, n + LA>{});
      }
      if constexpr (n >= 104 && n < 108)
        swv[n - 104] = __hip_atomic_load(sw + mt * 128 + (n - 104) * 32 + r32, __ATOMIC_RELAXED, __HIP_MEMORY_SCOPE_AGENT);
      __builtin_amdgcn_sched_barrier(0);
    });
    for (int it = 0; it < (1 << 20); ++it) {
      if (__all(swv[0] != 0u && swv[1] != 0u && swv[2] != 0u && swv[3] != 0u)) break;
      __builtin_amdgcn_s_sleep(1);
#pragma unroll
      for (int ct = 0; ct < 4; ++ct)
        swv[ct] = __hip_atomic_load(sw + mt * 128 + ct * 32 + r32, __ATOMIC_RELAXED, __HIP_MEMORY_SCOPE_AGENT);
    }
    asm volatile("" : "+v"(swv[0]), "+v"(swv[1]), "+v"(swv[2]), "+v"(swv[3]));
#pragma unroll
    for (int q = 0; q < 4; ++q) ws4[q] = *(const f32x4*)(smem + 9 * 16384 + 256 + (wave * 32 + 8 * q + 4 * hh) * 4);
#pragma unroll
    for (int ct = 0; ct < 4; ++ct) {
      const float rs = __builtin_amdgcn_rcpf(bf_hi(swv[ct]));
      const float cm = -bf_lo(swv[ct]) * rs;
      char* dst = hbuf + ((size_t)((mt * 4 + ct) * 112 + ft * 2) * 64 + lane) * 16;
      const f32x2 c2 = {cm, cm}, r2 = {rs, rs};
#pragma unroll
      for (int s = 0; s < 2; ++s) {
        u32x4 o;
#pragma unroll
        for (int j = 0; j < 4; ++j) {
          const int reg = 8 * s + 2 * j;
          const f32x2 wsum = {ws4[reg >> 2][reg & 3], ws4[reg >> 2][(reg & 3) + 1]};
          const f32x2 a2 = {acc[ct][reg], acc[ct][reg + 1]};
          const f32x2 v2 = __builtin_elementwise_fma(a2, r2, c2 * wsum);
          const s16x2 pv = __builtin_bit_cast(s16x2, __builtin_convertvector(v2, bf16x2));
          const s16x2 z = {0, 0};
          o[j] = __builtin_bit_cast(unsigned, __builtin_elementwise_max(pv, z));
        }
        st16_sc1(dst + s * 1024, o);
      }
    }
    wait_vm<0>();
    if (lane == 0) {
      const unsigned old = __hip_atomic_fetch_add(lcnt, 1u, __ATOMIC_RELAXED, __HIP_MEMORY_SCOPE_WORKGROUP);
      if (old == 3u) __hip_atomic_store(flags + (mt * 16 + nt) * FLS, 1u, __ATOMIC_RELAXED, __HIP_MEMORY_SCOPE_AGENT);
    }
    static_for<0, 8>([&](auto J) { pf_xr(J); });
  }
  pin7(wr); pin7(wr + 7); pin7(wr + 14); pin7(wr + 21);
  {
    unsigned x0_ = xr[0], x1_ = xr[1], x2_ = xr[2], x3_ = xr[3], x4_ = xr[4], x5_ = xr[5], x6_ = xr[6], x7_ = xr[7];
    asm volatile("" : "+v"(x0_), "+v"(x1_), "+v"(x2_), "+v"(x3_), "+v"(x4_), "+v"(x5_), "+v"(x6_), "+v"(x7_));
    xr[0] = x0_; xr[1] = x1_; xr[2] = x2_; xr[3] = x3_; xr[4] = x4_; xr[5] = x5_; xr[6] = x6_; xr[7] = x7_;
  }
  __builtin_amdgcn_s_barrier();
  if (wave8 == 7) {
    const unsigned* fp = flags + ((mt2 >> 1) * 16 + (lane < 14 ? lane : 0)) * FLS;
    for (int it = 0; it < (1 << 20); ++it) {
      const unsigned f = __hip_atomic_load(fp, __ATOMIC_RELAXED, __HIP_MEMORY_SCOPE_AGENT);
      if (__all(f != 0u)) break;
      __builtin_amdgcn_s_sleep(POLL_SLEEP);
    }
  }
  asm volatile("s_waitcnt lgkmcnt(0)" ::: "memory");
  __builtin_amdgcn_s_barrier();
  const char* hsrc = hbuf + (size_t)(mt2 * 2) * 112 * 1024 + wave8 * 1024 + lane * 16;
  char* dstb = smem + wave8 * 2048;
  auto issue2 = [&](int c) {
#pragma unroll
    for (int mi = 0; mi < 2; ++mi)
      __builtin_amdgcn_global_load_lds((const unsigned*)(hsrc + (size_t)mi * 112 * 1024 + c * 8192),
                                       (LAS unsigned*)(dstb + (c % G2_R) * 16384 + mi * 1024), 16, 0, 16  );
  };
  f32x16 acc[2];
#pragma unroll
  for (int i = 0; i < 16; ++i) { acc[0][i] = 0.f; acc[1][i] = 0.f; }
  const char* rdh = smem + kq * 4096 + lane * 16;
  bf16x8 hf[2][4];
#pragma unroll
  for (int c = 0; c < g2_iss(0); ++c) issue2(c);
  static_for<0, 15>([&](auto C) {
    constexpr int c = C;
    auto mma = [&](auto JJ) {
      constexpr int j = JJ;
      if constexpr (c >= 1) {
        constexpr int pc = c - 1;
#pragma unroll
        for (int mi = 0; mi < 2; ++mi)
          acc[mi] = __builtin_amdgcn_mfma_f32_32x32x16_bf16(__builtin_bit_cast(bf16x8, wr[2 * pc + j]), hf[pc & 1][j * 2 + mi], acc[mi], 0, 0, 0);
      }
    };
    mma(std::integral_constant<int, 0>{});
    __builtin_amdgcn_sched_barrier(0);
    if constexpr (c < 14) {
      wait_vm_lgkm0<2 * (g2_iss(c) - (c + 1))>();
      __builtin_amdgcn_s_barrier();
#pragma unroll
      for (int e = 0; e < 4; ++e) hf[c & 1][e] = *(const bf16x8*)(rdh + (c % G2_R) * 16384 + e * 1024);
    }
    __builtin_amdgcn_sched_barrier(0);
    mma(std::integral_constant<int, 1>{});
    __builtin_amdgcn_sched_barrier(0);
    if constexpr (c < 14) {
#pragma unroll
      for (int cn = g2_iss(c); cn < g2_iss(c + 1); ++cn) issue2(cn);
    }
    __builtin_amdgcn_sched_barrier(0);
  });
  char* red = smem + 5 * 16384;
  {
    char* dst = red + (ni * 16 + kq) * 2048 + lane * 16;
#pragma unroll
    for (int pc = 0; pc < 4; ++pc) {
      f32x4 g0, g1;
#pragma unroll
      for (int i = 0; i < 4; ++i) { g0[i] = acc[pc >> 1][8 * (pc & 1) + i]; g1[i] = acc[pc >> 1][8 * (pc & 1) + 4 + i]; }
      *(f32x4*)(dst + pc * 8192) = g0;
      *(f32x4*)(dst + pc * 8192 + 1024) = g1;
    }
  }
  asm volatile("s_waitcnt lgkmcnt(0)" ::: "memory");
  __builtin_amdgcn_s_barrier();
  float mine[8];
  {
    const char* src = red + (ni * 16 + kq * 4) * 2048 + lane * 16;
    f32x4 o[8];
#pragma unroll
    for (int sl = 0; sl < 8; ++sl) o[sl] = *(const f32x4*)(src + sl * 1024);
    asm volatile("" : "+v"(o[0]), "+v"(o[1]), "+v"(o[2]), "+v"(o[3]), "+v"(o[4]), "+v"(o[5]), "+v"(o[6]), "+v"(o[7]));
    const f32x4 t0 = (o[0] + o[2]) + (o[4] + o[6]), t1 = (o[1] + o[3]) + (o[5] + o[7]);
#pragma unroll
    for (int i = 0; i < 4; ++i) { mine[i] = t0[i]; mine[4 + i] = t1[i]; }
  }
  float xo[8];
  float s1 = 0.f, s2 = 0.f;
#pragma unroll
  for (int i = 0; i < 8; ++i) {
    const float v = rbf(__uint_as_float(((unsigned)xr[i]) << 16) + rbf(mine[i]));
    xo[i] = v;
    s1 += v; s2 += v * v;
  }
  if (LAYER == 1) {
#pragma unroll
    for (int q = 0; q < 2; ++q)
      *(u32x2*)(p.out + (size_t)tok * D_ + dbase + 8 * q) =
          u32x2{pk_bf16(xo[4 * q], xo[4 * q + 1]), pk_bf16(xo[4 * q + 2], xo[4 * q + 3])};
  } else {
    unsigned short* xout = (unsigned short*)(ws + WS_XT2);
#pragma unroll
    for (int i = 0; i < 8; ++i)
      __hip_atomic_store(xout + (size_t)(dbase + (i & 3) + 8 * (i >> 2)) * L_ + tok,
                         (unsigned short)(__float_as_uint(xo[i]) >> 16), __ATOMIC_RELAXED, __HIP_MEMORY_SCOPE_AGENT);
    s1 += __shfl_xor(s1, 32, 64);
    s2 += __shfl_xor(s2, 32, 64);
    float* sred = (float*)smem;
    sred[wave8 * 64 + lane] = hh ? s2 : s1;
    asm volatile("s_waitcnt lgkmcnt(0)" ::: "memory");
    __builtin_amdgcn_s_barrier();
    if (wave8 < 2) {
      const int w0 = wave8 * 4;
      const float t = sred[w0 * 64 + lane] + sred[(w0 + 1) * 64 + lane] + sred[(w0 + 2) * 64 + lane] + sred[(w0 + 3) * 64 + lane];
      (void)__hip_atomic_fetch_add((float*)(ws + WS_ST1) + 2 * (mt2 * 64 + wave8 * 32 + r32) + hh, t, __ATOMIC_RELAXED, __HIP_MEMORY_SCOPE_AGENT);
    }
    wait_vm_lgkm0<0>();
    __builtin_amdgcn_s_barrier();
    if (tid < CREP) (void)__hip_atomic_fetch_add(eflags + (tid * 16 + (blockIdx.x & 15)) * 32, 1u, __ATOMIC_RELAXED, __HIP_MEMORY_SCOPE_AGENT);
  }
  }
}

extern "C" void kernel_launch(void* const* d_in, const int* in_sizes, int n_in,
                              void* d_out, int out_size, void* d_ws, size_t ws_size,
                              hipStream_t stream) {
  const unsigned short* x0 = (const unsigned short*)d_in[0];
  const unsigned short* logA = (const unsigned short*)d_in[1];
  const unsigned short* Aim = (const unsigned short*)d_in[2];
  const unsigned short* Cre = (const unsigned short*)d_in[3];
  const unsigned short* Cim = (const unsigned short*)d_in[4];
  const unsigned short* logdt = (const unsigned short*)d_in[5];
  const unsigned short* dskip = (const unsigned short*)d_in[6];
  const unsigned short* w1 = (const unsigned short*)d_in[8];
  const unsigned short* w2 = (const unsigned short*)d_in[9];
  char* ws = (char*)d_ws;
  (void)hipFuncSetAttribute((const void*)s4d_layer_kernel, hipFuncAttributeMaxDynamicSharedMemorySize, LAYER_LDS);
  prep_kernel<<<PREP_BLOCKS, 256, 0, stream>>>(x0, logA, Aim, Cre, Cim, logdt, w1, w2, ws);
  LayerArgs a0 = {dskip, (unsigned short*)d_out, ws};
  s4d_layer_kernel<<<256, 512, LAYER_LDS, stream>>>(a0);
}
```
